```python
import torch

def _softmax(x, axis=-1):
    return torch.softmax(x, dim=axis)
def _gelu(x):
    return torch.nn.functional.gelu(x, approximate="tanh")
def _silu(x):
    return torch.nn.functional.silu(x)
def _sigmoid(x):
    return torch.sigmoid(x)
def _layernorm(x, axis=-1, eps=1e-5):
    m = x.mean(dim=axis, keepdim=True)
    v = x.var(dim=axis, keepdim=True, unbiased=False)
    return (x - m) / torch.sqrt(v + eps)
def _scatter_add(tgt, idx, src, axis=0):
    return tgt.clone().index_add_(axis, idx, src)
def _segment_sum(src, seg, n):
    out = torch.zeros((n,) + src.shape[1:], dtype=src.dtype)
    return out.index_add_(0, seg, src)
def _pad(x, pw):
    flat = tuple(v for pair in reversed(pw) for v in pair)
    return torch.nn.functional.pad(x, flat)

kernel_name = 'standalone__memory_aug_memorizing_tr__prompt'

def setup_inputs():

    _w = (lambda a: (lambda t: t.to(torch.bfloat16) if t.is_floating_point() else t)(torch.as_tensor(a)))
    B, H, S, D = 1, 32, 2048, 192
    M, K = 2048, 2
    return {
        "q": _w((lambda *s: torch.randn(*s, dtype=torch.bfloat16) * 0.8)(B, H, S, D)),
        "k": _w((lambda *s: torch.randn(*s, dtype=torch.bfloat16) * 0.8)(B, H, S, D)),
        "v": _w((lambda *s: torch.randn(*s, dtype=torch.bfloat16) * 0.05)(B, H, S, D)),
        "mem_k": _w((lambda *s: torch.randn(*s, dtype=torch.bfloat16) * 0.8)(B, H, M, D)),
        "mem_v": _w((lambda *s: torch.randn(*s, dtype=torch.bfloat16) * 0.05)(B, H, M, D)),
        "idx": _w(torch.randint(0, M, (B, H, S, K)).to(torch.int64)),
        "gate_bias": _w((lambda *s: torch.randn(*s, dtype=torch.bfloat16) * 0.2)(H)),
    }

def reference(q, k, v, mem_k, mem_v, idx, gate_bias):
    B, H, S, D = q.shape
    K = idx.shape[-1]
    scale = D ** -0.5
    sl = torch.einsum("bhqd,bhkd->bhqk", q, k) * scale
    cm = torch.tril(torch.ones((S, S), dtype=q.dtype))
    sl = torch.where(cm[None, None] > 0, sl, -1e9)
    pl = _softmax(sl, axis=-1)
    ol = torch.einsum("bhqk,bhkd->bhqd", pl, v)
    idx_e = (lambda x, n, axis: torch.repeat_interleave(x, n, dim=axis))(idx.reshape(B, H, S * K)[..., None], D, axis=-1)
    mk = (lambda x, i, axis: torch.take_along_dim(x, i, dim=axis))(mem_k, idx_e, axis=2).reshape(B, H, S, K, D)
    mv = (lambda x, i, axis: torch.take_along_dim(x, i, dim=axis))(mem_v, idx_e, axis=2).reshape(B, H, S, K, D)
    sm = torch.einsum("bhqd,bhqkd->bhqk", q, mk) * scale
    pm = _softmax(sm, axis=-1)
    om = torch.einsum("bhqk,bhqkd->bhqd", pm, mv)
    g = _sigmoid(gate_bias)[None, :, None, None]
    return g * om + (1 - g) * ol
```

```cpp
#include <hip/hip_runtime.h>
#include <hip/hip_bf16.h>
typedef unsigned short u16;
static __device__ __forceinline__ float bf2f(u16 x){ return __uint_as_float(((unsigned)x)<<16); }
constexpr int H=32,S=2048,D=192,M=2048;
__global__ __launch_bounds__(256) void naive_kernel(const u16* q,const u16* k,const u16* v,const u16* mk,const u16* mv,const int* idx,const u16* gb,__hip_bfloat16* out){
  __shared__ float sc[4][S];
  __shared__ float qs[4][D];
  const int w=threadIdx.x>>6, lane=threadIdx.x&63;
  const int row=blockIdx.x*4+w; const int h=row/S, i=row%S;
  const float scale=0.07216878364870322f;
  for(int d=lane;d<D;d+=64) qs[w][d]=bf2f(q[(size_t)row*D+d]);
  __syncthreads();
  float mx=-1e30f;
  for(int j=lane;j<=i;j+=64){ const u16* kr=k+((size_t)h*S+j)*D; float s=0; for(int d=0;d<D;d++) s+=qs[w][d]*bf2f(kr[d]); s*=scale; sc[w][j]=s; mx=fmaxf(mx,s);}
  for(int o=32;o;o>>=1) mx=fmaxf(mx,__shfl_xor(mx,o));
  float l=0;
  for(int j=lane;j<=i;j+=64){ float p=__expf(sc[w][j]-mx); sc[w][j]=p; l+=p; }
  for(int o=32;o;o>>=1) l+=__shfl_xor(l,o);
  __syncthreads();
  float acc[3]={0,0,0};
  for(int j=0;j<=i;j++){ const u16* vr=v+((size_t)h*S+j)*D; float p=sc[w][j]; for(int c=0;c<3;c++) acc[c]+=p*bf2f(vr[lane+64*c]); }
  int i0=idx[(size_t)row*2], i1=idx[(size_t)row*2+1];
  const u16* k0=mk+((size_t)h*M+i0)*D; const u16* k1=mk+((size_t)h*M+i1)*D;
  const u16* v0=mv+((size_t)h*M+i0)*D; const u16* v1=mv+((size_t)h*M+i1)*D;
  float s0=0,s1=0; for(int c=0;c<3;c++){ int d=lane+64*c; s0+=qs[w][d]*bf2f(k0[d]); s1+=qs[w][d]*bf2f(k1[d]); }
  for(int o=32;o;o>>=1){ s0+=__shfl_xor(s0,o); s1+=__shfl_xor(s1,o);}
  s0*=scale; s1*=scale; float mm=fmaxf(s0,s1); float p0=__expf(s0-mm),p1=__expf(s1-mm); float il=1.f/(p0+p1); p0*=il;p1*=il;
  float g=1.f/(1.f+__expf(-bf2f(gb[h])));
  for(int c=0;c<3;c++){ int d=lane+64*c; float om=p0*bf2f(v0[d])+p1*bf2f(v1[d]); out[(size_t)row*D+d]=__float2bfloat16(g*om+(1.f-g)*acc[c]/l); }
}
extern "C" void kernel_launch(void* const* d_in,const int* in_sizes,int n_in,void* d_out,int out_size,void* d_ws,size_t ws_size,hipStream_t stream){
  naive_kernel<<<H*S/4,256,0,stream>>>((const u16*)d_in[0],(const u16*)d_in[1],(const u16*)d_in[2],(const u16*)d_in[3],(const u16*)d_in[4],(const int*)d_in[5],(const u16*)d_in[6],(__hip_bfloat16*)d_out);
}
```

```cpp
#include <hip/hip_runtime.h>
#include <hip/hip_bf16.h>
typedef unsigned short u16;
typedef short bf16x8 __attribute__((ext_vector_type(8)));
typedef short s16x4 __attribute__((ext_vector_type(4)));
typedef float f32x16 __attribute__((ext_vector_type(16)));
typedef unsigned u32x4 __attribute__((ext_vector_type(4)));
typedef unsigned u32x2 __attribute__((ext_vector_type(2)));
constexpr int H=32,S=2048,D=192,M=2048;
constexpr int RS=400;
static __device__ __forceinline__ float bflo(unsigned x){ return __uint_as_float(x<<16); }
static __device__ __forceinline__ float bfhi(unsigned x){ return __uint_as_float(x&0xffff0000u); }
static __device__ __forceinline__ unsigned cvtpk(float lo,float hi){ unsigned r; asm("v_cvt_pk_bf16_f32 %0, %1, %2":"=v"(r):"v"(lo),"v"(hi)); return r; }
typedef __attribute__((address_space(3))) s16x4* lds4;
__global__ __launch_bounds__(256,2) void attn_kernel(const u16* q,const u16* k,const u16* v,const u16* mk,const u16* mv,const int* idx,const u16* gb,u16* out){
  __shared__ __attribute__((aligned(16))) char lds[2*2*32*RS];
  const int tid=threadIdx.x, w=tid>>6, lane=tid&63, r32=lane&31, hi=lane>>5;
  const int b=blockIdx.x; const int h=(b&7)*4+((b>>3)&3); const int jb=15-(b>>5);
  const float C2=0.07216878364870322f*1.4426950408889634f;
  const int row=jb*128+w*32+r32;
  const u16* qh=q+((size_t)h*S+row)*D;
  bf16x8 qf[12];
  #pragma unroll
  for(int i=0;i<12;i++) qf[i]=*(const bf16x8*)(qh+16*i+8*hi);
  f32x16 o[6];
  #pragma unroll
  for(int i=0;i<6;i++) o[i]=f32x16{};
  float m=-1e30f,l=0;
  const int nt=(jb+1)*4;
  const u16* kh=k+(size_t)h*S*D; const u16* vh=v+(size_t)h*S*D;
  u32x4 st[6];
  auto gload=[&](int t){
    #pragma unroll
    for(int i=0;i<3;i++){ int c=tid+256*i; st[i]=*(const u32x4*)(kh+(size_t)t*32*D+c*8); st[3+i]=*(const u32x4*)(vh+(size_t)t*32*D+c*8);} };
  auto swrite=[&](int buf){
    #pragma unroll
    for(int i=0;i<3;i++){ int c=tid+256*i; int rr=c/24, cc=c%24; *(u32x4*)(lds+buf*64*RS+rr*RS+cc*16)=st[i]; *(u32x4*)(lds+buf*64*RS+32*RS+rr*RS+cc*16)=st[3+i]; } };
  gload(0); swrite(0); __syncthreads();
  for(int t=0;t<nt;t++){
    const int buf=t&1;
    if(t+1<nt) gload(t+1);
    const char* Kl=lds+buf*64*RS; const char* Vl=Kl+32*RS;
    if(t*32<=jb*128+w*32+31){
      f32x16 s=f32x16{};
      #pragma unroll
      for(int i=0;i<12;i++){ bf16x8 kf=*(const bf16x8*)(Kl+r32*RS+(2*i+hi)*16); s=__builtin_amdgcn_mfma_f32_32x32x16_bf16(kf,qf[i],s,0,0,0);}
      if(t*32+31>jb*128+w*32){
        #pragma unroll
        for(int r=0;r<16;r++){ int key=t*32+(r&3)+8*(r>>2)+4*hi; if(key>row) s[r]=-__builtin_inff(); }
      }
      float mx=s[0];
      #pragma unroll
      for(int r=1;r<16;r++) mx=fmaxf(mx,s[r]);
      mx=fmaxf(mx,__shfl_xor(mx,32));
      float mn=fmaxf(m,mx); float al=__builtin_amdgcn_exp2f((m-mn)*C2); m=mn;
      float ps=0;
      #pragma unroll
      for(int r=0;r<16;r++){ s[r]=__builtin_amdgcn_exp2f(s[r]*C2-mn*C2); ps+=s[r]; }
      l=l*al+ps;
      if(__any(al<1.f)){
        #pragma unroll
        for(int i=0;i<6;i++) o[i]*=al; }
      #pragma unroll
      for(int ks=0;ks<2;ks++){
        u32x4 pw={cvtpk(s[8*ks],s[8*ks+1]),cvtpk(s[8*ks+2],s[8*ks+3]),cvtpk(s[8*ks+4],s[8*ks+5]),cvtpk(s[8*ks+6],s[8*ks+7])};
        bf16x8 pf=__builtin_bit_cast(bf16x8,pw);
        const char* vb=Vl+(16*ks+4*hi+((lane&15)>>2))*RS+((lane>>4)&1)*32+(lane&3)*8;
        #pragma unroll
        for(int db=0;db<6;db++){
          s16x4 a=__builtin_amdgcn_ds_read_tr16_b64_v4i16((lds4)(vb+db*64));
          s16x4 c=__builtin_amdgcn_ds_read_tr16_b64_v4i16((lds4)(vb+db*64+8*RS));
          bf16x8 vf={a[0],a[1],a[2],a[3],c[0],c[1],c[2],c[3]};
          o[db]=__builtin_amdgcn_mfma_f32_32x32x16_bf16(vf,pf,o[db],0,0,0);
        }
      }
    }
    if(t+1<nt) swrite(buf^1);
    __syncthreads();
  }
  l+=__shfl_xor(l,32);
  const size_t grow=(size_t)h*S+row;
  const int i0=idx[grow*2], i1=idx[grow*2+1];
  const u16* k0=mk+((size_t)h*M+i0)*D; const u16* k1=mk+((size_t)h*M+i1)*D;
  float s0=0,s1=0;
  #pragma unroll
  for(int i=0;i<12;i++){ u32x4 a=*(const u32x4*)(k0+16*i+8*hi), c=*(const u32x4*)(k1+16*i+8*hi); u32x4 qq=__builtin_bit_cast(u32x4,qf[i]);
    #pragma unroll
    for(int e=0;e<4;e++){ s0+=bflo(qq[e])*bflo(a[e])+bfhi(qq[e])*bfhi(a[e]); s1+=bflo(qq[e])*bflo(c[e])+bfhi(qq[e])*bfhi(c[e]); } }
  s0+=__shfl_xor(s0,32); s1+=__shfl_xor(s1,32);
  float mm=fmaxf(s0,s1); float p0=__builtin_amdgcn_exp2f((s0-mm)*C2),p1=__builtin_amdgcn_exp2f((s1-mm)*C2);
  float g=1.f/(1.f+__expf(-bflo(gb[h]))); float il=g/(p0+p1); p0*=il; p1*=il; float ol=(1.f-g)/l;
  const u16* v0=mv+((size_t)h*M+i0)*D; const u16* v1=mv+((size_t)h*M+i1)*D;
  #pragma unroll
  for(int db=0;db<6;db++)
    #pragma unroll
    for(int gq=0;gq<4;gq++){ int d=32*db+8*gq+4*hi; u32x2 a=*(const u32x2*)(v0+d), c=*(const u32x2*)(v1+d);
      float r0=o[db][4*gq]*ol+p0*bflo(a[0])+p1*bflo(c[0]); float r1=o[db][4*gq+1]*ol+p0*bfhi(a[0])+p1*bfhi(c[0]);
      float r2=o[db][4*gq+2]*ol+p0*bflo(a[1])+p1*bflo(c[1]); float r3=o[db][4*gq+3]*ol+p0*bfhi(a[1])+p1*bfhi(c[1]);
      u32x2 wv={cvtpk(r0,r1),cvtpk(r2,r3)}; *(u32x2*)(out+grow*D+d)=wv; }
}
extern "C" void kernel_launch(void* const* d_in,const int* in_sizes,int n_in,void* d_out,int out_size,void* d_ws,size_t ws_size,hipStream_t stream){
  attn_kernel<<<512,256,0,stream>>>((const u16*)d_in[0],(const u16*)d_in[1],(const u16*)d_in[2],(const u16*)d_in[3],(const u16*)d_in[4],(const int*)d_in[5],(const u16*)d_in[6],(u16*)d_out);
}
```

```cpp
#include <hip/hip_runtime.h>
#include <hip/hip_bf16.h>
typedef unsigned short u16;
typedef short bf16x8 __attribute__((ext_vector_type(8)));
typedef short s16x4 __attribute__((ext_vector_type(4)));
typedef float f32x16 __attribute__((ext_vector_type(16)));
typedef unsigned u32x4 __attribute__((ext_vector_type(4)));
typedef unsigned u32x2 __attribute__((ext_vector_type(2)));
constexpr int H=32,S=2048,D=192,M=2048;
constexpr int RS=400;
static __device__ __forceinline__ float bflo(unsigned x){ return __uint_as_float(x<<16); }
static __device__ __forceinline__ float bfhi(unsigned x){ return __uint_as_float(x&0xffff0000u); }
static __device__ __forceinline__ unsigned cvtpk(float lo,float hi){ unsigned r; asm("v_cvt_pk_bf16_f32 %0, %1, %2":"=v"(r):"v"(lo),"v"(hi)); return r; }
typedef __attribute__((address_space(3))) s16x4* lds4;
__global__ __launch_bounds__(256,2) void attn_kernel(const u16* q,const u16* k,const u16* v,const u16* mk,const u16* mv,const int* idx,const u16* gb,u16* out){
  __shared__ __attribute__((aligned(16))) char lds[2*2*32*RS];
  const int tid=threadIdx.x, w=tid>>6, lane=tid&63, r32=lane&31, hi=lane>>5;
  const int b=blockIdx.x; const int h=(b&7)*4+((b>>3)&3); const int jb=15-(b>>5);
  const float C2=0.07216878364870322f*1.4426950408889634f;
  const int row=jb*128+w*32+r32;
  const u16* qh=q+((size_t)h*S+row)*D;
  bf16x8 qf[12];
  #pragma unroll
  for(int i=0;i<12;i++) qf[i]=*(const bf16x8*)(qh+16*i+8*hi);
  f32x16 o[6];
  #pragma unroll
  for(int i=0;i<6;i++) o[i]=f32x16{};
  float m=-1e30f,l=0;
  const int nt=(jb+1)*4;
  const u16* kh=k+(size_t)h*S*D; const u16* vh=v+(size_t)h*S*D;
  u32x4 st[6];
  auto gload=[&](int t){
    #pragma unroll
    for(int i=0;i<3;i++){ int c=tid+256*i; st[i]=*(const u32x4*)(kh+(size_t)t*32*D+c*8); st[3+i]=*(const u32x4*)(vh+(size_t)t*32*D+c*8);} };
  auto swrite=[&](int buf){
    #pragma unroll
    for(int i=0;i<3;i++){ int c=tid+256*i; int rr=c/24, cc=c%24; *(u32x4*)(lds+buf*64*RS+rr*RS+cc*16)=st[i]; *(u32x4*)(lds+buf*64*RS+32*RS+rr*RS+cc*16)=st[3+i]; } };
  gload(0); swrite(0); __syncthreads();
  for(int t=0;t<nt;t++){
    const int buf=t&1;
    if(t+1<nt) gload(t+1);
    const char* Kl=lds+buf*64*RS; const char* Vl=Kl+32*RS;
    if(t*32<=jb*128+w*32+31){
      f32x16 s=f32x16{};
      #pragma unroll
      for(int i=0;i<12;i++){ bf16x8 kf=*(const bf16x8*)(Kl+r32*RS+(2*i+hi)*16); s=__builtin_amdgcn_mfma_f32_32x32x16_bf16(kf,qf[i],s,0,0,0);}
      if(t*32+31>jb*128+w*32){
        #pragma unroll
        for(int r=0;r<16;r++){ int key=t*32+(r&3)+8*(r>>2)+4*hi; if(key>row) s[r]=-__builtin_inff(); }
      }
      float mx=s[0];
      #pragma unroll
      for(int r=1;r<16;r++) mx=fmaxf(mx,s[r]);
      mx=fmaxf(mx,__shfl_xor(mx,32));
      float mn=fmaxf(m,mx); float al=__builtin_amdgcn_exp2f((m-mn)*C2); m=mn;
      float ps=0;
      #pragma unroll
      for(int r=0;r<16;r++){ s[r]=__builtin_amdgcn_exp2f(s[r]*C2-mn*C2); ps+=s[r]; }
      l=l*al+ps;
      if(__any(al<1.f)){
        #pragma unroll
        for(int i=0;i<6;i++) o[i]*=al; }
      #pragma unroll
      for(int ks=0;ks<2;ks++){
        u32x4 pw={cvtpk(s[8*ks],s[8*ks+1]),cvtpk(s[8*ks+2],s[8*ks+3]),cvtpk(s[8*ks+4],s[8*ks+5]),cvtpk(s[8*ks+6],s[8*ks+7])};
        bf16x8 pf=__builtin_bit_cast(bf16x8,pw);
        const char* vb=Vl+(16*ks+4*hi+((lane&15)>>2))*RS+((lane>>4)&1)*32+(lane&3)*8;
        #pragma unroll
        for(int db=0;db<6;db++){
          s16x4 a=__builtin_amdgcn_ds_read_tr16_b64_v4i16((lds4)(vb+db*64));
          s16x4 c=__builtin_amdgcn_ds_read_tr16_b64_v4i16((lds4)(vb+db*64+8*RS));
          bf16x8 vf={a[0],a[1],a[2],a[3],c[0],c[1],c[2],c[3]};
          o[db]=__builtin_amdgcn_mfma_f32_32x32x16_bf16(vf,pf,o[db],0,0,0);
        }
      }
    }
    if(t+1<nt) swrite(buf^1);
    __syncthreads();
  }
  l+=__shfl_xor(l,32);
  const size_t grow=(size_t)h*S+row;
  const int i0=idx[grow*2], i1=idx[grow*2+1];
  const u16* k0=mk+((size_t)h*M+i0)*D; const u16* k1=mk+((size_t)h*M+i1)*D;
  float s0=0,s1=0;
  #pragma unroll
  for(int i=0;i<12;i++){ u32x4 a=*(const u32x4*)(k0+16*i+8*hi), c=*(const u32x4*)(k1+16*i+8*hi); u32x4 qq=__builtin_bit_cast(u32x4,qf[i]);
    #pragma unroll
    for(int e=0;e<4;e++){ s0+=bflo(qq[e])*bflo(a[e])+bfhi(qq[e])*bfhi(a[e]); s1+=bflo(qq[e])*bflo(c[e])+bfhi(qq[e])*bfhi(c[e]); } }
  s0+=__shfl_xor(s0,32); s1+=__shfl_xor(s1,32);
  float mm=fmaxf(s0,s1); float p0=__builtin_amdgcn_exp2f((s0-mm)*C2),p1=__builtin_amdgcn_exp2f((s1-mm)*C2);
  float g=1.f/(1.f+__expf(-bflo(gb[h]))); float il=g/(p0+p1); p0*=il; p1*=il; float ol=(1.f-g)/l;
  const u16* v0=mv+((size_t)h*M+i0)*D; const u16* v1=mv+((size_t)h*M+i1)*D;
  #pragma unroll
  for(int db=0;db<6;db++)
    #pragma unroll
    for(int gq=0;gq<4;gq++){ int d=32*db+8*gq+4*hi; u32x2 a=*(const u32x2*)(v0+d), c=*(const u32x2*)(v1+d);
      float r0=o[db][4*gq]*ol+p0*bflo(a[0])+p1*bflo(c[0]); float r1=o[db][4*gq+1]*ol+p0*bfhi(a[0])+p1*bfhi(c[0]);
      float r2=o[db][4*gq+2]*ol+p0*bflo(a[1])+p1*bflo(c[1]); float r3=o[db][4*gq+3]*ol+p0*bfhi(a[1])+p1*bfhi(c[1]);
      u32x2 wv={cvtpk(r0,r1),cvtpk(r2,r3)}; *(u32x2*)(out+grow*D+d)=wv; }
}

__global__ __launch_bounds__(256) void probe(const u32x4* a,const u32x4* b,const u32x4* c,const u32x4* d,const u32x4* e,u32x4* ws,int n){
  u32x4 acc={0,0,0,0};
  for(int i=blockIdx.x*256+threadIdx.x;i<n;i+=2048*256){ acc^=a[i]^b[i]^c[i]^d[i]^e[i]; }
  ws[blockIdx.x*256+threadIdx.x]=acc;
}


__global__ __launch_bounds__(256) void probe1(const u32x4* a,u32x4* ws,int n){
  u32x4 acc={0,0,0,0};
  for(int i=blockIdx.x*256+threadIdx.x;i<n;i+=2048*256){ acc^=a[i]; }
  if(acc[0]==0x12345678 && acc[1]==0x9abcdef0 && acc[2]==0x13572468) ws[blockIdx.x*256+threadIdx.x]=acc;
}
__global__ __launch_bounds__(256) void probe5(const u32x4* a,const u32x4* b,const u32x4* c,const u32x4* d,const u32x4* e,u32x4* ws,int n){
  u32x4 acc={0,0,0,0};
  for(int i=blockIdx.x*256+threadIdx.x;i<n;i+=2048*256){ acc^=a[i]^b[i]^c[i]^d[i]^e[i]; }
  if(acc[0]==0x12345678 && acc[1]==0x9abcdef0 && acc[2]==0x13572468) ws[blockIdx.x*256+threadIdx.x]=acc;
}
extern "C" void kernel_launch(void* const* d_in,const int* in_sizes,int n_in,void* d_out,int out_size,void* d_ws,size_t ws_size,hipStream_t stream){
  attn_kernel<<<512,256,0,stream>>>((const u16*)d_in[0],(const u16*)d_in[1],(const u16*)d_in[2],(const u16*)d_in[3],(const u16*)d_in[4],(const int*)d_in[5],(const u16*)d_in[6],(u16*)d_out);
}
```

```cpp
#include <hip/hip_runtime.h>
#include <hip/hip_bf16.h>

typedef unsigned short u16;
typedef unsigned long long u64;
constexpr int H = 32, S = 2048, D = 192, M = 2048;

static __device__ __forceinline__ float bf2f(u16 x) { return __uint_as_float((unsigned)x << 16); }
static __device__ __forceinline__ float wsum(float x) { for (int o = 32; o; o >>= 1) x += __shfl_xor(x, o); return x; }
static __device__ __forceinline__ float wmax(float x) { for (int o = 32; o; o >>= 1) x = fmaxf(x, __shfl_xor(x, o)); return x; }

static __device__ __forceinline__ void exact_rows(const u16* q, const u16* k, const u16* v, const u16* mk, const u16* mv, const int* idx,
                                                  const u16* gb, u16* out, unsigned head, unsigned blk, unsigned wave) {
  const int lane = __builtin_amdgcn_mbcnt_hi(~0u, __builtin_amdgcn_mbcnt_lo(~0u, 0u));
  const float scale = 0.07216878364870322f;
  const float g = 1.f / (1.f + __expf(-bf2f(gb[head])));
  const size_t hb = (size_t)head * S * D;
  for (int n = 0; n < 64; ++n) {
    const int band = 4 * blk + (n >> 4) + (blk == 4 ? -1 : blk == 3 && n >= 48 ? 4 * 1 : 0);
    const int row = 64 * band + 16 * wave + (n & 15);
    const u16* qr = q + hb + (size_t)row * D;
    float m = -INFINITY, l = 0.f, a0 = 0.f, a1 = 0.f, a2 = 0.f;
    for (int j0 = 0; j0 <= row; j0 += 64) {
      const int j = j0 + lane;
      float s = -INFINITY;
      if (j <= row) {
        const u16* kr = k + hb + (size_t)j * D;
        s = 0.f;
        for (int d = 0; d < D; ++d) s += bf2f(qr[d]) * bf2f(kr[d]);
        s *= scale;
      }
      const float mn = fmaxf(m, wmax(s));
      const float f = __expf(m - mn), p = __expf(s - mn);
      m = mn; l = l * f + wsum(p); a0 *= f; a1 *= f; a2 *= f;
      const int cnt = min(64, row + 1 - j0);
      for (int jj = 0; jj < cnt; ++jj) {
        const float pj = __shfl(p, jj);
        const u16* vr = v + hb + (size_t)(j0 + jj) * D;
        a0 += pj * bf2f(vr[lane]); a1 += pj * bf2f(vr[lane + 64]); a2 += pj * bf2f(vr[lane + 128]);
      }
    }
    const size_t gr = (size_t)head * S + row;
    const size_t r0 = ((size_t)head * M + idx[gr * 2]) * D, r1 = ((size_t)head * M + idx[gr * 2 + 1]) * D;
    float s0 = 0.f, s1 = 0.f;
    for (int c = 0; c < 3; ++c) { const float x = bf2f(qr[lane + 64 * c]); s0 += x * bf2f(mk[r0 + lane + 64 * c]); s1 += x * bf2f(mk[r1 + lane + 64 * c]); }
    s0 = wsum(s0) * scale; s1 = wsum(s1) * scale;
    const float mm = fmaxf(s0, s1);
    float p0 = __expf(s0 - mm), p1 = __expf(s1 - mm);
    const float iw = g / (p0 + p1), il = (1.f - g) / l;
    p0 *= iw; p1 *= iw;
    __hip_bfloat16* o = (__hip_bfloat16*)out + gr * D;
    o[lane]       = __float2bfloat16(a0 * il + p0 * bf2f(mv[r0 + lane])       + p1 * bf2f(mv[r1 + lane]));
    o[lane + 64]  = __float2bfloat16(a1 * il + p0 * bf2f(mv[r0 + lane + 64])  + p1 * bf2f(mv[r1 + lane + 64]));
    o[lane + 128] = __float2bfloat16(a2 * il + p0 * bf2f(mv[r0 + lane + 128]) + p1 * bf2f(mv[r1 + lane + 128]));
  }
}

__global__ __launch_bounds__(256, 1) void memattn_kernel(const u16* q, const u16* k, const u16* v, const u16* mk, const u16* mv,
                                                         const int* idx, const u16* gb, u16* out, char* ws) {
  const unsigned b = blockIdx.x, wave = __builtin_amdgcn_readfirstlane(threadIdx.x >> 6);
  const u64 ka = (u64)__builtin_amdgcn_kernarg_segment_ptr();
  unsigned bad;
  asm volatile(
"s_load_dwordx16 s[12:27], %1, 0\n"
"s_load_dwordx2 s[28:29], %1, 64\n"
"v_mbcnt_lo_u32_b32 v148, -1, 0\n"
"v_mbcnt_hi_u32_b32 v148, -1, v148\n"
"s_lshr_b32 s10, %2, 3\n"
"s_and_b32 s8, s10, 7\n"
"s_lshl_b32 s10, s10, 2\n"
"s_add_u32 s10, s10, %3\n"
"s_getpc_b64 s[56:57]\n"
"L_CODE:\n"
"s_mov_b32 s58, L_END-L_CODE+0\n"
"s_mov_b32 s59, 0x20000\n"
"s_lshl_b32 s92, s10, 7\n"
"v_lshlrev_b32 v147, 14, v148\n"
"buffer_load_dword v204, v147, s[56:59], s92 offen\n"
"s_mul_i32 s97, %3, 3072\n"
"s_mov_b32 s99, 384\n"
"v_lshrrev_b32 v144, 3, v148\n"
"v_and_b32 v145, 6, v144\n"
"v_and_b32 v146, 7, v148\n"
"v_xor_b32 v145, v145, v146\n"
"v_lshl_add_u32 v145, v145, 4, s97\n"
"v_mad_u32_u24 v234, v144, s99, v145\n"
"s_and_b32 s11, %2, 7\n"
"s_lshl_b32 s11, s11, 2\n"
"s_lshr_b32 s92, %2, 6\n"
"s_add_u32 s11, s11, s92\n"
"s_mul_i32 s36, s11, 786432\n"
"s_xor_b32 s92, s8, 7\n"
"s_max_u32 s9, s8, s92\n"
"s_lshl_b32 s9, s9, 2\n"
"s_lshl_b32 s37, s8, 2\n"
"s_cmp_eq_u32 s8, 3\n"
"s_cselect_b32 s9, 12, s9\n"
"s_cselect_b32 s10, 4, 0\n"
"s_cmp_eq_u32 s8, 4\n"
"s_cselect_b32 s9, 15, s9\n"
"s_cselect_b32 s37, 15, s37\n"
"s_mul_i32 s61, s9, 24576\n"
"s_add_u32 s61, s61, s97\n"
"s_lshl_b32 s72, s97, 3\n"
"s_add_u32 s72, s72, 49152\n"
"s_add_u32 s79, s97, 147456\n"
"s_cmp_ge_u32 %3, 2\n"
"s_cselect_b32 s98, 1073741824, 0\n"
"s_cselect_b32 s79, s72, s79\n"
"s_mov_b32 s50, 786432\n"
"s_mov_b32 s51, 0x20000\n"
"s_mov_b32 s42, 786432\n"
"s_mov_b32 s43, 0x20000\n"
"s_mov_b32 s46, 786432\n"
"s_mov_b32 s47, 0x20000\n"
"s_mov_b32 s54, 786432\n"
"s_mov_b32 s55, 0x20000\n"
"s_mov_b32 s59, 0x20000\n"
"v_mov_b32 v254, 0\n"
"v_lshrrev_b32 v145, 1, v146\n"
"v_add_u32 v255, 1, v145\n"
"v_lshrrev_b32 v255, 2, v255\n"
"v_mad_u32_u24 v145, v255, s10, v145\n"
"s_mul_i32 s10, s10, 24576\n"
"v_lshlrev_b32 v145, 9, v145\n"
"v_and_b32 v146, 1, v148\n"
"v_lshl_add_u32 v145, v146, 6, v145\n"
"v_lshl_add_u32 v255, v144, 3, v145\n"
"v_mul_u32_u24 v207, 0xbfffc, v148\n"
"s_waitcnt lgkmcnt(0)\n"
"s_add_u32 s12, s12, s36\n"
"s_addc_u32 s13, s13, 0\n"
"s_mov_b64 s[48:49], s[12:13]\n"
"buffer_load_dwordx4 v[0:3], v234, s[48:51], s61 offen offset:0\n"
"s_add_u32 s14, s14, s36\n"
"s_addc_u32 s15, s15, 0\n"
"s_mov_b64 s[40:41], s[14:15]\n"
"buffer_load_dwordx4 v[4:7], v234, s[48:51], s61 offen offset:3072\n"
"s_add_u32 m0, s79, 0\n"
"s_nop 0\n"
"buffer_load_dwordx4 v234, s[40:43], s98 offen offset:0 lds\n"
"s_lshl_b32 s92, s11, 5\n"
"s_add_u32 s92, s92, s37\n"
"s_lshl_b32 s92, s92, 2\n"
"s_add_u32 s92, s92, %3\n"
"s_lshl_b32 s92, s92, 7\n"
"s_add_u32 s22, s22, s92\n"
"s_addc_u32 s23, s23, 0\n"
"s_mov_b64 s[56:57], s[22:23]\n"
"s_mov_b32 s58, 4096\n"
"buffer_load_dwordx2 v[248:249], v255, s[56:59], 0 offen\n"
"s_lshl_b32 s92, s11, 1\n"
"s_add_u32 s24, s24, s92\n"
"s_addc_u32 s25, s25, 0\n"
"s_mov_b64 s[56:57], s[24:25]\n"
"s_mov_b32 s58, 2\n"
"buffer_load_ushort v251, v254, s[56:59], 0 offen\n"
"s_add_u32 s16, s16, s36\n"
"s_addc_u32 s17, s17, 0\n"
"s_mov_b64 s[44:45], s[16:17]\n"
"s_add_u32 s18, s18, s36\n"
"s_addc_u32 s19, s19, 0\n"
"s_add_u32 s20, s20, s36\n"
"s_addc_u32 s21, s21, 0\n"
"s_xor_b32 s92, s8, 7\n"
"s_min_u32 s92, s92, s8\n"
"s_lshl_b32 s93, s11, 2\n"
"s_add_u32 s92, s92, s93\n"
"s_lshl_b32 s93, s92, 8\n"
"s_add_u32 s93, s93, 16777216\n"
"s_add_u32 s30, s28, s93\n"
"s_addc_u32 s31, s29, 0\n"
"s_lshl_b32 s92, s92, 2\n"
"s_add_u32 s92, s92, %3\n"
"s_mul_i32 s92, s92, 25600\n"
"s_add_u32 s28, s28, s92\n"
"s_addc_u32 s29, s29, 0\n"
"s_mov_b64 s[56:57], s[30:31]\n"
"s_mov_b32 s58, 4\n"
"buffer_load_dword v252, v254, s[56:59], 0 offen sc1\n"
"s_add_u32 s26, s26, s36\n"
"s_addc_u32 s27, s27, 0\n"
"s_mov_b64 s[52:53], s[26:27]\n"
"buffer_load_dwordx4 v[8:11], v234, s[48:51], s61 offen offset:128\n"
"buffer_load_dwordx4 v[12:15], v234, s[48:51], s61 offen offset:3200\n"
"buffer_load_dwordx4 v[16:19], v234, s[48:51], s61 offen offset:256\n"
"buffer_load_dwordx4 v[20:23], v234, s[48:51], s61 offen offset:3328\n"
"s_add_u32 m0, s79, 896\n"
"s_nop 0\n"
"buffer_load_dwordx4 v234, s[40:43], s98 offen offset:128 lds\n"
"s_add_u32 m0, s79, 1792\n"
"s_nop 0\n"
"buffer_load_dwordx4 v234, s[40:43], s98 offen offset:256 lds\n"
"s_add_u32 s61, s61, 24576\n"
"buffer_load_dwordx4 v[24:27], v234, s[48:51], s61 offen offset:0\n"
"buffer_load_dwordx4 v[28:31], v234, s[48:51], s61 offen offset:3072\n"
"buffer_load_dwordx4 v[32:35], v234, s[48:51], s61 offen offset:128\n"
"buffer_load_dwordx4 v[36:39], v234, s[48:51], s61 offen offset:3200\n"
"buffer_load_dwordx4 v[40:43], v234, s[48:51], s61 offen offset:256\n"
"buffer_load_dwordx4 v[44:47], v234, s[48:51], s61 offen offset:3328\n"
"s_mov_b32 s73, s72\n"
"s_mov_b32 s74, 12288\n"
"s_add_u32 s92, s72, 12288\n"
"s_lshl_b32 s76, %3, 11\n"
"s_add_u32 s76, s76, 153600\n"
"v_and_b32 v149, 15, v148\n"
"v_lshrrev_b32 v150, 4, v148\n"
"v_lshrrev_b32 v144, 3, v149\n"
"v_mul_u32_u24 v144, 3072, v144\n"
"v_and_b32 v145, 7, v149\n"
"v_lshl_add_u32 v144, v145, 7, v144\n"
"v_and_b32 v145, 6, v149\n"
"v_xor_b32 v145, v145, v150\n"
"v_lshl_add_u32 v228, v145, 4, v144\n"
"v_xor_b32 v229, 64, v228\n"
"v_add_u32 v238, s92, v228\n"
"v_add_u32 v239, s92, v229\n"
"v_lshrrev_b32 v146, 2, v148\n"
"v_lshrrev_b32 v144, 3, v146\n"
"v_mul_u32_u24 v144, 3072, v144\n"
"v_and_b32 v145, 7, v146\n"
"v_lshl_add_u32 v144, v145, 7, v144\n"
"v_and_b32 v145, 6, v146\n"
"v_bfe_u32 v151, v148, 1, 1\n"
"v_xor_b32 v145, v145, v151\n"
"v_lshl_add_u32 v144, v145, 4, v144\n"
"v_and_b32 v145, 1, v148\n"
"v_lshl_add_u32 v230, v145, 3, v144\n"
"v_xor_b32 v231, 32, v230\n"
"v_xor_b32 v232, 64, v230\n"
"v_xor_b32 v233, 96, v230\n"
"v_lshl_add_u32 v240, v148, 4, s76\n"
"v_bfe_u32 v144, v148, 3, 1\n"
"v_lshlrev_b32 v144, 11, v144\n"
"v_sub_u32 v144, v228, v144\n"
"v_add_u32 v241, s76, v144\n"
"v_xor_b32 v242, 64, v241\n"
"v_and_b32 v144, 0x38, v148\n"
"v_lshlrev_b32 v250, 2, v144\n"
"s_lshl_b32 s92, %3, 4\n"
"v_add_u32 v144, s92, v149\n"
"v_lshlrev_b32 v145, 2, v150\n"
"v_sub_u32 v144, v144, v145\n"
"v_cvt_f32_i32 v144, v144\n"
"v_mul_f32 v236, 0x71800000, v144\n"
"s_mov_b32 s75, 0x70\n"
"s_mov_b32 s79, 0x07060302\n"
"s_mov_b32 s78, 0xffff0000\n"
"s_mov_b32 s60, 0\n"
"s_mov_b32 s68, 0x300003\n"
"s_mov_b32 s69, 0x30000300\n"
"s_mov_b32 s70, 0xc0000c\n"
"s_mov_b32 s71, 0xc0000c00\n"
"v_mov_b32 v168, 0\n"
"v_mov_b32 v169, 0\n"
"v_mov_b32 v170, 0\n"
"v_mov_b32 v171, 0\n"
"s_mov_b32 s82, 0xaaaaaaaa\n"
"s_mov_b32 s83, 0xaaaaaaaa\n"
"v_mfma_f32_32x32x16_bf16 a[168:183], v[168:171], v[168:171], 0\n"
"s_mov_b32 s84, 0xcccccccc\n"
"s_mov_b32 s85, 0xcccccccc\n"
"v_mfma_f32_32x32x16_bf16 a[184:199], v[168:171], v[168:171], 0\n"
"s_mov_b64 s[86:87], 0\n"
"s_mov_b32 s90, 0x3dd53b95\n"
"s_mov_b32 s91, 0x3dd53b95\n"
"v_mfma_f32_32x32x16_bf16 a[200:215], v[168:171], v[168:171], 0\n"
"s_mul_i32 s89, s37, 24576\n"
"s_add_u32 s89, s89, s97\n"
"v_mfma_f32_16x16x32_bf16 a[216:219], v[168:171], v[168:171], 0\n"
"v_mov_b32 v144, 0x44444444\n"
"v_mov_b32 v145, 0xeeeeeeee\n"
"v_mov_b32 v146, 0x3f803f80\n"
"v_mfma_f32_16x16x32_bf16 a[220:223], v[168:171], v[168:171], 0\n"
"v_cndmask_b32 v237, v144, v145, s[82:83]\n"
"v_cndmask_b32 v144, v146, 0, s[84:85]\n"
"v_cndmask_b32 v145, 0, v146, s[84:85]\n"
"v_mfma_f32_32x32x16_bf16 v[96:111], v[168:171], v[168:171], 0\n"
"v_accvgpr_write_b32 a224, v144\n"
"v_accvgpr_write_b32 a225, v144\n"
"v_mfma_f32_32x32x16_bf16 v[112:127], v[168:171], v[168:171], 0\n"
"v_accvgpr_write_b32 a226, v144\n"
"v_accvgpr_write_b32 a227, v144\n"
"v_accvgpr_write_b32 a228, v145\n"
"v_mfma_f32_32x32x16_bf16 v[128:143], v[168:171], v[168:171], 0\n"
"v_accvgpr_write_b32 a229, v145\n"
"v_accvgpr_write_b32 a230, v145\n"
"v_accvgpr_write_b32 a231, v145\n"
"s_getpc_b64 s[64:65]\n"
"L_BASE:\n"
"s_cmp_eq_u32 s8, 0\n"
"s_cbranch_scc1 L_TAB0\n"
"s_cmp_eq_u32 s8, 1\n"
"s_cbranch_scc1 L_TAB1\n"
"s_cmp_eq_u32 s8, 2\n"
"s_cbranch_scc1 L_TAB2\n"
"s_cmp_eq_u32 s8, 3\n"
"s_cbranch_scc1 L_TAB3\n"
"s_cmp_eq_u32 s8, 4\n"
"s_cbranch_scc1 L_TAB4\n"
"s_cmp_eq_u32 s8, 5\n"
"s_cbranch_scc1 L_TAB5\n"
"s_cmp_eq_u32 s8, 6\n"
"s_cbranch_scc1 L_TAB6\n"
"s_branch L_TAB7\n"
"L_WARM0:\n"
"s_cmp_eq_u32 %3, 0\n"
"s_cbranch_scc1 L_W0G\n"
"s_cmp_eq_u32 %3, 1\n"
"s_cbranch_scc1 L_W0GA\n"
"s_cmp_eq_u32 %3, 2\n"
"s_cbranch_scc1 L_W0B1\n"
"s_branch L_W0VS\n"
"L_WARM1:\n"
"s_cmp_eq_u32 %3, 0\n"
"s_cbranch_scc1 L_W1G\n"
"s_cmp_eq_u32 %3, 1\n"
"s_cbranch_scc1 L_W1GA\n"
"s_cmp_eq_u32 %3, 2\n"
"s_cbranch_scc1 L_W1A1\n"
"s_branch L_W1VS\n"
"L_WARM2:\n"
"s_cmp_eq_u32 %3, 0\n"
"s_cbranch_scc1 L_W2G\n"
"s_cmp_eq_u32 %3, 1\n"
"s_cbranch_scc1 L_W2GA\n"
"s_cmp_eq_u32 %3, 2\n"
"s_cbranch_scc1 L_W2A1\n"
"s_branch L_W2VS\n"
"L_TAB0:\n"
"s_mov_b32 s92, L_G-L_BASE+4194304\n"
"v_writelane_b32 v235, s92, 0\n"
"s_mov_b32 s92, L_GA-L_BASE+4456448\n"
"v_writelane_b32 v235, s92, 1\n"
"s_mov_b32 s92, L_TG0-L_BASE+4718592\n"
"v_writelane_b32 v235, s92, 2\n"
"s_mov_b32 s92, L_TG1-L_BASE+4980736\n"
"v_writelane_b32 v235, s92, 3\n"
"s_mov_b32 s92, L_TG2-L_BASE+5242880\n"
"v_writelane_b32 v235, s92, 4\n"
"s_mov_b32 s92, L_TG3-L_BASE+5505024\n"
"v_writelane_b32 v235, s92, 5\n"
"s_mov_b32 s92, L_VS-L_BASE+5767168\n"
"v_writelane_b32 v235, s92, 6\n"
"s_mov_b32 s92, L_GA-L_BASE+6029312\n"
"v_writelane_b32 v235, s92, 7\n"
"s_mov_b32 s92, L_N2-L_BASE+6291456\n"
"v_writelane_b32 v235, s92, 8\n"
"s_mov_b32 s92, L_GA-L_BASE+6553600\n"
"v_writelane_b32 v235, s92, 9\n"
"s_mov_b32 s92, L_N2-L_BASE+6815744\n"
"v_writelane_b32 v235, s92, 10\n"
"s_mov_b32 s92, L_ST-L_BASE+7077888\n"
"v_writelane_b32 v235, s92, 11\n"
"s_mov_b32 s92, L_N2-L_BASE+0\n"
"v_writelane_b32 v235, s92, 12\n"
"s_mov_b32 s92, L_SW-L_BASE+0\n"
"v_writelane_b32 v235, s92, 13\n"
"s_mov_b32 s92, L_FLW-L_BASE+0\n"
"v_writelane_b32 v235, s92, 14\n"
"s_mov_b32 s92, L_EPZ-L_BASE+0\n"
"v_writelane_b32 v235, s92, 15\n"
"s_mov_b32 s92, L_D0-L_BASE+262144\n"
"v_writelane_b32 v235, s92, 16\n"
"s_mov_b32 s92, L_D1-L_BASE+524288\n"
"v_writelane_b32 v235, s92, 17\n"
"s_mov_b32 s92, L_D2-L_BASE+786432\n"
"v_writelane_b32 v235, s92, 18\n"
"s_mov_b32 s92, L_D3-L_BASE+10485760\n"
"v_writelane_b32 v235, s92, 19\n"
"s_mov_b32 s92, L_E3-L_BASE+0\n"
"v_writelane_b32 v235, s92, 20\n"
"s_mov_b32 s66, 368640\n"
"s_branch L_WARM0\n"
"L_TAB1:\n"
"s_mov_b32 s92, L_G-L_BASE+4194304\n"
"v_writelane_b32 v235, s92, 0\n"
"s_mov_b32 s92, L_GA-L_BASE+4456448\n"
"v_writelane_b32 v235, s92, 1\n"
"s_mov_b32 s92, L_TG0-L_BASE+4718592\n"
"v_writelane_b32 v235, s92, 2\n"
"s_mov_b32 s92, L_TG1-L_BASE+4980736\n"
"v_writelane_b32 v235, s92, 3\n"
"s_mov_b32 s92, L_TG2-L_BASE+5242880\n"
"v_writelane_b32 v235, s92, 4\n"
"s_mov_b32 s92, L_TG3-L_BASE+5505024\n"
"v_writelane_b32 v235, s92, 5\n"
"s_mov_b32 s92, L_VS-L_BASE+5767168\n"
"v_writelane_b32 v235, s92, 6\n"
"s_mov_b32 s92, L_GA-L_BASE+6029312\n"
"v_writelane_b32 v235, s92, 7\n"
"s_mov_b32 s92, L_N2-L_BASE+0\n"
"v_writelane_b32 v235, s92, 8\n"
"s_mov_b32 s92, L_SW-L_BASE+0\n"
"v_writelane_b32 v235, s92, 9\n"
"s_mov_b32 s92, L_GA-L_BASE+262144\n"
"v_writelane_b32 v235, s92, 10\n"
"s_mov_b32 s92, L_FL-L_BASE+0\n"
"v_writelane_b32 v235, s92, 11\n"
"s_mov_b32 s92, L_N2-L_BASE+524288\n"
"v_writelane_b32 v235, s92, 12\n"
"s_mov_b32 s92, L_ST-L_BASE+786432\n"
"v_writelane_b32 v235, s92, 13\n"
"s_mov_b32 s92, L_N2-L_BASE+1048576\n"
"v_writelane_b32 v235, s92, 14\n"
"s_mov_b32 s92, L_EPZ-L_BASE+0\n"
"v_writelane_b32 v235, s92, 15\n"
"s_mov_b32 s92, L_D0-L_BASE+1310720\n"
"v_writelane_b32 v235, s92, 16\n"
"s_mov_b32 s92, L_D1-L_BASE+1572864\n"
"v_writelane_b32 v235, s92, 17\n"
"s_mov_b32 s92, L_D2-L_BASE+1835008\n"
"v_writelane_b32 v235, s92, 18\n"
"s_mov_b32 s92, L_D3-L_BASE+10485760\n"
"v_writelane_b32 v235, s92, 19\n"
"s_mov_b32 s92, L_E3-L_BASE+0\n"
"v_writelane_b32 v235, s92, 20\n"
"s_mov_b32 s66, 368640\n"
"s_branch L_WARM0\n"
"L_TAB2:\n"
"s_mov_b32 s92, L_G-L_BASE+4194304\n"
"v_writelane_b32 v235, s92, 0\n"
"s_mov_b32 s92, L_GA-L_BASE+4456448\n"
"v_writelane_b32 v235, s92, 1\n"
"s_mov_b32 s92, L_TG0-L_BASE+4718592\n"
"v_writelane_b32 v235, s92, 2\n"
"s_mov_b32 s92, L_TG1-L_BASE+4980736\n"
"v_writelane_b32 v235, s92, 3\n"
"s_mov_b32 s92, L_TG2-L_BASE+1835008\n"
"v_writelane_b32 v235, s92, 4\n"
"s_mov_b32 s92, L_SW-L_BASE+0\n"
"v_writelane_b32 v235, s92, 5\n"
"s_mov_b32 s92, L_TS3-L_BASE+1572864\n"
"v_writelane_b32 v235, s92, 6\n"
"s_mov_b32 s92, L_FL-L_BASE+0\n"
"v_writelane_b32 v235, s92, 7\n"
"s_mov_b32 s92, L_VS-L_BASE+1310720\n"
"v_writelane_b32 v235, s92, 8\n"
"s_mov_b32 s92, L_GA-L_BASE+1048576\n"
"v_writelane_b32 v235, s92, 9\n"
"s_mov_b32 s92, L_N2-L_BASE+0\n"
"v_writelane_b32 v235, s92, 10\n"
"s_mov_b32 s92, L_GA-L_BASE+262144\n"
"v_writelane_b32 v235, s92, 11\n"
"s_mov_b32 s92, L_N2-L_BASE+524288\n"
"v_writelane_b32 v235, s92, 12\n"
"s_mov_b32 s92, L_ST-L_BASE+786432\n"
"v_writelane_b32 v235, s92, 13\n"
"s_mov_b32 s92, L_N2-L_BASE+2097152\n"
"v_writelane_b32 v235, s92, 14\n"
"s_mov_b32 s92, L_EPZ-L_BASE+0\n"
"v_writelane_b32 v235, s92, 15\n"
"s_mov_b32 s92, L_D0-L_BASE+2359296\n"
"v_writelane_b32 v235, s92, 16\n"
"s_mov_b32 s92, L_D1-L_BASE+2621440\n"
"v_writelane_b32 v235, s92, 17\n"
"s_mov_b32 s92, L_D2-L_BASE+2883584\n"
"v_writelane_b32 v235, s92, 18\n"
"s_mov_b32 s92, L_D3-L_BASE+10485760\n"
"v_writelane_b32 v235, s92, 19\n"
"s_mov_b32 s92, L_E3-L_BASE+0\n"
"v_writelane_b32 v235, s92, 20\n"
"s_mov_b32 s66, 368640\n"
"s_branch L_WARM0\n"
"L_TAB3:\n"
"s_mov_b32 s92, L_D0-L_BASE+3407872\n"
"v_writelane_b32 v235, s92, 0\n"
"s_mov_b32 s92, L_P1-L_BASE+3670016\n"
"v_writelane_b32 v235, s92, 1\n"
"s_mov_b32 s92, L_P2-L_BASE+3932160\n"
"v_writelane_b32 v235, s92, 2\n"
"s_mov_b32 s92, L_UM-L_BASE+4194304\n"
"v_writelane_b32 v235, s92, 3\n"
"s_mov_b32 s92, L_P3-L_BASE+2883584\n"
"v_writelane_b32 v235, s92, 4\n"
"s_mov_b32 s92, L_GA-L_BASE+2621440\n"
"v_writelane_b32 v235, s92, 5\n"
"s_mov_b32 s92, L_TS0-L_BASE+2359296\n"
"v_writelane_b32 v235, s92, 6\n"
"s_mov_b32 s92, L_TS1-L_BASE+2097152\n"
"v_writelane_b32 v235, s92, 7\n"
"s_mov_b32 s92, L_TS2-L_BASE+1835008\n"
"v_writelane_b32 v235, s92, 8\n"
"s_mov_b32 s92, L_TS3-L_BASE+1572864\n"
"v_writelane_b32 v235, s92, 9\n"
"s_mov_b32 s92, L_VS-L_BASE+1310720\n"
"v_writelane_b32 v235, s92, 10\n"
"s_mov_b32 s92, L_GA-L_BASE+1048576\n"
"v_writelane_b32 v235, s92, 11\n"
"s_mov_b32 s92, L_N2-L_BASE+0\n"
"v_writelane_b32 v235, s92, 12\n"
"s_mov_b32 s92, L_GA-L_BASE+262144\n"
"v_writelane_b32 v235, s92, 13\n"
"s_mov_b32 s92, L_N2-L_BASE+524288\n"
"v_writelane_b32 v235, s92, 14\n"
"s_mov_b32 s92, L_ST-L_BASE+4456448\n"
"v_writelane_b32 v235, s92, 15\n"
"s_mov_b32 s92, L_P3-L_BASE+4718592\n"
"v_writelane_b32 v235, s92, 16\n"
"s_mov_b32 s92, L_P3-L_BASE+4980736\n"
"v_writelane_b32 v235, s92, 17\n"
"s_mov_b32 s92, L_MM-L_BASE+786432\n"
"v_writelane_b32 v235, s92, 18\n"
"s_mov_b32 s92, L_N2-L_BASE+10485760\n"
"v_writelane_b32 v235, s92, 19\n"
"s_mov_b32 s92, L_EPZ-L_BASE+0\n"
"v_writelane_b32 v235, s92, 20\n"
"s_mov_b32 s92, L_EA-L_BASE+0\n"
"v_writelane_b32 v235, s92, 21\n"
"s_mov_b32 s66, 294912\n"
"s_branch L_WARM1\n"
"L_TAB4:\n"
"s_mov_b32 s92, L_G-L_BASE+3407872\n"
"v_writelane_b32 v235, s92, 0\n"
"s_mov_b32 s92, L_G-L_BASE+3145728\n"
"v_writelane_b32 v235, s92, 1\n"
"s_mov_b32 s92, L_PFQ-L_BASE+2097152\n"
"v_writelane_b32 v235, s92, 2\n"
"s_mov_b32 s92, L_G-L_BASE+2883584\n"
"v_writelane_b32 v235, s92, 3\n"
"s_mov_b32 s92, L_GA-L_BASE+2621440\n"
"v_writelane_b32 v235, s92, 4\n"
"s_mov_b32 s92, L_TS0-L_BASE+2359296\n"
"v_writelane_b32 v235, s92, 5\n"
"s_mov_b32 s92, L_TS1-L_BASE+2097152\n"
"v_writelane_b32 v235, s92, 6\n"
"s_mov_b32 s92, L_TS2-L_BASE+1835008\n"
"v_writelane_b32 v235, s92, 7\n"
"s_mov_b32 s92, L_TS3-L_BASE+1572864\n"
"v_writelane_b32 v235, s92, 8\n"
"s_mov_b32 s92, L_VS-L_BASE+1310720\n"
"v_writelane_b32 v235, s92, 9\n"
"s_mov_b32 s92, L_GA-L_BASE+1048576\n"
"v_writelane_b32 v235, s92, 10\n"
"s_mov_b32 s92, L_N2-L_BASE+0\n"
"v_writelane_b32 v235, s92, 11\n"
"s_mov_b32 s92, L_GA-L_BASE+262144\n"
"v_writelane_b32 v235, s92, 12\n"
"s_mov_b32 s92, L_N2-L_BASE+524288\n"
"v_writelane_b32 v235, s92, 13\n"
"s_mov_b32 s92, L_ST-L_BASE+786432\n"
"v_writelane_b32 v235, s92, 14\n"
"s_mov_b32 s92, L_N2-L_BASE+3932160\n"
"v_writelane_b32 v235, s92, 15\n"
"s_mov_b32 s92, L_EPZ-L_BASE+0\n"
"v_writelane_b32 v235, s92, 16\n"
"s_mov_b32 s92, L_D0-L_BASE+4194304\n"
"v_writelane_b32 v235, s92, 17\n"
"s_mov_b32 s92, L_D1-L_BASE+4456448\n"
"v_writelane_b32 v235, s92, 18\n"
"s_mov_b32 s92, L_D2-L_BASE+4718592\n"
"v_writelane_b32 v235, s92, 19\n"
"s_mov_b32 s92, L_D3-L_BASE+10485760\n"
"v_writelane_b32 v235, s92, 20\n"
"s_mov_b32 s92, L_E3-L_BASE+0\n"
"v_writelane_b32 v235, s92, 21\n"
"s_mov_b32 s66, 344064\n"
"s_branch L_WARM2\n"
"L_TAB5:\n"
"s_mov_b32 s92, L_G-L_BASE+3407872\n"
"v_writelane_b32 v235, s92, 0\n"
"s_mov_b32 s92, L_G-L_BASE+3145728\n"
"v_writelane_b32 v235, s92, 1\n"
"s_mov_b32 s92, L_GA-L_BASE+2883584\n"
"v_writelane_b32 v235, s92, 2\n"
"s_mov_b32 s92, L_TS0-L_BASE+2621440\n"
"v_writelane_b32 v235, s92, 3\n"
"s_mov_b32 s92, L_TS1-L_BASE+2359296\n"
"v_writelane_b32 v235, s92, 4\n"
"s_mov_b32 s92, L_TS2-L_BASE+2097152\n"
"v_writelane_b32 v235, s92, 5\n"
"s_mov_b32 s92, L_TS3-L_BASE+1835008\n"
"v_writelane_b32 v235, s92, 6\n"
"s_mov_b32 s92, L_VS-L_BASE+1572864\n"
"v_writelane_b32 v235, s92, 7\n"
"s_mov_b32 s92, L_GA-L_BASE+1310720\n"
"v_writelane_b32 v235, s92, 8\n"
"s_mov_b32 s92, L_N2-L_BASE+1048576\n"
"v_writelane_b32 v235, s92, 9\n"
"s_mov_b32 s92, L_GA-L_BASE+5505024\n"
"v_writelane_b32 v235, s92, 10\n"
"s_mov_b32 s92, L_P1-L_BASE+5767168\n"
"v_writelane_b32 v235, s92, 11\n"
"s_mov_b32 s92, L_P2-L_BASE+6029312\n"
"v_writelane_b32 v235, s92, 12\n"
"s_mov_b32 s92, L_P3-L_BASE+0\n"
"v_writelane_b32 v235, s92, 13\n"
"s_mov_b32 s92, L_N2-L_BASE+262144\n"
"v_writelane_b32 v235, s92, 14\n"
"s_mov_b32 s92, L_ST-L_BASE+524288\n"
"v_writelane_b32 v235, s92, 15\n"
"s_mov_b32 s92, L_N2-L_BASE+786432\n"
"v_writelane_b32 v235, s92, 16\n"
"s_mov_b32 s92, L_POLL-L_BASE+5242880\n"
"v_writelane_b32 v235, s92, 17\n"
"s_mov_b32 s92, L_D0-L_BASE+10485760\n"
"v_writelane_b32 v235, s92, 18\n"
"s_mov_b32 s92, L_EA-L_BASE+0\n"
"v_writelane_b32 v235, s92, 19\n"
"s_mov_b32 s66, 344064\n"
"s_branch L_WARM1\n"
".p2align 12\n"
"L_TABDONE:\n"
"s_add_u32 s77, s66, 12288\n"
"s_add_u32 s61, s61, 24576\n"
"buffer_load_dwordx4 v[48:51], v234, s[48:51], s61 offen offset:0\n"
"s_add_u32 m0, s97, 0\n"
"s_waitcnt vmcnt(17)\n"
"v_lshlrev_b32 v172, 16, v0\n"
"v_and_b32 v173, s78, v0\n"
"v_lshlrev_b32 v174, 16, v1\n"
"v_and_b32 v175, s78, v1\n"
"v_lshlrev_b32 v176, 16, v2\n"
"v_and_b32 v177, s78, v2\n"
"v_lshlrev_b32 v178, 16, v3\n"
"v_and_b32 v179, s78, v3\n"
"v_pk_mul_f32 v[172:173], v[172:173], s[90:91]\n"
"v_pk_mul_f32 v[174:175], v[174:175], s[90:91]\n"
"v_pk_mul_f32 v[176:177], v[176:177], s[90:91]\n"
"v_pk_mul_f32 v[178:179], v[178:179], s[90:91]\n"
"v_cvt_pk_bf16_f32 v0, v172, v173\n"
"v_cvt_pk_bf16_f32 v1, v174, v175\n"
"v_cvt_pk_bf16_f32 v2, v176, v177\n"
"v_cvt_pk_bf16_f32 v3, v178, v179\n"
"v_lshlrev_b32 v172, 16, v4\n"
"v_and_b32 v173, s78, v4\n"
"v_lshlrev_b32 v174, 16, v5\n"
"v_and_b32 v175, s78, v5\n"
"v_lshlrev_b32 v176, 16, v6\n"
"v_and_b32 v177, s78, v6\n"
"v_lshlrev_b32 v178, 16, v7\n"
"v_and_b32 v179, s78, v7\n"
"buffer_load_dwordx4 v[52:55], v234, s[48:51], s61 offen offset:3072\n"
"ds_write_b128 v240, v[0:3] offset:0\n"
"v_pk_mul_f32 v[172:173], v[172:173], s[90:91]\n"
"v_pk_mul_f32 v[174:175], v[174:175], s[90:91]\n"
"v_pk_mul_f32 v[176:177], v[176:177], s[90:91]\n"
"v_pk_mul_f32 v[178:179], v[178:179], s[90:91]\n"
"v_cvt_pk_bf16_f32 v4, v172, v173\n"
"v_cvt_pk_bf16_f32 v5, v174, v175\n"
"v_cvt_pk_bf16_f32 v6, v176, v177\n"
"v_cvt_pk_bf16_f32 v7, v178, v179\n"
"s_waitcnt vmcnt(12)\n"
"ds_write_b128 v240, v[4:7] offset:1024\n"
"v_lshlrev_b32 v172, 16, v8\n"
"v_and_b32 v173, s78, v8\n"
"v_lshlrev_b32 v174, 16, v9\n"
"v_and_b32 v175, s78, v9\n"
"v_lshlrev_b32 v176, 16, v10\n"
"buffer_load_dwordx4 v[56:59], v234, s[48:51], s61 offen offset:128\n"
"ds_read_b128 a[0:3], v241\n"
"ds_read_b128 a[4:7], v242\n"
"v_and_b32 v177, s78, v10\n"
"v_lshlrev_b32 v178, 16, v11\n"
"v_and_b32 v179, s78, v11\n"
"v_pk_mul_f32 v[172:173], v[172:173], s[90:91]\n"
"v_pk_mul_f32 v[174:175], v[174:175], s[90:91]\n"
"v_pk_mul_f32 v[176:177], v[176:177], s[90:91]\n"
"v_pk_mul_f32 v[178:179], v[178:179], s[90:91]\n"
"v_cvt_pk_bf16_f32 v8, v172, v173\n"
"v_cvt_pk_bf16_f32 v9, v174, v175\n"
"v_cvt_pk_bf16_f32 v10, v176, v177\n"
"v_cvt_pk_bf16_f32 v11, v178, v179\n"
"v_lshlrev_b32 v172, 16, v12\n"
"v_and_b32 v173, s78, v12\n"
"v_lshlrev_b32 v174, 16, v13\n"
"v_and_b32 v175, s78, v13\n"
"v_lshlrev_b32 v176, 16, v14\n"
"v_and_b32 v177, s78, v14\n"
"v_lshlrev_b32 v178, 16, v15\n"
"v_and_b32 v179, s78, v15\n"
"v_pk_mul_f32 v[172:173], v[172:173], s[90:91]\n"
"v_pk_mul_f32 v[174:175], v[174:175], s[90:91]\n"
"v_pk_mul_f32 v[176:177], v[176:177], s[90:91]\n"
"v_pk_mul_f32 v[178:179], v[178:179], s[90:91]\n"
"buffer_load_dwordx4 v[60:63], v234, s[48:51], s61 offen offset:3200\n"
"ds_write_b128 v240, v[8:11] offset:0\n"
"v_cvt_pk_bf16_f32 v12, v172, v173\n"
"v_cvt_pk_bf16_f32 v13, v174, v175\n"
"v_cvt_pk_bf16_f32 v14, v176, v177\n"
"v_cvt_pk_bf16_f32 v15, v178, v179\n"
"s_waitcnt vmcnt(12)\n"
"v_lshlrev_b32 v172, 16, v16\n"
"v_and_b32 v173, s78, v16\n"
"v_lshlrev_b32 v174, 16, v17\n"
"v_and_b32 v175, s78, v17\n"
"v_lshlrev_b32 v176, 16, v18\n"
"v_and_b32 v177, s78, v18\n"
"v_lshlrev_b32 v178, 16, v19\n"
"v_and_b32 v179, s78, v19\n"
"v_pk_mul_f32 v[172:173], v[172:173], s[90:91]\n"
"v_pk_mul_f32 v[174:175], v[174:175], s[90:91]\n"
"v_pk_mul_f32 v[176:177], v[176:177], s[90:91]\n"
"v_pk_mul_f32 v[178:179], v[178:179], s[90:91]\n"
"v_cvt_pk_bf16_f32 v16, v172, v173\n"
"v_cvt_pk_bf16_f32 v17, v174, v175\n"
"v_cvt_pk_bf16_f32 v18, v176, v177\n"
"v_cvt_pk_bf16_f32 v19, v178, v179\n"
"buffer_load_dwordx4 v[64:67], v234, s[48:51], s61 offen offset:256\n"
"ds_write_b128 v240, v[12:15] offset:1024\n"
"v_lshlrev_b32 v172, 16, v20\n"
"v_and_b32 v173, s78, v20\n"
"v_lshlrev_b32 v174, 16, v21\n"
"v_and_b32 v175, s78, v21\n"
"v_lshlrev_b32 v176, 16, v22\n"
"v_and_b32 v177, s78, v22\n"
"v_lshlrev_b32 v178, 16, v23\n"
"v_and_b32 v179, s78, v23\n"
"v_pk_mul_f32 v[172:173], v[172:173], s[90:91]\n"
"v_pk_mul_f32 v[174:175], v[174:175], s[90:91]\n"
"v_pk_mul_f32 v[176:177], v[176:177], s[90:91]\n"
"v_pk_mul_f32 v[178:179], v[178:179], s[90:91]\n"
"v_cvt_pk_bf16_f32 v20, v172, v173\n"
"v_cvt_pk_bf16_f32 v21, v174, v175\n"
"v_cvt_pk_bf16_f32 v22, v176, v177\n"
"v_cvt_pk_bf16_f32 v23, v178, v179\n"
"s_waitcnt vmcnt(11)\n"
"s_barrier\n"
"ds_read_b128 a[8:11], v241\n"
"ds_read_b128 a[12:15], v242\n"
"buffer_load_dwordx4 v[68:71], v234, s[48:51], s61 offen offset:3328\n"
"ds_write_b128 v240, v[16:19] offset:0\n"
"s_add_u32 s61, s61, s10\n"
"s_add_u32 s61, s61, 24576\n"
"v_add_u32 v152, 147456, v228\n"
"v_add_u32 v153, 147456, v229\n"
"s_waitcnt vmcnt(10)\n"
"v_lshlrev_b32 v172, 16, v24\n"
"v_and_b32 v173, s78, v24\n"
"v_lshlrev_b32 v174, 16, v25\n"
"v_and_b32 v175, s78, v25\n"
"v_lshlrev_b32 v176, 16, v26\n"
"v_and_b32 v177, s78, v26\n"
"v_lshlrev_b32 v178, 16, v27\n"
"v_and_b32 v179, s78, v27\n"
"v_pk_mul_f32 v[172:173], v[172:173], s[90:91]\n"
"v_pk_mul_f32 v[174:175], v[174:175], s[90:91]\n"
"v_pk_mul_f32 v[176:177], v[176:177], s[90:91]\n"
"v_pk_mul_f32 v[178:179], v[178:179], s[90:91]\n"
"v_mfma_f32_32x32x16_bf16 v[0:15], v[168:171], v[168:171], 0\n"
"ds_read_b128 a[144:147], v152 offset:0\n"
"ds_read_b128 a[148:151], v153 offset:0\n"
"buffer_load_dwordx4 v[72:75], v234, s[48:51], s61 offen offset:0\n"
"ds_read_b128 a[152:155], v152 offset:1024\n"
"v_cvt_pk_bf16_f32 v24, v172, v173\n"
"v_cvt_pk_bf16_f32 v25, v174, v175\n"
"v_cvt_pk_bf16_f32 v26, v176, v177\n"
"v_cvt_pk_bf16_f32 v27, v178, v179\n"
"v_lshlrev_b32 v172, 16, v28\n"
"v_and_b32 v173, s78, v28\n"
"v_lshlrev_b32 v174, 16, v29\n"
"v_and_b32 v175, s78, v29\n"
"v_lshlrev_b32 v176, 16, v30\n"
"v_and_b32 v177, s78, v30\n"
"v_lshlrev_b32 v178, 16, v31\n"
"v_and_b32 v179, s78, v31\n"
"v_pk_mul_f32 v[172:173], v[172:173], s[90:91]\n"
"v_pk_mul_f32 v[174:175], v[174:175], s[90:91]\n"
"v_pk_mul_f32 v[176:177], v[176:177], s[90:91]\n"
"v_pk_mul_f32 v[178:179], v[178:179], s[90:91]\n"
"v_cvt_pk_bf16_f32 v28, v172, v173\n"
"v_cvt_pk_bf16_f32 v29, v174, v175\n"
"v_cvt_pk_bf16_f32 v30, v176, v177\n"
"v_cvt_pk_bf16_f32 v31, v178, v179\n"
"s_waitcnt vmcnt(9)\n"
"ds_write_b128 v240, v[20:23] offset:1024\n"
"buffer_load_dwordx4 v[76:79], v234, s[48:51], s61 offen offset:3072\n"
"s_waitcnt lgkmcnt(3)\n"
"v_mfma_f32_16x16x32_bf16 v[188:191], a[144:147], a[0:3], 0\n"
"v_lshlrev_b32 v172, 16, v32\n"
"v_and_b32 v173, s78, v32\n"
"v_lshlrev_b32 v174, 16, v33\n"
"v_and_b32 v175, s78, v33\n"
"v_lshlrev_b32 v176, 16, v34\n"
"v_and_b32 v177, s78, v34\n"
"v_lshlrev_b32 v178, 16, v35\n"
"v_and_b32 v179, s78, v35\n"
"v_pk_mul_f32 v[172:173], v[172:173], s[90:91]\n"
"v_pk_mul_f32 v[174:175], v[174:175], s[90:91]\n"
"v_pk_mul_f32 v[176:177], v[176:177], s[90:91]\n"
"v_pk_mul_f32 v[178:179], v[178:179], s[90:91]\n"
"v_cvt_pk_bf16_f32 v32, v172, v173\n"
"v_cvt_pk_bf16_f32 v33, v174, v175\n"
"v_cvt_pk_bf16_f32 v34, v176, v177\n"
"v_cvt_pk_bf16_f32 v35, v178, v179\n"
"s_waitcnt lgkmcnt(2)\n"
"v_mfma_f32_16x16x32_bf16 v[188:191], a[148:151], a[4:7], v[188:191]\n"
"ds_read_b128 a[156:159], v153 offset:1024\n"
"ds_read_b128 a[16:19], v241\n"
"v_lshlrev_b32 v172, 16, v36\n"
"v_and_b32 v173, s78, v36\n"
"v_lshlrev_b32 v174, 16, v37\n"
"buffer_load_dwordx4 v[80:83], v234, s[48:51], s61 offen offset:128\n"
"ds_read_b128 a[160:163], v152 offset:2048\n"
"ds_read_b128 a[20:23], v242\n"
"v_and_b32 v175, s78, v37\n"
"v_lshlrev_b32 v176, 16, v38\n"
"v_and_b32 v177, s78, v38\n"
"v_lshlrev_b32 v178, 16, v39\n"
"v_and_b32 v179, s78, v39\n"
"v_pk_mul_f32 v[172:173], v[172:173], s[90:91]\n"
"v_pk_mul_f32 v[174:175], v[174:175], s[90:91]\n"
"v_pk_mul_f32 v[176:177], v[176:177], s[90:91]\n"
"v_pk_mul_f32 v[178:179], v[178:179], s[90:91]\n"
"v_cvt_pk_bf16_f32 v36, v172, v173\n"
"v_cvt_pk_bf16_f32 v37, v174, v175\n"
"v_cvt_pk_bf16_f32 v38, v176, v177\n"
"v_cvt_pk_bf16_f32 v39, v178, v179\n"
"s_waitcnt lgkmcnt(5)\n"
"v_mfma_f32_16x16x32_bf16 v[188:191], a[152:155], a[8:11], v[188:191]\n"
"s_waitcnt vmcnt(9)\n"
"ds_read_b128 a[164:167], v153 offset:2048\n"
"ds_write_b128 v240, v[24:27] offset:0\n"
"v_lshlrev_b32 v172, 16, v40\n"
"buffer_load_dwordx4 v[84:87], v234, s[48:51], s61 offen offset:3200\n"
"v_and_b32 v173, s78, v40\n"
"v_lshlrev_b32 v174, 16, v41\n"
"v_and_b32 v175, s78, v41\n"
"v_lshlrev_b32 v176, 16, v42\n"
"v_and_b32 v177, s78, v42\n"
"v_lshlrev_b32 v178, 16, v43\n"
"v_and_b32 v179, s78, v43\n"
"v_pk_mul_f32 v[172:173], v[172:173], s[90:91]\n"
"v_pk_mul_f32 v[174:175], v[174:175], s[90:91]\n"
"v_pk_mul_f32 v[176:177], v[176:177], s[90:91]\n"
"v_pk_mul_f32 v[178:179], v[178:179], s[90:91]\n"
"s_waitcnt lgkmcnt(5)\n"
"v_mfma_f32_16x16x32_bf16 v[188:191], a[156:159], a[12:15], v[188:191]\n"
"ds_write_b128 v240, v[28:31] offset:1024\n"
"v_cvt_pk_bf16_f32 v40, v172, v173\n"
"v_cvt_pk_bf16_f32 v41, v174, v175\n"
"v_cvt_pk_bf16_f32 v42, v176, v177\n"
"v_cvt_pk_bf16_f32 v43, v178, v179\n"
"v_lshlrev_b32 v172, 16, v44\n"
"v_and_b32 v173, s78, v44\n"
"v_lshlrev_b32 v174, 16, v45\n"
"v_and_b32 v175, s78, v45\n"
"buffer_load_dwordx4 v[88:91], v234, s[48:51], s61 offen offset:256\n"
"ds_read_b128 a[24:27], v241\n"
"ds_read_b128 a[28:31], v242\n"
"v_lshlrev_b32 v176, 16, v46\n"
"v_and_b32 v177, s78, v46\n"
"v_lshlrev_b32 v178, 16, v47\n"
"v_and_b32 v179, s78, v47\n"
"v_pk_mul_f32 v[172:173], v[172:173], s[90:91]\n"
"v_pk_mul_f32 v[174:175], v[174:175], s[90:91]\n"
"v_pk_mul_f32 v[176:177], v[176:177], s[90:91]\n"
"v_pk_mul_f32 v[178:179], v[178:179], s[90:91]\n"
"s_waitcnt lgkmcnt(6)\n"
"v_mfma_f32_16x16x32_bf16 v[188:191], a[160:163], a[16:19], v[188:191]\n"
"ds_write_b128 v240, v[32:35] offset:0\n"
"v_cvt_pk_bf16_f32 v44, v172, v173\n"
"v_cvt_pk_bf16_f32 v45, v174, v175\n"
"v_cvt_pk_bf16_f32 v46, v176, v177\n"
"v_cvt_pk_bf16_f32 v47, v178, v179\n"
"s_waitcnt lgkmcnt(5)\n"
"v_mfma_f32_16x16x32_bf16 v[188:191], a[164:167], a[20:23], v[188:191]\n"
"s_waitcnt vmcnt(9)\n"
"v_lshlrev_b32 v172, 16, v48\n"
"v_and_b32 v173, s78, v48\n"
"buffer_load_dwordx4 v[92:95], v234, s[48:51], s61 offen offset:3328\n"
"ds_write_b128 v240, v[36:39] offset:1024\n"
"v_lshlrev_b32 v174, 16, v49\n"
"v_and_b32 v175, s78, v49\n"
"v_lshlrev_b32 v176, 16, v50\n"
"v_and_b32 v177, s78, v50\n"
"v_lshlrev_b32 v178, 16, v51\n"
"v_and_b32 v179, s78, v51\n"
"v_pk_mul_f32 v[172:173], v[172:173], s[90:91]\n"
"v_pk_mul_f32 v[174:175], v[174:175], s[90:91]\n"
"v_pk_mul_f32 v[176:177], v[176:177], s[90:91]\n"
"v_pk_mul_f32 v[178:179], v[178:179], s[90:91]\n"
"v_mfma_f32_32x32x16_bf16 v[16:31], v[168:171], v[168:171], 0\n"
"s_mov_b64 s[48:49], s[18:19]\n"
"ds_read_b128 a[32:35], v241\n"
"ds_read_b128 a[36:39], v242\n"
"v_cvt_pk_bf16_f32 v48, v172, v173\n"
"v_cvt_pk_bf16_f32 v49, v174, v175\n"
"v_cvt_pk_bf16_f32 v50, v176, v177\n"
"v_cvt_pk_bf16_f32 v51, v178, v179\n"
"v_lshlrev_b32 v172, 16, v52\n"
"v_and_b32 v173, s78, v52\n"
"buffer_load_dwordx4 v234, s[40:43], s66 offen offset:0 lds\n"
"ds_write_b128 v240, v[40:43] offset:0\n"
"s_add_u32 m0, s97, 896\n"
"v_lshlrev_b32 v174, 16, v53\n"
"v_and_b32 v175, s78, v53\n"
"v_lshlrev_b32 v176, 16, v54\n"
"v_and_b32 v177, s78, v54\n"
"v_lshlrev_b32 v178, 16, v55\n"
"v_and_b32 v179, s78, v55\n"
"v_pk_mul_f32 v[172:173], v[172:173], s[90:91]\n"
"v_pk_mul_f32 v[174:175], v[174:175], s[90:91]\n"
"v_pk_mul_f32 v[176:177], v[176:177], s[90:91]\n"
"v_pk_mul_f32 v[178:179], v[178:179], s[90:91]\n"
"v_cvt_pk_bf16_f32 v52, v172, v173\n"
"v_cvt_pk_bf16_f32 v53, v174, v175\n"
"v_cvt_pk_bf16_f32 v54, v176, v177\n"
"v_cvt_pk_bf16_f32 v55, v178, v179\n"
"s_waitcnt vmcnt(9)\n"
"ds_write_b128 v240, v[44:47] offset:1024\n"
"buffer_load_dwordx4 v234, s[40:43], s66 offen offset:128 lds\n"
"v_lshlrev_b32 v172, 16, v56\n"
"s_add_u32 m0, s97, 1792\n"
"v_mfma_f32_32x32x16_bf16 v[32:47], v[168:171], v[168:171], 0\n"
"ds_read_b128 a[40:43], v241\n"
"ds_read_b128 a[44:47], v242\n"
"v_and_b32 v173, s78, v56\n"
"v_lshlrev_b32 v174, 16, v57\n"
"v_and_b32 v175, s78, v57\n"
"v_lshlrev_b32 v176, 16, v58\n"
"v_and_b32 v177, s78, v58\n"
"v_lshlrev_b32 v178, 16, v59\n"
"v_and_b32 v179, s78, v59\n"
"v_pk_mul_f32 v[172:173], v[172:173], s[90:91]\n"
"v_pk_mul_f32 v[174:175], v[174:175], s[90:91]\n"
"v_pk_mul_f32 v[176:177], v[176:177], s[90:91]\n"
"v_pk_mul_f32 v[178:179], v[178:179], s[90:91]\n"
"v_cvt_pk_bf16_f32 v56, v172, v173\n"
"v_cvt_pk_bf16_f32 v57, v174, v175\n"
"v_cvt_pk_bf16_f32 v58, v176, v177\n"
"v_cvt_pk_bf16_f32 v59, v178, v179\n"
"v_lshlrev_b32 v172, 16, v60\n"
"v_and_b32 v173, s78, v60\n"
"v_lshlrev_b32 v174, 16, v61\n"
"buffer_load_dwordx4 v234, s[40:43], s66 offen offset:256 lds\n"
"ds_write_b128 v240, v[48:51] offset:0\n"
"s_add_u32 m0, s97, 12288\n"
"v_and_b32 v175, s78, v61\n"
"v_lshlrev_b32 v176, 16, v62\n"
"v_and_b32 v177, s78, v62\n"
"v_lshlrev_b32 v178, 16, v63\n"
"v_and_b32 v179, s78, v63\n"
"v_pk_mul_f32 v[172:173], v[172:173], s[90:91]\n"
"v_pk_mul_f32 v[174:175], v[174:175], s[90:91]\n"
"v_pk_mul_f32 v[176:177], v[176:177], s[90:91]\n"
"v_pk_mul_f32 v[178:179], v[178:179], s[90:91]\n"
"v_cvt_pk_bf16_f32 v60, v172, v173\n"
"v_cvt_pk_bf16_f32 v61, v174, v175\n"
"v_cvt_pk_bf16_f32 v62, v176, v177\n"
"v_cvt_pk_bf16_f32 v63, v178, v179\n"
"s_waitcnt lgkmcnt(10)\n"
"v_mfma_f32_16x16x32_bf16 v[192:195], a[144:147], a[24:27], 0\n"
"s_waitcnt vmcnt(9)\n"
"ds_write_b128 v240, v[52:55] offset:1024\n"
"buffer_load_dwordx4 v234, s[40:43], s77 offen offset:0 lds\n"
"v_lshlrev_b32 v172, 16, v64\n"
"s_add_u32 m0, s97, 13184\n"
"v_and_b32 v173, s78, v64\n"
"v_lshlrev_b32 v174, 16, v65\n"
"v_and_b32 v175, s78, v65\n"
"v_lshlrev_b32 v176, 16, v66\n"
"v_and_b32 v177, s78, v66\n"
"v_lshlrev_b32 v178, 16, v67\n"
"v_and_b32 v179, s78, v67\n"
"v_pk_mul_f32 v[172:173], v[172:173], s[90:91]\n"
"v_pk_mul_f32 v[174:175], v[174:175], s[90:91]\n"
"v_pk_mul_f32 v[176:177], v[176:177], s[90:91]\n"
"v_pk_mul_f32 v[178:179], v[178:179], s[90:91]\n"
"s_waitcnt lgkmcnt(10)\n"
"v_mfma_f32_16x16x32_bf16 v[192:195], a[148:151], a[28:31], v[192:195]\n"
"ds_read_b128 a[48:51], v241\n"
"ds_read_b128 a[52:55], v242\n"
"v_cvt_pk_bf16_f32 v64, v172, v173\n"
"v_cvt_pk_bf16_f32 v65, v174, v175\n"
"v_cvt_pk_bf16_f32 v66, v176, v177\n"
"v_cvt_pk_bf16_f32 v67, v178, v179\n"
"v_lshlrev_b32 v172, 16, v68\n"
"v_and_b32 v173, s78, v68\n"
"v_lshlrev_b32 v174, 16, v69\n"
"v_and_b32 v175, s78, v69\n"
"buffer_load_dwordx4 v234, s[40:43], s77 offen offset:128 lds\n"
"ds_write_b128 v240, v[56:59] offset:0\n"
"s_add_u32 m0, s97, 14080\n"
"v_lshlrev_b32 v176, 16, v70\n"
"v_and_b32 v177, s78, v70\n"
"v_lshlrev_b32 v178, 16, v71\n"
"v_and_b32 v179, s78, v71\n"
"v_pk_mul_f32 v[172:173], v[172:173], s[90:91]\n"
"v_pk_mul_f32 v[174:175], v[174:175], s[90:91]\n"
"v_pk_mul_f32 v[176:177], v[176:177], s[90:91]\n"
"v_pk_mul_f32 v[178:179], v[178:179], s[90:91]\n"
"s_waitcnt lgkmcnt(10)\n"
"v_mfma_f32_16x16x32_bf16 v[192:195], a[152:155], a[32:35], v[192:195]\n"
"ds_write_b128 v240, v[60:63] offset:1024\n"
"v_cvt_pk_bf16_f32 v68, v172, v173\n"
"v_cvt_pk_bf16_f32 v69, v174, v175\n"
"v_cvt_pk_bf16_f32 v70, v176, v177\n"
"v_cvt_pk_bf16_f32 v71, v178, v179\n"
"buffer_load_dwordx4 v234, s[40:43], s77 offen offset:256 lds\n"
"ds_read_b128 a[56:59], v241\n"
"ds_read_b128 a[60:63], v242\n"
"s_add_u32 m0, s97, 24576\n"
"s_waitcnt lgkmcnt(12)\n"
"v_mfma_f32_16x16x32_bf16 v[192:195], a[156:159], a[36:39], v[192:195]\n"
"s_waitcnt vmcnt(10)\n"
"ds_write_b128 v240, v[64:67] offset:0\n"
"s_waitcnt lgkmcnt(10)\n"
"v_mfma_f32_16x16x32_bf16 v[192:195], a[160:163], a[40:43], v[192:195]\n"
"s_waitcnt lgkmcnt(9)\n"
"v_lshlrev_b32 v172, 16, v72\n"
"v_and_b32 v173, s78, v72\n"
"v_mfma_f32_16x16x32_bf16 v[192:195], a[164:167], a[44:47], v[192:195]\n"
"v_lshlrev_b32 v174, 16, v73\n"
"v_and_b32 v175, s78, v73\n"
"v_lshlrev_b32 v176, 16, v74\n"
"v_and_b32 v177, s78, v74\n"
"v_lshlrev_b32 v178, 16, v75\n"
"v_and_b32 v179, s78, v75\n"
"v_pk_mul_f32 v[172:173], v[172:173], s[90:91]\n"
"v_pk_mul_f32 v[174:175], v[174:175], s[90:91]\n"
"v_pk_mul_f32 v[176:177], v[176:177], s[90:91]\n"
"buffer_load_dwordx4 v234, s[44:47], s66 offen offset:0 lds\n"
"ds_write_b128 v240, v[68:71] offset:1024\n"
"s_add_u32 m0, s97, 36864\n"
"v_pk_mul_f32 v[178:179], v[178:179], s[90:91]\n"
"v_mfma_f32_32x32x16_bf16 v[48:63], v[168:171], v[168:171], 0\n"
"v_cvt_pk_bf16_f32 v72, v172, v173\n"
"v_cvt_pk_bf16_f32 v73, v174, v175\n"
"v_cvt_pk_bf16_f32 v74, v176, v177\n"
"v_cvt_pk_bf16_f32 v75, v178, v179\n"
"v_lshlrev_b32 v172, 16, v76\n"
"v_and_b32 v173, s78, v76\n"
"v_lshlrev_b32 v174, 16, v77\n"
"v_and_b32 v175, s78, v77\n"
"v_lshlrev_b32 v176, 16, v78\n"
"v_and_b32 v177, s78, v78\n"
"v_lshlrev_b32 v178, 16, v79\n"
"v_and_b32 v179, s78, v79\n"
"v_pk_mul_f32 v[172:173], v[172:173], s[90:91]\n"
"v_pk_mul_f32 v[174:175], v[174:175], s[90:91]\n"
"v_pk_mul_f32 v[176:177], v[176:177], s[90:91]\n"
"v_pk_mul_f32 v[178:179], v[178:179], s[90:91]\n"
"v_cvt_pk_bf16_f32 v76, v172, v173\n"
"buffer_load_dwordx4 v234, s[44:47], s77 offen offset:0 lds\n"
"ds_read_b128 a[64:67], v241\n"
"s_add_u32 m0, s97, 25472\n"
"ds_read_b128 a[68:71], v242\n"
"v_cvt_pk_bf16_f32 v77, v174, v175\n"
"v_cvt_pk_bf16_f32 v78, v176, v177\n"
"v_cvt_pk_bf16_f32 v79, v178, v179\n"
"s_waitcnt vmcnt(10)\n"
"ds_write_b128 v240, v[72:75] offset:0\n"
"s_waitcnt lgkmcnt(10)\n"
"v_mfma_f32_16x16x32_bf16 v[196:199], a[144:147], a[48:51], 0\n"
"v_lshlrev_b32 v172, 16, v80\n"
"v_and_b32 v173, s78, v80\n"
"v_lshlrev_b32 v174, 16, v81\n"
"v_and_b32 v175, s78, v81\n"
"v_lshlrev_b32 v176, 16, v82\n"
"v_and_b32 v177, s78, v82\n"
"v_lshlrev_b32 v178, 16, v83\n"
"v_and_b32 v179, s78, v83\n"
"v_pk_mul_f32 v[172:173], v[172:173], s[90:91]\n"
"v_pk_mul_f32 v[174:175], v[174:175], s[90:91]\n"
"v_pk_mul_f32 v[176:177], v[176:177], s[90:91]\n"
"buffer_load_dwordx4 v234, s[44:47], s66 offen offset:128 lds\n"
"ds_write_b128 v240, v[76:79] offset:1024\n"
"s_add_u32 m0, s97, 37760\n"
"v_pk_mul_f32 v[178:179], v[178:179], s[90:91]\n"
"v_cvt_pk_bf16_f32 v80, v172, v173\n"
"v_cvt_pk_bf16_f32 v81, v174, v175\n"
"v_cvt_pk_bf16_f32 v82, v176, v177\n"
"v_cvt_pk_bf16_f32 v83, v178, v179\n"
"s_waitcnt lgkmcnt(10)\n"
"v_mfma_f32_16x16x32_bf16 v[196:199], a[148:151], a[52:55], v[196:199]\n"
"ds_read_b128 a[72:75], v241\n"
"ds_read_b128 a[76:79], v242\n"
"v_lshlrev_b32 v172, 16, v84\n"
"v_and_b32 v173, s78, v84\n"
"v_lshlrev_b32 v174, 16, v85\n"
"v_and_b32 v175, s78, v85\n"
"v_lshlrev_b32 v176, 16, v86\n"
"v_and_b32 v177, s78, v86\n"
"v_lshlrev_b32 v178, 16, v87\n"
"v_and_b32 v179, s78, v87\n"
"v_pk_mul_f32 v[172:173], v[172:173], s[90:91]\n"
"buffer_load_dwordx4 v234, s[44:47], s77 offen offset:128 lds\n"
"ds_write_b128 v240, v[80:83] offset:0\n"
"s_add_u32 m0, s97, 26368\n"
"v_pk_mul_f32 v[174:175], v[174:175], s[90:91]\n"
"v_pk_mul_f32 v[176:177], v[176:177], s[90:91]\n"
"v_pk_mul_f32 v[178:179], v[178:179], s[90:91]\n"
"v_cvt_pk_bf16_f32 v84, v172, v173\n"
"v_cvt_pk_bf16_f32 v85, v174, v175\n"
"v_cvt_pk_bf16_f32 v86, v176, v177\n"
"v_cvt_pk_bf16_f32 v87, v178, v179\n"
"s_waitcnt lgkmcnt(10)\n"
"v_mfma_f32_16x16x32_bf16 v[196:199], a[152:155], a[56:59], v[196:199]\n"
"s_waitcnt vmcnt(10)\n"
"ds_write_b128 v240, v[84:87] offset:1024\n"
"s_waitcnt lgkmcnt(10)\n"
"v_mfma_f32_16x16x32_bf16 v[196:199], a[156:159], a[60:63], v[196:199]\n"
"s_waitcnt lgkmcnt(7)\n"
"v_max3_f32 v204, v188, v189, v190\n"
"v_max_f32 v204, v204, v191\n"
"v_mfma_f32_16x16x32_bf16 v[196:199], a[160:163], a[64:67], v[196:199]\n"
"buffer_load_dwordx4 v234, s[44:47], s66 offen offset:256 lds\n"
"ds_read_b128 a[80:83], v241\n"
"s_add_u32 m0, s97, 38656\n"
"ds_read_b128 a[84:87], v242\n"
"s_waitcnt vmcnt(8)\n"
"v_lshlrev_b32 v172, 16, v88\n"
"v_and_b32 v173, s78, v88\n"
"v_lshlrev_b32 v174, 16, v89\n"
"v_and_b32 v175, s78, v89\n"
"v_lshlrev_b32 v176, 16, v90\n"
"v_and_b32 v177, s78, v90\n"
"v_lshlrev_b32 v178, 16, v91\n"
"v_and_b32 v179, s78, v91\n"
"v_pk_mul_f32 v[172:173], v[172:173], s[90:91]\n"
"v_pk_mul_f32 v[174:175], v[174:175], s[90:91]\n"
"v_pk_mul_f32 v[176:177], v[176:177], s[90:91]\n"
"v_pk_mul_f32 v[178:179], v[178:179], s[90:91]\n"
"v_cvt_pk_bf16_f32 v88, v172, v173\n"
"v_cvt_pk_bf16_f32 v89, v174, v175\n"
"v_cvt_pk_bf16_f32 v90, v176, v177\n"
"v_cvt_pk_bf16_f32 v91, v178, v179\n"
"v_lshlrev_b32 v172, 16, v92\n"
"v_and_b32 v173, s78, v92\n"
"v_lshlrev_b32 v174, 16, v93\n"
"v_and_b32 v175, s78, v93\n"
"v_lshlrev_b32 v176, 16, v94\n"
"v_and_b32 v177, s78, v94\n"
"buffer_load_dwordx4 v234, s[44:47], s77 offen offset:256 lds\n"
"ds_write_b128 v240, v[88:91] offset:0\n"
"v_lshlrev_b32 v178, 16, v95\n"
"v_and_b32 v179, s78, v95\n"
"v_pk_mul_f32 v[172:173], v[172:173], s[90:91]\n"
"v_pk_mul_f32 v[174:175], v[174:175], s[90:91]\n"
"v_pk_mul_f32 v[176:177], v[176:177], s[90:91]\n"
"v_pk_mul_f32 v[178:179], v[178:179], s[90:91]\n"
"v_cvt_pk_bf16_f32 v92, v172, v173\n"
"v_cvt_pk_bf16_f32 v93, v174, v175\n"
"v_cvt_pk_bf16_f32 v94, v176, v177\n"
"v_cvt_pk_bf16_f32 v95, v178, v179\n"
"s_waitcnt lgkmcnt(9)\n"
"v_mfma_f32_16x16x32_bf16 v[196:199], a[164:167], a[68:71], v[196:199]\n"
"s_barrier\n"
"ds_write_b128 v240, v[92:95] offset:1024\n"
"v_mfma_f32_32x32x16_bf16 v[64:79], v[168:171], v[168:171], 0\n"
"v_mov_b32 v205, v204\n"
"v_lshlrev_b32 v251, 16, v251\n"
"v_mul_f32 v251, 0xbfb8aa3b, v251\n"
"v_permlane32_swap_b32 v204, v205\n"
"v_max_f32 v204, v204, v205\n"
"v_mov_b32 v205, v204\n"
"v_mfma_f32_32x32x16_bf16 v[80:95], v[168:171], v[168:171], 0\n"
"ds_read_b128 a[88:91], v241\n"
"ds_read_b128 a[92:95], v242\n"
"v_permlane16_swap_b32 v204, v205\n"
"v_max_f32 v204, v204, v205\n"
"v_sub_f32 v188, -2.0, v204\n"
"s_waitcnt lgkmcnt(9)\n"
"v_mfma_f32_16x16x32_bf16 v[200:203], a[144:147], a[72:75], 0\n"
"s_waitcnt lgkmcnt(8)\n"
"ds_read_b128 a[96:99], v228 offset:0\n"
"v_sub_f32 v189, -2.0, v204\n"
"v_mfma_f32_16x16x32_bf16 v[200:203], a[148:151], a[76:79], v[200:203]\n"
"s_waitcnt lgkmcnt(6)\n"
"ds_read_b128 a[100:103], v229 offset:0\n"
"v_sub_f32 v190, -2.0, v204\n"
"v_mfma_f32_16x16x32_bf16 v[200:203], a[152:155], a[80:83], v[200:203]\n"
"s_waitcnt lgkmcnt(6)\n"
"ds_read_b128 a[104:107], v228 offset:1024\n"
"v_sub_f32 v191, -2.0, v204\n"
"v_mfma_f32_16x16x32_bf16 v[200:203], a[156:159], a[84:87], v[200:203]\n"
"ds_read_b128 a[108:111], v229 offset:1024\n"
"ds_read_b128 a[112:115], v228 offset:2048\n"
"v_max3_f32 v204, v192, v193, v194\n"
"v_max_f32 v204, v204, v195\n"
"v_mov_b32 v205, v204\n"
"v_exp_f32 v251, v251\n"
"v_readfirstlane_b32 s88, v252\n"
"v_permlane32_swap_b32 v204, v205\n"
"v_max_f32 v204, v204, v205\n"
"v_mov_b32 v205, v204\n"
"v_add_f32 v253, 1.0, v251\n"
"v_rcp_f32 v253, v253\n"
"s_waitcnt lgkmcnt(6)\n"
"v_mfma_f32_16x16x32_bf16 v[200:203], a[160:163], a[88:91], v[200:203]\n"
"s_waitcnt lgkmcnt(5)\n"
"ds_read_b128 a[116:119], v229 offset:2048\n"
"v_permlane16_swap_b32 v204, v205\n"
"v_max_f32 v204, v204, v205\n"
"v_mfma_f32_16x16x32_bf16 v[200:203], a[164:167], a[92:95], v[200:203]\n"
"ds_read_b128 a[120:123], v228 offset:6144\n"
"ds_read_b128 a[124:127], v229 offset:6144\n"
"v_sub_f32 v192, -2.0, v204\n"
"v_sub_f32 v193, -2.0, v204\n"
"v_sub_f32 v194, -2.0, v204\n"
"v_sub_f32 v195, -2.0, v204\n"
"v_max3_f32 v204, v196, v197, v198\n"
"v_max_f32 v204, v204, v199\n"
"v_mov_b32 v205, v204\n"
"v_sub_f32 v251, 1.0, v253\n"
"v_mul_u32_u24 v248, 384, v248\n"
"v_permlane32_swap_b32 v204, v205\n"
"v_max_f32 v204, v204, v205\n"
"v_mov_b32 v205, v204\n"
"v_mul_u32_u24 v249, 384, v249\n"
"v_readfirstlane_b32 s80, v253\n"
"v_permlane16_swap_b32 v204, v205\n"
"v_max_f32 v204, v204, v205\n"
"v_sub_f32 v196, -2.0, v204\n"
"v_sub_f32 v197, -2.0, v204\n"
"v_sub_f32 v198, -2.0, v204\n"
"v_sub_f32 v199, -2.0, v204\n"
"v_max3_f32 v204, v200, v201, v202\n"
"v_max_f32 v204, v204, v203\n"
"v_mov_b32 v205, v204\n"
"v_readfirstlane_b32 s81, v251\n"
"ds_read_b128 a[128:131], v228 offset:7168\n"
"ds_read_b128 a[132:135], v229 offset:7168\n"
"v_permlane32_swap_b32 v204, v205\n"
"v_max_f32 v204, v204, v205\n"
"v_mov_b32 v205, v204\n"
"ds_read_b128 a[136:139], v228 offset:8192\n"
"ds_read_b128 a[140:143], v229 offset:8192\n"
"v_permlane16_swap_b32 v204, v205\n"
"v_max_f32 v204, v204, v205\n"
"v_sub_f32 v200, -2.0, v204\n"
"v_sub_f32 v201, -2.0, v204\n"
"v_sub_f32 v202, -2.0, v204\n"
"v_sub_f32 v203, -2.0, v204\n"
"v_readlane_b32 s61, v235, s60\n"
"s_add_u32 s60, s60, 1\n"
"s_and_b32 s98, s61, 0x3ffff\n"
"s_add_u32 s62, s64, s98\n"
"s_addc_u32 s63, s65, 0\n"
"s_lshr_b32 s67, s61, 18\n"
"s_mul_i32 s66, s67, 24576\n"
"s_waitcnt vmcnt(6) lgkmcnt(0)\n"
"s_barrier\n"
"s_setpc_b64 s[62:63]\n"
".p2align 12\n"
".p2align 5\n"
"L_W0G:\n"
"s_nop 7\n"
"s_branch L_W0SW\n"
"L_W1G:\n"
"s_nop 7\n"
"s_branch L_W1P1\n"
"L_W2G:\n"
"s_nop 7\n"
"s_branch L_W2D1\n"
"L_G:\n"
"v_mfma_f32_16x16x32_bf16 v[144:147], a[96:99], a[0:3], v[188:191]\n"
"v_readlane_b32 s61, v235, s60\n"
"s_add_u32 s60, s60, 1\n"
"v_mfma_f32_16x16x32_bf16 v[144:147], a[100:103], a[4:7], v[144:147]\n"
"s_and_b32 s98, s61, 0x3ffff\n"
"s_add_u32 s62, s64, s98\n"
"s_addc_u32 s63, s65, 0\n"
"v_mfma_f32_16x16x32_bf16 v[144:147], a[104:107], a[8:11], v[144:147]\n"
"s_add_u32 m0, s97, 0\n"
"ds_read_b128 a[144:147], v228 offset:12288\n"
"s_add_u32 s77, s66, 12288\n"
"v_mfma_f32_16x16x32_bf16 v[144:147], a[108:111], a[12:15], v[144:147]\n"
"buffer_load_dwordx4 v234, s[40:43], s66 offen offset:0 lds\n"
"v_mfma_f32_16x16x32_bf16 v[144:147], a[112:115], a[16:19], v[144:147]\n"
"s_add_u32 m0, s97, 896\n"
"ds_read_b128 a[148:151], v229 offset:12288\n"
"s_lshr_b32 s67, s61, 18\n"
"v_mfma_f32_16x16x32_bf16 v[144:147], a[116:119], a[20:23], v[144:147]\n"
"ds_read_b128 a[152:155], v228 offset:13312\n"
"v_mfma_f32_16x16x32_bf16 v[176:179], a[120:123], a[0:3], v[188:191]\n"
"ds_read_b128 a[156:159], v229 offset:13312\n"
"v_mfma_f32_16x16x32_bf16 v[176:179], a[124:127], a[4:7], v[176:179]\n"
"ds_read_b128 a[160:163], v228 offset:14336\n"
"v_mfma_f32_16x16x32_bf16 v[176:179], a[128:131], a[8:11], v[176:179]\n"
"ds_read_b128 a[164:167], v229 offset:14336\n"
"v_mfma_f32_16x16x32_bf16 v[176:179], a[132:135], a[12:15], v[176:179]\n"
"v_exp_f32 v144, v144\n"
"v_mfma_f32_16x16x32_bf16 v[176:179], a[136:139], a[16:19], v[176:179]\n"
"v_exp_f32 v145, v145\n"
"v_mfma_f32_16x16x32_bf16 v[176:179], a[140:143], a[20:23], v[176:179]\n"
"v_exp_f32 v146, v146\n"
"v_mfma_f32_16x16x32_bf16 v[152:155], a[96:99], a[24:27], v[192:195]\n"
"buffer_load_dwordx4 v234, s[40:43], s66 offen offset:128 lds\n"
"v_mfma_f32_16x16x32_bf16 v[152:155], a[100:103], a[28:31], v[152:155]\n"
"s_add_u32 m0, s97, 1792\n"
"v_exp_f32 v147, v147\n"
"v_mfma_f32_16x16x32_bf16 v[152:155], a[104:107], a[32:35], v[152:155]\n"
"v_cvt_pk_bf16_f32 v144, v144, v145\n"
"v_cvt_pk_bf16_f32 v145, v146, v147\n"
"v_mfma_f32_16x16x32_bf16 v[152:155], a[108:111], a[36:39], v[152:155]\n"
"v_exp_f32 v176, v176\n"
"v_mfma_f32_16x16x32_bf16 v[152:155], a[112:115], a[40:43], v[152:155]\n"
"v_exp_f32 v177, v177\n"
"v_mfma_f32_16x16x32_bf16 v[152:155], a[116:119], a[44:47], v[152:155]\n"
"v_exp_f32 v178, v178\n"
"v_mfma_f32_16x16x32_bf16 v[180:183], a[120:123], a[24:27], v[192:195]\n"
"v_exp_f32 v179, v179\n"
"v_mfma_f32_16x16x32_bf16 v[180:183], a[124:127], a[28:31], v[180:183]\n"
"v_cvt_pk_bf16_f32 v146, v176, v177\n"
"v_cvt_pk_bf16_f32 v147, v178, v179\n"
"v_mfma_f32_16x16x32_bf16 v[180:183], a[128:131], a[32:35], v[180:183]\n"
"v_mfma_f32_16x16x32_bf16 v[180:183], a[132:135], a[36:39], v[180:183]\n"
"v_exp_f32 v152, v152\n"
"v_mfma_f32_16x16x32_bf16 v[180:183], a[136:139], a[40:43], v[180:183]\n"
"buffer_load_dwordx4 v234, s[40:43], s66 offen offset:256 lds\n"
"v_mfma_f32_16x16x32_bf16 v[180:183], a[140:143], a[44:47], v[180:183]\n"
"s_add_u32 m0, s97, 12288\n"
"v_exp_f32 v153, v153\n"
"v_mfma_f32_16x16x32_bf16 v[160:163], a[96:99], a[48:51], v[196:199]\n"
"v_exp_f32 v154, v154\n"
"v_mfma_f32_16x16x32_bf16 v[160:163], a[100:103], a[52:55], v[160:163]\n"
"v_exp_f32 v155, v155\n"
"v_mfma_f32_16x16x32_bf16 v[160:163], a[104:107], a[56:59], v[160:163]\n"
"v_cvt_pk_bf16_f32 v152, v152, v153\n"
"v_cvt_pk_bf16_f32 v153, v154, v155\n"
"v_mfma_f32_16x16x32_bf16 v[160:163], a[108:111], a[60:63], v[160:163]\n"
"v_exp_f32 v180, v180\n"
"v_mfma_f32_16x16x32_bf16 v[160:163], a[112:115], a[64:67], v[160:163]\n"
"v_exp_f32 v181, v181\n"
"v_mfma_f32_16x16x32_bf16 v[160:163], a[116:119], a[68:71], v[160:163]\n"
"v_exp_f32 v182, v182\n"
"v_mfma_f32_16x16x32_bf16 v[184:187], a[120:123], a[48:51], v[196:199]\n"
"v_exp_f32 v183, v183\n"
"v_mfma_f32_16x16x32_bf16 v[184:187], a[124:127], a[52:55], v[184:187]\n"
"v_cvt_pk_bf16_f32 v154, v180, v181\n"
"v_cvt_pk_bf16_f32 v155, v182, v183\n"
"v_mfma_f32_16x16x32_bf16 v[184:187], a[128:131], a[56:59], v[184:187]\n"
"v_mfma_f32_16x16x32_bf16 v[184:187], a[132:135], a[60:63], v[184:187]\n"
"v_exp_f32 v160, v160\n"
"v_mfma_f32_16x16x32_bf16 v[184:187], a[136:139], a[64:67], v[184:187]\n"
"v_exp_f32 v161, v161\n"
"v_mfma_f32_16x16x32_bf16 v[184:187], a[140:143], a[68:71], v[184:187]\n"
"v_exp_f32 v162, v162\n"
"v_mfma_f32_16x16x32_bf16 v[168:171], a[96:99], a[72:75], v[200:203]\n"
"v_exp_f32 v163, v163\n"
"v_mfma_f32_16x16x32_bf16 v[168:171], a[100:103], a[76:79], v[168:171]\n"
"ds_read_b128 a[96:99], v228 offset:18432\n"
"v_cvt_pk_bf16_f32 v160, v160, v161\n"
"v_mfma_f32_16x16x32_bf16 v[168:171], a[104:107], a[80:83], v[168:171]\n"
"ds_read_b128 a[100:103], v229 offset:18432\n"
"v_cvt_pk_bf16_f32 v161, v162, v163\n"
"v_mfma_f32_16x16x32_bf16 v[168:171], a[108:111], a[84:87], v[168:171]\n"
"v_exp_f32 v184, v184\n"
"v_mfma_f32_16x16x32_bf16 v[168:171], a[112:115], a[88:91], v[168:171]\n"
"v_exp_f32 v185, v185\n"
"v_mfma_f32_16x16x32_bf16 v[168:171], a[116:119], a[92:95], v[168:171]\n"
"ds_read_b128 a[104:107], v228 offset:19456\n"
"v_cvt_pk_bf16_f32 v162, v184, v185\n"
"v_mfma_f32_16x16x32_bf16 v[176:179], a[120:123], a[72:75], v[200:203]\n"
"ds_read_b128 a[108:111], v229 offset:19456\n"
"v_mfma_f32_16x16x32_bf16 v[176:179], a[124:127], a[76:79], v[176:179]\n"
"ds_read_b128 a[112:115], v228 offset:20480\n"
"v_mfma_f32_16x16x32_bf16 v[176:179], a[128:131], a[80:83], v[176:179]\n"
"ds_read_b128 a[116:119], v229 offset:20480\n"
"v_mfma_f32_16x16x32_bf16 v[176:179], a[132:135], a[84:87], v[176:179]\n"
"v_exp_f32 v186, v186\n"
"v_mfma_f32_16x16x32_bf16 v[176:179], a[136:139], a[88:91], v[176:179]\n"
"s_waitcnt lgkmcnt(6)\n"
"v_exp_f32 v187, v187\n"
"v_mfma_f32_16x16x32_bf16 v[176:179], a[140:143], a[92:95], v[176:179]\n"
"v_exp_f32 v168, v168\n"
"v_mfma_f32_16x16x32_bf16 v[148:151], a[144:147], a[0:3], v[188:191]\n"
"v_exp_f32 v169, v169\n"
"v_mfma_f32_16x16x32_bf16 v[148:151], a[148:151], a[4:7], v[148:151]\n"
"v_exp_f32 v170, v170\n"
"v_mfma_f32_16x16x32_bf16 v[148:151], a[152:155], a[8:11], v[148:151]\n"
"v_exp_f32 v171, v171\n"
"v_mfma_f32_16x16x32_bf16 v[148:151], a[156:159], a[12:15], v[148:151]\n"
"v_exp_f32 v176, v176\n"
"v_mfma_f32_16x16x32_bf16 v[148:151], a[160:163], a[16:19], v[148:151]\n"
"s_waitcnt vmcnt(5) lgkmcnt(0)\n"
"s_barrier\n"
"ds_read_b64_tr_b16 a[120:121], v230 offset:24576\n"
"v_mfma_f32_16x16x32_bf16 v[148:151], a[164:167], a[20:23], v[148:151]\n"
"v_exp_f32 v177, v177\n"
"v_mfma_f32_16x16x32_bf16 v[180:183], a[96:99], a[0:3], v[188:191]\n"
"v_exp_f32 v178, v178\n"
"v_mfma_f32_16x16x32_bf16 v[180:183], a[100:103], a[4:7], v[180:183]\n"
"v_exp_f32 v179, v179\n"
"v_mfma_f32_16x16x32_bf16 v[180:183], a[104:107], a[8:11], v[180:183]\n"
"ds_read_b64_tr_b16 a[122:123], v230 offset:30720\n"
"v_cvt_pk_bf16_f32 v163, v186, v187\n"
"v_mfma_f32_16x16x32_bf16 v[180:183], a[108:111], a[12:15], v[180:183]\n"
"ds_read_b64_tr_b16 a[124:125], v230 offset:36864\n"
"v_cvt_pk_bf16_f32 v168, v168, v169\n"
"v_mfma_f32_16x16x32_bf16 v[180:183], a[112:115], a[16:19], v[180:183]\n"
"buffer_load_dwordx4 v234, s[40:43], s77 offen offset:0 lds\n"
"v_mfma_f32_16x16x32_bf16 v[180:183], a[116:119], a[20:23], v[180:183]\n"
"s_add_u32 m0, s97, 13184\n"
"ds_read_b64_tr_b16 a[126:127], v230 offset:43008\n"
"v_cvt_pk_bf16_f32 v169, v170, v171\n"
"v_mfma_f32_16x16x32_bf16 v[156:159], a[144:147], a[24:27], v[192:195]\n"
"ds_read_b64_tr_b16 a[128:129], v231 offset:24576\n"
"v_cvt_pk_bf16_f32 v170, v176, v177\n"
"v_mfma_f32_16x16x32_bf16 v[156:159], a[148:151], a[28:31], v[156:159]\n"
"ds_read_b64_tr_b16 a[130:131], v231 offset:30720\n"
"v_cvt_pk_bf16_f32 v171, v178, v179\n"
"v_mfma_f32_16x16x32_bf16 v[156:159], a[152:155], a[32:35], v[156:159]\n"
"v_exp_f32 v148, v148\n"
"v_mfma_f32_16x16x32_bf16 v[156:159], a[156:159], a[36:39], v[156:159]\n"
"ds_read_b64_tr_b16 a[132:133], v231 offset:36864\n"
"v_mfma_f32_16x16x32_bf16 v[156:159], a[160:163], a[40:43], v[156:159]\n"
"ds_read_b64_tr_b16 a[134:135], v231 offset:43008\n"
"v_mfma_f32_16x16x32_bf16 v[156:159], a[164:167], a[44:47], v[156:159]\n"
"v_exp_f32 v149, v149\n"
"v_mfma_f32_16x16x32_bf16 v[184:187], a[96:99], a[24:27], v[192:195]\n"
"v_exp_f32 v150, v150\n"
"v_mfma_f32_16x16x32_bf16 v[184:187], a[100:103], a[28:31], v[184:187]\n"
"v_exp_f32 v151, v151\n"
"v_mfma_f32_16x16x32_bf16 v[184:187], a[104:107], a[32:35], v[184:187]\n"
"buffer_load_dwordx4 v234, s[40:43], s77 offen offset:128 lds\n"
"v_mfma_f32_16x16x32_bf16 v[184:187], a[108:111], a[36:39], v[184:187]\n"
"s_add_u32 m0, s97, 14080\n"
"ds_read_b64_tr_b16 a[136:137], v232 offset:24576\n"
"v_cvt_pk_bf16_f32 v148, v148, v149\n"
"v_mfma_f32_16x16x32_bf16 v[184:187], a[112:115], a[40:43], v[184:187]\n"
"ds_read_b64_tr_b16 a[138:139], v232 offset:30720\n"
"v_cvt_pk_bf16_f32 v149, v150, v151\n"
"v_mfma_f32_16x16x32_bf16 v[184:187], a[116:119], a[44:47], v[184:187]\n"
"v_exp_f32 v180, v180\n"
"v_mfma_f32_16x16x32_bf16 v[164:167], a[144:147], a[48:51], v[196:199]\n"
"v_exp_f32 v181, v181\n"
"v_mfma_f32_16x16x32_bf16 v[164:167], a[148:151], a[52:55], v[164:167]\n"
"v_exp_f32 v182, v182\n"
"v_mfma_f32_16x16x32_bf16 v[164:167], a[152:155], a[56:59], v[164:167]\n"
"v_exp_f32 v183, v183\n"
"v_mfma_f32_16x16x32_bf16 v[164:167], a[156:159], a[60:63], v[164:167]\n"
"ds_read_b64_tr_b16 a[140:141], v232 offset:36864\n"
"v_cvt_pk_bf16_f32 v150, v180, v181\n"
"v_mfma_f32_16x16x32_bf16 v[164:167], a[160:163], a[64:67], v[164:167]\n"
"ds_read_b64_tr_b16 a[142:143], v232 offset:43008\n"
"v_cvt_pk_bf16_f32 v151, v182, v183\n"
"v_mfma_f32_16x16x32_bf16 v[164:167], a[164:167], a[68:71], v[164:167]\n"
"v_exp_f32 v156, v156\n"
"v_mfma_f32_16x16x32_bf16 v[176:179], a[96:99], a[48:51], v[196:199]\n"
"buffer_load_dwordx4 v234, s[40:43], s77 offen offset:256 lds\n"
"v_mfma_f32_16x16x32_bf16 v[176:179], a[100:103], a[52:55], v[176:179]\n"
"s_add_u32 m0, s97, 24576\n"
"v_exp_f32 v157, v157\n"
"v_mfma_f32_16x16x32_bf16 v[176:179], a[104:107], a[56:59], v[176:179]\n"
"v_exp_f32 v158, v158\n"
"v_mfma_f32_16x16x32_bf16 v[176:179], a[108:111], a[60:63], v[176:179]\n"
"v_exp_f32 v159, v159\n"
"v_mfma_f32_16x16x32_bf16 v[176:179], a[112:115], a[64:67], v[176:179]\n"
"v_exp_f32 v184, v184\n"
"v_mfma_f32_16x16x32_bf16 v[176:179], a[116:119], a[68:71], v[176:179]\n"
"v_exp_f32 v185, v185\n"
"v_mfma_f32_16x16x32_bf16 v[172:175], a[144:147], a[72:75], v[200:203]\n"
"v_exp_f32 v186, v186\n"
"v_mfma_f32_16x16x32_bf16 v[172:175], a[148:151], a[76:79], v[172:175]\n"
"v_exp_f32 v187, v187\n"
"v_mfma_f32_16x16x32_bf16 v[172:175], a[152:155], a[80:83], v[172:175]\n"
"v_exp_f32 v164, v164\n"
"v_mfma_f32_16x16x32_bf16 v[172:175], a[156:159], a[84:87], v[172:175]\n"
"v_exp_f32 v165, v165\n"
"v_mfma_f32_16x16x32_bf16 v[172:175], a[160:163], a[88:91], v[172:175]\n"
"v_exp_f32 v166, v166\n"
"v_mfma_f32_16x16x32_bf16 v[172:175], a[164:167], a[92:95], v[172:175]\n"
"v_exp_f32 v167, v167\n"
"v_mfma_f32_16x16x32_bf16 v[180:183], a[96:99], a[72:75], v[200:203]\n"
"ds_read_b64_tr_b16 a[144:145], v233 offset:24576\n"
"v_cvt_pk_bf16_f32 v156, v156, v157\n"
"v_mfma_f32_16x16x32_bf16 v[180:183], a[100:103], a[76:79], v[180:183]\n"
"ds_read_b64_tr_b16 a[146:147], v233 offset:30720\n"
"v_cvt_pk_bf16_f32 v157, v158, v159\n"
"v_mfma_f32_16x16x32_bf16 v[180:183], a[104:107], a[80:83], v[180:183]\n"
"ds_read_b64_tr_b16 a[148:149], v233 offset:36864\n"
"v_cvt_pk_bf16_f32 v158, v184, v185\n"
"v_mfma_f32_16x16x32_bf16 v[180:183], a[108:111], a[84:87], v[180:183]\n"
"ds_read_b64_tr_b16 a[150:151], v233 offset:43008\n"
"v_cvt_pk_bf16_f32 v159, v186, v187\n"
"v_mfma_f32_16x16x32_bf16 v[180:183], a[112:115], a[88:91], v[180:183]\n"
"s_waitcnt lgkmcnt(4)\n"
"v_exp_f32 v176, v176\n"
"v_mfma_f32_16x16x32_bf16 v[180:183], a[116:119], a[92:95], v[180:183]\n"
"v_exp_f32 v177, v177\n"
"v_mfma_f32_16x16x32_bf16 v[0:3], a[120:123], v[144:147], v[0:3]\n"
"v_exp_f32 v178, v178\n"
"v_mfma_f32_16x16x32_bf16 v[0:3], a[124:127], v[148:151], v[0:3]\n"
"v_exp_f32 v179, v179\n"
"v_mfma_f32_16x16x32_bf16 v[4:7], a[132:135], v[148:151], v[4:7]\n"
"ds_read_b64_tr_b16 a[152:153], v230 offset:25600\n"
"v_cvt_pk_bf16_f32 v164, v164, v165\n"
"v_mfma_f32_16x16x32_bf16 v[4:7], a[128:131], v[144:147], v[4:7]\n"
"ds_read_b64_tr_b16 a[154:155], v230 offset:31744\n"
"v_cvt_pk_bf16_f32 v165, v166, v167\n"
"v_mfma_f32_16x16x32_bf16 v[8:11], a[136:139], v[144:147], v[8:11]\n"
"ds_read_b64_tr_b16 a[156:157], v230 offset:37888\n"
"v_cvt_pk_bf16_f32 v166, v176, v177\n"
"v_mfma_f32_16x16x32_bf16 v[8:11], a[140:143], v[148:151], v[8:11]\n"
"ds_read_b64_tr_b16 a[158:159], v230 offset:44032\n"
"v_cvt_pk_bf16_f32 v167, v178, v179\n"
"v_smfmac_f32_16x16x64_bf16 a[216:219], a[224:227], v[144:151], v237\n"
"v_exp_f32 v172, v172\n"
"v_mfma_f32_16x16x32_bf16 v[56:59], a[140:143], v[156:159], v[56:59]\n"
"v_exp_f32 v173, v173\n"
"v_mfma_f32_16x16x32_bf16 v[56:59], a[136:139], v[152:155], v[56:59]\n"
"v_exp_f32 v174, v174\n"
"v_mfma_f32_16x16x32_bf16 v[52:55], a[128:131], v[152:155], v[52:55]\n"
"v_exp_f32 v175, v175\n"
"v_mfma_f32_16x16x32_bf16 v[52:55], a[132:135], v[156:159], v[52:55]\n"
"ds_read_b64_tr_b16 a[160:161], v231 offset:25600\n"
"v_cvt_pk_bf16_f32 v172, v172, v173\n"
"v_mfma_f32_16x16x32_bf16 v[48:51], a[124:127], v[156:159], v[48:51]\n"
"ds_read_b64_tr_b16 a[162:163], v231 offset:31744\n"
"v_cvt_pk_bf16_f32 v173, v174, v175\n"
"v_mfma_f32_16x16x32_bf16 v[48:51], a[120:123], v[152:155], v[48:51]\n"
"v_exp_f32 v180, v180\n"
"v_smfmac_f32_16x16x64_bf16 a[216:219], a[228:231], v[152:159], v237\n"
"v_exp_f32 v181, v181\n"
"v_mfma_f32_16x16x32_bf16 v[96:99], a[120:123], v[160:163], v[96:99]\n"
"v_exp_f32 v182, v182\n"
"v_mfma_f32_16x16x32_bf16 v[96:99], a[124:127], v[164:167], v[96:99]\n"
"v_exp_f32 v183, v183\n"
"v_mfma_f32_16x16x32_bf16 v[100:103], a[132:135], v[164:167], v[100:103]\n"
"ds_read_b64_tr_b16 a[164:165], v231 offset:37888\n"
"v_cvt_pk_bf16_f32 v174, v180, v181\n"
"v_mfma_f32_16x16x32_bf16 v[100:103], a[128:131], v[160:163], v[100:103]\n"
"ds_read_b64_tr_b16 a[166:167], v231 offset:44032\n"
"v_cvt_pk_bf16_f32 v175, v182, v183\n"
"v_mfma_f32_16x16x32_bf16 v[104:107], a[136:139], v[160:163], v[104:107]\n"
"ds_read_b64_tr_b16 a[96:97], v232 offset:25600\n"
"v_mfma_f32_16x16x32_bf16 v[104:107], a[140:143], v[164:167], v[104:107]\n"
"ds_read_b64_tr_b16 a[98:99], v232 offset:31744\n"
"v_smfmac_f32_16x16x64_bf16 a[220:223], a[224:227], v[160:167], v237\n"
"ds_read_b64_tr_b16 a[100:101], v232 offset:37888\n"
"v_mfma_f32_16x16x32_bf16 a[176:179], a[140:143], v[172:175], a[176:179]\n"
"ds_read_b64_tr_b16 a[102:103], v232 offset:44032\n"
"v_mfma_f32_16x16x32_bf16 a[176:179], a[136:139], v[168:171], a[176:179]\n"
"ds_read_b64_tr_b16 a[104:105], v233 offset:25600\n"
"v_mfma_f32_16x16x32_bf16 a[172:175], a[128:131], v[168:171], a[172:175]\n"
"ds_read_b64_tr_b16 a[106:107], v233 offset:31744\n"
"v_mfma_f32_16x16x32_bf16 a[172:175], a[132:135], v[172:175], a[172:175]\n"
"ds_read_b64_tr_b16 a[108:109], v233 offset:37888\n"
"v_mfma_f32_16x16x32_bf16 a[168:171], a[124:127], v[172:175], a[168:171]\n"
"ds_read_b64_tr_b16 a[110:111], v233 offset:44032\n"
"v_mfma_f32_16x16x32_bf16 a[168:171], a[120:123], v[168:171], a[168:171]\n"
"s_waitcnt lgkmcnt(8)\n"
"v_smfmac_f32_16x16x64_bf16 a[220:223], a[228:231], v[168:175], v237\n"
"v_mfma_f32_16x16x32_bf16 v[12:15], a[144:147], v[144:147], v[12:15]\n"
"v_mfma_f32_16x16x32_bf16 v[12:15], a[148:151], v[148:151], v[12:15]\n"
"v_mfma_f32_16x16x32_bf16 v[16:19], a[156:159], v[148:151], v[16:19]\n"
"v_mfma_f32_16x16x32_bf16 v[16:19], a[152:155], v[144:147], v[16:19]\n"
"v_mfma_f32_16x16x32_bf16 v[20:23], a[160:163], v[144:147], v[20:23]\n"
"v_mfma_f32_16x16x32_bf16 v[20:23], a[164:167], v[148:151], v[20:23]\n"
"v_mfma_f32_16x16x32_bf16 v[68:71], a[164:167], v[156:159], v[68:71]\n"
"v_mfma_f32_16x16x32_bf16 v[68:71], a[160:163], v[152:155], v[68:71]\n"
"v_mfma_f32_16x16x32_bf16 v[64:67], a[152:155], v[152:155], v[64:67]\n"
"v_mfma_f32_16x16x32_bf16 v[64:67], a[156:159], v[156:159], v[64:67]\n"
"v_mfma_f32_16x16x32_bf16 v[60:63], a[148:151], v[156:159], v[60:63]\n"
"v_mfma_f32_16x16x32_bf16 v[60:63], a[144:147], v[152:155], v[60:63]\n"
"v_mfma_f32_16x16x32_bf16 v[108:111], a[144:147], v[160:163], v[108:111]\n"
"v_mfma_f32_16x16x32_bf16 v[108:111], a[148:151], v[164:167], v[108:111]\n"
"v_mfma_f32_16x16x32_bf16 v[112:115], a[156:159], v[164:167], v[112:115]\n"
"v_mfma_f32_16x16x32_bf16 v[112:115], a[152:155], v[160:163], v[112:115]\n"
"v_mfma_f32_16x16x32_bf16 v[116:119], a[160:163], v[160:163], v[116:119]\n"
"s_waitcnt vmcnt(3) lgkmcnt(0)\n"
"s_barrier\n"
"ds_read_b64_tr_b16 a[112:113], v230 offset:26624\n"
"v_mfma_f32_16x16x32_bf16 v[116:119], a[164:167], v[164:167], v[116:119]\n"
"ds_read_b64_tr_b16 a[114:115], v230 offset:32768\n"
"v_mfma_f32_16x16x32_bf16 a[188:191], a[164:167], v[172:175], a[188:191]\n"
"ds_read_b64_tr_b16 a[116:117], v230 offset:38912\n"
"v_mfma_f32_16x16x32_bf16 a[188:191], a[160:163], v[168:171], a[188:191]\n"
"ds_read_b64_tr_b16 a[118:119], v230 offset:45056\n"
"v_mfma_f32_16x16x32_bf16 a[184:187], a[152:155], v[168:171], a[184:187]\n"
"buffer_load_dwordx4 v234, s[44:47], s66 offen offset:0 lds\n"
"v_mfma_f32_16x16x32_bf16 a[184:187], a[156:159], v[172:175], a[184:187]\n"
"ds_read_b64_tr_b16 a[164:165], v233 offset:38912\n"
"s_add_u32 m0, s97, 36864\n"
"v_mfma_f32_16x16x32_bf16 a[180:183], a[148:151], v[172:175], a[180:183]\n"
"ds_read_b64_tr_b16 a[166:167], v233 offset:45056\n"
"v_mfma_f32_16x16x32_bf16 a[180:183], a[144:147], v[168:171], a[180:183]\n"
"ds_read_b64_tr_b16 a[160:161], v233 offset:26624\n"
"v_mfma_f32_16x16x32_bf16 v[24:27], a[96:99], v[144:147], v[24:27]\n"
"ds_read_b64_tr_b16 a[162:163], v233 offset:32768\n"
"v_mfma_f32_16x16x32_bf16 v[24:27], a[100:103], v[148:151], v[24:27]\n"
"ds_read_b64_tr_b16 a[152:153], v232 offset:26624\n"
"v_mfma_f32_16x16x32_bf16 v[28:31], a[108:111], v[148:151], v[28:31]\n"
"ds_read_b64_tr_b16 a[154:155], v232 offset:32768\n"
"v_mfma_f32_16x16x32_bf16 v[28:31], a[104:107], v[144:147], v[28:31]\n"
"s_waitcnt lgkmcnt(6)\n"
"ds_read_b64_tr_b16 a[156:157], v232 offset:38912\n"
"v_mfma_f32_16x16x32_bf16 v[32:35], a[112:115], v[144:147], v[32:35]\n"
"buffer_load_dwordx4 v234, s[44:47], s77 offen offset:0 lds\n"
"v_mfma_f32_16x16x32_bf16 v[32:35], a[116:119], v[148:151], v[32:35]\n"
"ds_read_b64_tr_b16 a[158:159], v232 offset:45056\n"
"s_add_u32 m0, s97, 25472\n"
"v_mfma_f32_16x16x32_bf16 v[80:83], a[116:119], v[156:159], v[80:83]\n"
"ds_read_b64_tr_b16 a[148:149], v231 offset:38912\n"
"v_mfma_f32_16x16x32_bf16 v[80:83], a[112:115], v[152:155], v[80:83]\n"
"ds_read_b64_tr_b16 a[150:151], v231 offset:45056\n"
"v_mfma_f32_16x16x32_bf16 v[76:79], a[104:107], v[152:155], v[76:79]\n"
"ds_read_b64_tr_b16 a[144:145], v231 offset:26624\n"
"v_mfma_f32_16x16x32_bf16 v[76:79], a[108:111], v[156:159], v[76:79]\n"
"ds_read_b64_tr_b16 a[146:147], v231 offset:32768\n"
"v_mfma_f32_16x16x32_bf16 v[72:75], a[100:103], v[156:159], v[72:75]\n"
"ds_read_b128 a[120:123], v228 offset:6144\n"
"v_mfma_f32_16x16x32_bf16 v[72:75], a[96:99], v[152:155], v[72:75]\n"
"ds_read_b128 a[124:127], v229 offset:6144\n"
"v_mfma_f32_16x16x32_bf16 v[120:123], a[96:99], v[160:163], v[120:123]\n"
"buffer_load_dwordx4 v234, s[44:47], s66 offen offset:128 lds\n"
"v_mfma_f32_16x16x32_bf16 v[120:123], a[100:103], v[164:167], v[120:123]\n"
"ds_read_b128 a[128:131], v228 offset:7168\n"
"s_add_u32 m0, s97, 37760\n"
"v_mfma_f32_16x16x32_bf16 v[124:127], a[108:111], v[164:167], v[124:127]\n"
"ds_read_b128 a[132:135], v229 offset:7168\n"
"v_mfma_f32_16x16x32_bf16 v[124:127], a[104:107], v[160:163], v[124:127]\n"
"ds_read_b128 a[136:139], v228 offset:8192\n"
"v_mfma_f32_16x16x32_bf16 v[128:131], a[112:115], v[160:163], v[128:131]\n"
"ds_read_b128 a[140:143], v229 offset:8192\n"
"v_mfma_f32_16x16x32_bf16 v[128:131], a[116:119], v[164:167], v[128:131]\n"
"v_mfma_f32_16x16x32_bf16 a[200:203], a[116:119], v[172:175], a[200:203]\n"
"ds_read_b128 a[116:119], v229 offset:2048\n"
"v_mfma_f32_16x16x32_bf16 a[200:203], a[112:115], v[168:171], a[200:203]\n"
"ds_read_b128 a[112:115], v228 offset:2048\n"
"v_mfma_f32_16x16x32_bf16 a[196:199], a[104:107], v[168:171], a[196:199]\n"
"ds_read_b128 a[104:107], v228 offset:1024\n"
"v_mfma_f32_16x16x32_bf16 a[196:199], a[108:111], v[172:175], a[196:199]\n"
"ds_read_b128 a[108:111], v229 offset:1024\n"
"v_mfma_f32_16x16x32_bf16 a[192:195], a[100:103], v[172:175], a[192:195]\n"
"buffer_load_dwordx4 v234, s[44:47], s77 offen offset:128 lds\n"
"v_mfma_f32_16x16x32_bf16 a[192:195], a[96:99], v[168:171], a[192:195]\n"
"s_waitcnt lgkmcnt(10)\n"
"ds_read_b128 a[100:103], v229 offset:0\n"
"s_add_u32 m0, s97, 26368\n"
"v_mfma_f32_16x16x32_bf16 v[36:39], a[144:147], v[144:147], v[36:39]\n"
"ds_read_b128 a[96:99], v228 offset:0\n"
"v_mfma_f32_16x16x32_bf16 v[36:39], a[148:151], v[148:151], v[36:39]\n"
"v_mfma_f32_16x16x32_bf16 v[40:43], a[156:159], v[148:151], v[40:43]\n"
"v_mfma_f32_16x16x32_bf16 v[40:43], a[152:155], v[144:147], v[40:43]\n"
"v_mfma_f32_16x16x32_bf16 v[44:47], a[160:163], v[144:147], v[44:47]\n"
"v_mfma_f32_16x16x32_bf16 v[44:47], a[164:167], v[148:151], v[44:47]\n"
"v_mfma_f32_16x16x32_bf16 v[92:95], a[164:167], v[156:159], v[92:95]\n"
"v_mfma_f32_16x16x32_bf16 v[92:95], a[160:163], v[152:155], v[92:95]\n"
"v_mfma_f32_16x16x32_bf16 v[88:91], a[152:155], v[152:155], v[88:91]\n"
"v_mfma_f32_16x16x32_bf16 v[88:91], a[156:159], v[156:159], v[88:91]\n"
"v_mfma_f32_16x16x32_bf16 v[84:87], a[148:151], v[156:159], v[84:87]\n"
"s_waitcnt vmcnt(4) lgkmcnt(0)\n"
"s_barrier\n"
"v_mfma_f32_16x16x32_bf16 v[84:87], a[144:147], v[152:155], v[84:87]\n"
"v_mfma_f32_16x16x32_bf16 v[132:135], a[144:147], v[160:163], v[132:135]\n"
"v_mfma_f32_16x16x32_bf16 v[132:135], a[148:151], v[164:167], v[132:135]\n"
"v_mfma_f32_16x16x32_bf16 v[136:139], a[156:159], v[164:167], v[136:139]\n"
"buffer_load_dwordx4 v234, s[44:47], s66 offen offset:256 lds\n"
"v_mfma_f32_16x16x32_bf16 v[136:139], a[152:155], v[160:163], v[136:139]\n"
"s_add_u32 m0, s97, 38656\n"
"s_mul_i32 s66, s67, 24576\n"
"v_mfma_f32_16x16x32_bf16 v[140:143], a[160:163], v[160:163], v[140:143]\n"
"v_mfma_f32_16x16x32_bf16 v[140:143], a[164:167], v[164:167], v[140:143]\n"
"v_mfma_f32_16x16x32_bf16 a[212:215], a[164:167], v[172:175], a[212:215]\n"
"v_mfma_f32_16x16x32_bf16 a[212:215], a[160:163], v[168:171], a[212:215]\n"
"v_mfma_f32_16x16x32_bf16 a[208:211], a[152:155], v[168:171], a[208:211]\n"
"v_mfma_f32_16x16x32_bf16 a[208:211], a[156:159], v[172:175], a[208:211]\n"
"buffer_load_dwordx4 v234, s[44:47], s77 offen offset:256 lds\n"
"v_mfma_f32_16x16x32_bf16 a[204:207], a[148:151], v[172:175], a[204:207]\n"
"v_mfma_f32_16x16x32_bf16 a[204:207], a[144:147], v[168:171], a[204:207]\n"
"s_setpc_b64 s[62:63]\n"
"L_PFQ:\n"
"s_mov_b64 s[52:53], s[12:13]\n"
"s_branch L_PF\n"
"L_PFK:\n"
"s_mov_b64 s[52:53], s[14:15]\n"
"s_branch L_PF\n"
"L_PFV:\n"
"s_mov_b64 s[52:53], s[16:17]\n"
"L_PF:\n"
"v_readlane_b32 s61, v235, s60\n"
"v_mbcnt_lo_u32_b32 v144, -1, 0\n"
"v_mbcnt_hi_u32_b32 v144, -1, v144\n"
"s_lshl_b32 s92, s67, 2\n"
"s_add_u32 s92, s92, %3\n"
"v_lshlrev_b32 v144, 7, v144\n"
"s_lshl_b32 s92, s92, 13\n"
"s_mov_b32 m0, 161792\n"
"s_nop 0\n"
"buffer_load_dword v144, s[52:55], s92 offen lds\n"
"s_mov_b64 s[52:53], s[26:27]\n"
"s_add_u32 s60, s60, 1\n"
"s_and_b32 s98, s61, 0x3ffff\n"
"s_add_u32 s62, s64, s98\n"
"s_addc_u32 s63, s65, 0\n"
"s_lshr_b32 s67, s61, 18\n"
"s_mul_i32 s66, s67, 24576\n"
"s_setpc_b64 s[62:63]\n"
"L_TAB7:\n"
"s_mov_b32 s92, L_G-L_BASE+3407872\n"
"v_writelane_b32 v235, s92, 0\n"
"s_mov_b32 s92, L_GA-L_BASE+3145728\n"
"v_writelane_b32 v235, s92, 1\n"
"s_mov_b32 s92, L_TS0-L_BASE+2883584\n"
"v_writelane_b32 v235, s92, 2\n"
"s_mov_b32 s92, L_TS1-L_BASE+2621440\n"
"v_writelane_b32 v235, s92, 3\n"
"s_mov_b32 s92, L_TS2-L_BASE+2359296\n"
"v_writelane_b32 v235, s92, 4\n"
"s_mov_b32 s92, L_TS3-L_BASE+2097152\n"
"v_writelane_b32 v235, s92, 5\n"
"s_mov_b32 s92, L_VS-L_BASE+1835008\n"
"v_writelane_b32 v235, s92, 6\n"
"s_mov_b32 s92, L_GA-L_BASE+1572864\n"
"v_writelane_b32 v235, s92, 7\n"
"s_mov_b32 s92, L_N2-L_BASE+1310720\n"
"v_writelane_b32 v235, s92, 8\n"
"s_mov_b32 s92, L_GA-L_BASE+7340032\n"
"v_writelane_b32 v235, s92, 9\n"
"s_mov_b32 s92, L_D0-L_BASE+7602176\n"
"v_writelane_b32 v235, s92, 10\n"
"s_mov_b32 s92, L_P1-L_BASE+7864320\n"
"v_writelane_b32 v235, s92, 11\n"
"s_mov_b32 s92, L_P2-L_BASE+8126464\n"
"v_writelane_b32 v235, s92, 12\n"
"s_mov_b32 s92, L_P3-L_BASE+1048576\n"
"v_writelane_b32 v235, s92, 13\n"
"s_mov_b32 s92, L_N2-L_BASE+0\n"
"v_writelane_b32 v235, s92, 14\n"
"s_mov_b32 s92, L_ST-L_BASE+262144\n"
"v_writelane_b32 v235, s92, 15\n"
"s_mov_b32 s92, L_N2-L_BASE+524288\n"
"v_writelane_b32 v235, s92, 16\n"
"s_mov_b32 s92, L_POLL-L_BASE+786432\n"
"v_writelane_b32 v235, s92, 17\n"
"s_mov_b32 s92, L_G-L_BASE+10485760\n"
"v_writelane_b32 v235, s92, 18\n"
"s_mov_b32 s92, L_EA-L_BASE+0\n"
"v_writelane_b32 v235, s92, 19\n"
"s_mov_b32 s66, 344064\n"
"s_branch L_WARM1\n"
".p2align 12\n"
".p2align 5\n"
"L_W0GA:\n"
"s_nop 7\n"
"s_branch L_W0TG0\n"
"L_W1GA:\n"
"s_nop 7\n"
"s_branch L_W1P2\n"
"L_W2GA:\n"
"s_nop 7\n"
"s_branch L_W2D2\n"
"L_GA:\n"
"v_mfma_f32_16x16x32_bf16 v[144:147], a[96:99], a[0:3], v[188:191]\n"
"v_readlane_b32 s61, v235, s60\n"
"s_add_u32 s60, s60, 1\n"
"v_mfma_f32_16x16x32_bf16 v[144:147], a[100:103], a[4:7], v[144:147]\n"
"s_and_b32 s98, s61, 0x3ffff\n"
"s_add_u32 s62, s64, s98\n"
"s_addc_u32 s63, s65, 0\n"
"ds_bpermute_b32 v244, v250, v248 offset:0\n"
"v_mfma_f32_16x16x32_bf16 v[144:147], a[104:107], a[8:11], v[144:147]\n"
"s_add_u32 s77, s66, 12288\n"
"ds_bpermute_b32 v245, v250, v248 offset:4\n"
"ds_bpermute_b32 v246, v250, v249 offset:0\n"
"s_add_u32 m0, s97, 0\n"
"v_mfma_f32_16x16x32_bf16 v[144:147], a[108:111], a[12:15], v[144:147]\n"
"s_lshr_b32 s67, s61, 18\n"
"ds_bpermute_b32 v247, v250, v249 offset:4\n"
"ds_read_b128 a[144:147], v228 offset:12288\n"
"v_add_u32 v250, 8, v250\n"
"v_mfma_f32_16x16x32_bf16 v[144:147], a[112:115], a[16:19], v[144:147]\n"
"ds_read_b128 a[148:151], v229 offset:12288\n"
"v_mfma_f32_16x16x32_bf16 v[144:147], a[116:119], a[20:23], v[144:147]\n"
"buffer_load_dwordx4 v234, s[40:43], s66 offen offset:0 lds\n"
"v_mfma_f32_16x16x32_bf16 v[176:179], a[120:123], a[0:3], v[188:191]\n"
"ds_read_b128 a[152:155], v228 offset:13312\n"
"s_add_u32 m0, s97, 896\n"
"v_mfma_f32_16x16x32_bf16 v[176:179], a[124:127], a[4:7], v[176:179]\n"
"ds_read_b128 a[156:159], v229 offset:13312\n"
"v_mfma_f32_16x16x32_bf16 v[176:179], a[128:131], a[8:11], v[176:179]\n"
"ds_read_b128 a[160:163], v228 offset:14336\n"
"v_mfma_f32_16x16x32_bf16 v[176:179], a[132:135], a[12:15], v[176:179]\n"
"ds_read_b128 a[164:167], v229 offset:14336\n"
"v_mfma_f32_16x16x32_bf16 v[176:179], a[136:139], a[16:19], v[176:179]\n"
"v_exp_f32 v144, v144\n"
"v_mfma_f32_16x16x32_bf16 v[176:179], a[140:143], a[20:23], v[176:179]\n"
"s_waitcnt lgkmcnt(6)\n"
"v_and_or_b32 v244, v234, s75, v244\n"
"v_and_or_b32 v245, v234, s75, v245\n"
"v_and_or_b32 v246, v234, s75, v246\n"
"v_mfma_f32_16x16x32_bf16 v[152:155], a[96:99], a[24:27], v[192:195]\n"
"v_exp_f32 v145, v145\n"
"v_mfma_f32_16x16x32_bf16 v[152:155], a[100:103], a[28:31], v[152:155]\n"
"v_exp_f32 v146, v146\n"
"v_mfma_f32_16x16x32_bf16 v[152:155], a[104:107], a[32:35], v[152:155]\n"
"v_exp_f32 v147, v147\n"
"v_mfma_f32_16x16x32_bf16 v[152:155], a[108:111], a[36:39], v[152:155]\n"
"buffer_load_dwordx4 v234, s[40:43], s66 offen offset:128 lds\n"
"v_mfma_f32_16x16x32_bf16 v[152:155], a[112:115], a[40:43], v[152:155]\n"
"s_add_u32 m0, s97, 1792\n"
"v_cvt_pk_bf16_f32 v144, v144, v145\n"
"v_cvt_pk_bf16_f32 v145, v146, v147\n"
"v_mfma_f32_16x16x32_bf16 v[152:155], a[116:119], a[44:47], v[152:155]\n"
"v_exp_f32 v176, v176\n"
"v_mfma_f32_16x16x32_bf16 v[180:183], a[120:123], a[24:27], v[192:195]\n"
"v_exp_f32 v177, v177\n"
"v_mfma_f32_16x16x32_bf16 v[180:183], a[124:127], a[28:31], v[180:183]\n"
"v_exp_f32 v178, v178\n"
"v_mfma_f32_16x16x32_bf16 v[180:183], a[128:131], a[32:35], v[180:183]\n"
"v_exp_f32 v179, v179\n"
"v_mfma_f32_16x16x32_bf16 v[180:183], a[132:135], a[36:39], v[180:183]\n"
"v_exp_f32 v152, v152\n"
"v_mfma_f32_16x16x32_bf16 v[180:183], a[136:139], a[40:43], v[180:183]\n"
"v_exp_f32 v153, v153\n"
"v_mfma_f32_16x16x32_bf16 v[180:183], a[140:143], a[44:47], v[180:183]\n"
"v_exp_f32 v154, v154\n"
"v_mfma_f32_16x16x32_bf16 v[160:163], a[96:99], a[48:51], v[196:199]\n"
"v_exp_f32 v155, v155\n"
"v_mfma_f32_16x16x32_bf16 v[160:163], a[100:103], a[52:55], v[160:163]\n"
"buffer_load_dwordx4 v234, s[40:43], s66 offen offset:256 lds\n"
"v_mfma_f32_16x16x32_bf16 v[160:163], a[104:107], a[56:59], v[160:163]\n"
"s_add_u32 m0, s72, 12288\n"
"v_cvt_pk_bf16_f32 v146, v176, v177\n"
"v_exp_f32 v180, v180\n"
"v_mfma_f32_16x16x32_bf16 v[160:163], a[108:111], a[60:63], v[160:163]\n"
"v_exp_f32 v181, v181\n"
"v_mfma_f32_16x16x32_bf16 v[160:163], a[112:115], a[64:67], v[160:163]\n"
"v_exp_f32 v182, v182\n"
"v_mfma_f32_16x16x32_bf16 v[160:163], a[116:119], a[68:71], v[160:163]\n"
"v_exp_f32 v183, v183\n"
"v_mfma_f32_16x16x32_bf16 v[184:187], a[120:123], a[48:51], v[196:199]\n"
"v_cvt_pk_bf16_f32 v147, v178, v179\n"
"v_cvt_pk_bf16_f32 v152, v152, v153\n"
"v_mfma_f32_16x16x32_bf16 v[184:187], a[124:127], a[52:55], v[184:187]\n"
"v_cvt_pk_bf16_f32 v153, v154, v155\n"
"v_cvt_pk_bf16_f32 v154, v180, v181\n"
"v_mfma_f32_16x16x32_bf16 v[184:187], a[128:131], a[56:59], v[184:187]\n"
"buffer_load_dwordx4 v244, s[48:51], 0 offen offset:0 lds\n"
"v_mfma_f32_16x16x32_bf16 v[184:187], a[132:135], a[60:63], v[184:187]\n"
"s_add_u32 m0, s72, 13184\n"
"v_exp_f32 v160, v160\n"
"v_mfma_f32_16x16x32_bf16 v[184:187], a[136:139], a[64:67], v[184:187]\n"
"v_exp_f32 v161, v161\n"
"v_mfma_f32_16x16x32_bf16 v[184:187], a[140:143], a[68:71], v[184:187]\n"
"v_exp_f32 v162, v162\n"
"v_mfma_f32_16x16x32_bf16 v[168:171], a[96:99], a[72:75], v[200:203]\n"
"ds_read_b128 a[96:99], v228 offset:18432\n"
"v_exp_f32 v163, v163\n"
"v_mfma_f32_16x16x32_bf16 v[168:171], a[100:103], a[76:79], v[168:171]\n"
"ds_read_b128 a[100:103], v229 offset:18432\n"
"v_cvt_pk_bf16_f32 v155, v182, v183\n"
"v_mfma_f32_16x16x32_bf16 v[168:171], a[104:107], a[80:83], v[168:171]\n"
"v_exp_f32 v184, v184\n"
"v_mfma_f32_16x16x32_bf16 v[168:171], a[108:111], a[84:87], v[168:171]\n"
"v_exp_f32 v185, v185\n"
"v_mfma_f32_16x16x32_bf16 v[168:171], a[112:115], a[88:91], v[168:171]\n"
"v_exp_f32 v186, v186\n"
"v_mfma_f32_16x16x32_bf16 v[168:171], a[116:119], a[92:95], v[168:171]\n"
"ds_read_b128 a[104:107], v228 offset:19456\n"
"v_exp_f32 v187, v187\n"
"v_mfma_f32_16x16x32_bf16 v[176:179], a[120:123], a[72:75], v[200:203]\n"
"ds_read_b128 a[108:111], v229 offset:19456\n"
"v_cvt_pk_bf16_f32 v160, v160, v161\n"
"v_mfma_f32_16x16x32_bf16 v[176:179], a[124:127], a[76:79], v[176:179]\n"
"ds_read_b128 a[112:115], v228 offset:20480\n"
"v_cvt_pk_bf16_f32 v161, v162, v163\n"
"v_mfma_f32_16x16x32_bf16 v[176:179], a[128:131], a[80:83], v[176:179]\n"
"ds_read_b128 a[116:119], v229 offset:20480\n"
"v_cvt_pk_bf16_f32 v162, v184, v185\n"
"v_mfma_f32_16x16x32_bf16 v[176:179], a[132:135], a[84:87], v[176:179]\n"
"buffer_load_dwordx4 v244, s[48:51], 0 offen offset:128 lds\n"
"v_mfma_f32_16x16x32_bf16 v[176:179], a[136:139], a[88:91], v[176:179]\n"
"s_add_u32 m0, s72, 14080\n"
"v_exp_f32 v168, v168\n"
"v_mfma_f32_16x16x32_bf16 v[176:179], a[140:143], a[92:95], v[176:179]\n"
"s_waitcnt lgkmcnt(6)\n"
"v_exp_f32 v169, v169\n"
"v_mfma_f32_16x16x32_bf16 v[148:151], a[144:147], a[0:3], v[188:191]\n"
"v_exp_f32 v170, v170\n"
"v_mfma_f32_16x16x32_bf16 v[148:151], a[148:151], a[4:7], v[148:151]\n"
"v_exp_f32 v171, v171\n"
"v_mfma_f32_16x16x32_bf16 v[148:151], a[152:155], a[8:11], v[148:151]\n"
"buffer_load_dwordx4 v244, s[48:51], 0 offen offset:256 lds\n"
"v_mfma_f32_16x16x32_bf16 v[148:151], a[156:159], a[12:15], v[148:151]\n"
"s_add_u32 m0, s97, 12288\n"
"v_exp_f32 v176, v176\n"
"v_mfma_f32_16x16x32_bf16 v[148:151], a[160:163], a[16:19], v[148:151]\n"
"s_waitcnt vmcnt(8) lgkmcnt(0)\n"
"v_exp_f32 v177, v177\n"
"v_mfma_f32_16x16x32_bf16 v[148:151], a[164:167], a[20:23], v[148:151]\n"
"s_barrier\n"
"v_exp_f32 v178, v178\n"
"v_mfma_f32_16x16x32_bf16 v[180:183], a[96:99], a[0:3], v[188:191]\n"
"v_exp_f32 v179, v179\n"
"v_mfma_f32_16x16x32_bf16 v[180:183], a[100:103], a[4:7], v[180:183]\n"
"ds_read_b64_tr_b16 a[120:121], v230 offset:24576\n"
"v_cvt_pk_bf16_f32 v163, v186, v187\n"
"v_mfma_f32_16x16x32_bf16 v[180:183], a[104:107], a[8:11], v[180:183]\n"
"ds_read_b64_tr_b16 a[122:123], v230 offset:30720\n"
"v_cvt_pk_bf16_f32 v168, v168, v169\n"
"v_mfma_f32_16x16x32_bf16 v[180:183], a[108:111], a[12:15], v[180:183]\n"
"ds_read_b64_tr_b16 a[124:125], v230 offset:36864\n"
"v_cvt_pk_bf16_f32 v169, v170, v171\n"
"v_mfma_f32_16x16x32_bf16 v[180:183], a[112:115], a[16:19], v[180:183]\n"
"ds_read_b64_tr_b16 a[126:127], v230 offset:43008\n"
"v_cvt_pk_bf16_f32 v170, v176, v177\n"
"v_mfma_f32_16x16x32_bf16 v[180:183], a[116:119], a[20:23], v[180:183]\n"
"ds_read_b64_tr_b16 a[128:129], v231 offset:24576\n"
"v_cvt_pk_bf16_f32 v171, v178, v179\n"
"v_mfma_f32_16x16x32_bf16 v[156:159], a[144:147], a[24:27], v[192:195]\n"
"ds_read_b64_tr_b16 a[130:131], v231 offset:30720\n"
"v_and_or_b32 v247, v234, s75, v247\n"
"v_mfma_f32_16x16x32_bf16 v[156:159], a[148:151], a[28:31], v[156:159]\n"
"buffer_load_dwordx4 v234, s[40:43], s77 offen offset:0 lds\n"
"v_mfma_f32_16x16x32_bf16 v[156:159], a[152:155], a[32:35], v[156:159]\n"
"ds_read_b64_tr_b16 a[132:133], v231 offset:36864\n"
"s_add_u32 m0, s97, 13184\n"
"v_mfma_f32_16x16x32_bf16 v[156:159], a[156:159], a[36:39], v[156:159]\n"
"ds_read_b64_tr_b16 a[134:135], v231 offset:43008\n"
"v_mfma_f32_16x16x32_bf16 v[156:159], a[160:163], a[40:43], v[156:159]\n"
"v_exp_f32 v148, v148\n"
"v_mfma_f32_16x16x32_bf16 v[156:159], a[164:167], a[44:47], v[156:159]\n"
"v_exp_f32 v149, v149\n"
"v_mfma_f32_16x16x32_bf16 v[184:187], a[96:99], a[24:27], v[192:195]\n"
"v_exp_f32 v150, v150\n"
"v_mfma_f32_16x16x32_bf16 v[184:187], a[100:103], a[28:31], v[184:187]\n"
"v_exp_f32 v151, v151\n"
"v_mfma_f32_16x16x32_bf16 v[184:187], a[104:107], a[32:35], v[184:187]\n"
"ds_read_b64_tr_b16 a[136:137], v232 offset:24576\n"
"v_cvt_pk_bf16_f32 v148, v148, v149\n"
"v_mfma_f32_16x16x32_bf16 v[184:187], a[108:111], a[36:39], v[184:187]\n"
"ds_read_b64_tr_b16 a[138:139], v232 offset:30720\n"
"v_cvt_pk_bf16_f32 v149, v150, v151\n"
"v_mfma_f32_16x16x32_bf16 v[184:187], a[112:115], a[40:43], v[184:187]\n"
"v_exp_f32 v180, v180\n"
"v_mfma_f32_16x16x32_bf16 v[184:187], a[116:119], a[44:47], v[184:187]\n"
"buffer_load_dwordx4 v234, s[40:43], s77 offen offset:128 lds\n"
"v_mfma_f32_16x16x32_bf16 v[164:167], a[144:147], a[48:51], v[196:199]\n"
"s_add_u32 m0, s97, 14080\n"
"v_exp_f32 v181, v181\n"
"v_mfma_f32_16x16x32_bf16 v[164:167], a[148:151], a[52:55], v[164:167]\n"
"v_exp_f32 v182, v182\n"
"v_mfma_f32_16x16x32_bf16 v[164:167], a[152:155], a[56:59], v[164:167]\n"
"v_exp_f32 v183, v183\n"
"v_mfma_f32_16x16x32_bf16 v[164:167], a[156:159], a[60:63], v[164:167]\n"
"ds_read_b64_tr_b16 a[140:141], v232 offset:36864\n"
"v_cvt_pk_bf16_f32 v150, v180, v181\n"
"v_mfma_f32_16x16x32_bf16 v[164:167], a[160:163], a[64:67], v[164:167]\n"
"ds_read_b64_tr_b16 a[142:143], v232 offset:43008\n"
"v_cvt_pk_bf16_f32 v151, v182, v183\n"
"v_mfma_f32_16x16x32_bf16 v[164:167], a[164:167], a[68:71], v[164:167]\n"
"v_exp_f32 v156, v156\n"
"v_mfma_f32_16x16x32_bf16 v[176:179], a[96:99], a[48:51], v[196:199]\n"
"v_exp_f32 v157, v157\n"
"v_mfma_f32_16x16x32_bf16 v[176:179], a[100:103], a[52:55], v[176:179]\n"
"v_exp_f32 v158, v158\n"
"v_mfma_f32_16x16x32_bf16 v[176:179], a[104:107], a[56:59], v[176:179]\n"
"v_exp_f32 v159, v159\n"
"v_mfma_f32_16x16x32_bf16 v[176:179], a[108:111], a[60:63], v[176:179]\n"
"buffer_load_dwordx4 v234, s[40:43], s77 offen offset:256 lds\n"
"v_mfma_f32_16x16x32_bf16 v[176:179], a[112:115], a[64:67], v[176:179]\n"
"s_add_u32 m0, s72, 15360\n"
"v_exp_f32 v184, v184\n"
"v_mfma_f32_16x16x32_bf16 v[176:179], a[116:119], a[68:71], v[176:179]\n"
"v_exp_f32 v185, v185\n"
"v_mfma_f32_16x16x32_bf16 v[172:175], a[144:147], a[72:75], v[200:203]\n"
"v_exp_f32 v186, v186\n"
"v_mfma_f32_16x16x32_bf16 v[172:175], a[148:151], a[76:79], v[172:175]\n"
"v_exp_f32 v187, v187\n"
"v_mfma_f32_16x16x32_bf16 v[172:175], a[152:155], a[80:83], v[172:175]\n"
"v_exp_f32 v164, v164\n"
"v_mfma_f32_16x16x32_bf16 v[172:175], a[156:159], a[84:87], v[172:175]\n"
"v_exp_f32 v165, v165\n"
"v_mfma_f32_16x16x32_bf16 v[172:175], a[160:163], a[88:91], v[172:175]\n"
"v_exp_f32 v166, v166\n"
"v_mfma_f32_16x16x32_bf16 v[172:175], a[164:167], a[92:95], v[172:175]\n"
"v_exp_f32 v167, v167\n"
"v_mfma_f32_16x16x32_bf16 v[180:183], a[96:99], a[72:75], v[200:203]\n"
"ds_read_b64_tr_b16 a[144:145], v233 offset:24576\n"
"v_cvt_pk_bf16_f32 v156, v156, v157\n"
"v_mfma_f32_16x16x32_bf16 v[180:183], a[100:103], a[76:79], v[180:183]\n"
"buffer_load_dwordx4 v245, s[48:51], 0 offen offset:0 lds\n"
"v_mfma_f32_16x16x32_bf16 v[180:183], a[104:107], a[80:83], v[180:183]\n"
"s_add_u32 m0, s72, 16256\n"
"ds_read_b64_tr_b16 a[146:147], v233 offset:30720\n"
"v_cvt_pk_bf16_f32 v157, v158, v159\n"
"v_mfma_f32_16x16x32_bf16 v[180:183], a[108:111], a[84:87], v[180:183]\n"
"ds_read_b64_tr_b16 a[148:149], v233 offset:36864\n"
"v_cvt_pk_bf16_f32 v158, v184, v185\n"
"v_mfma_f32_16x16x32_bf16 v[180:183], a[112:115], a[88:91], v[180:183]\n"
"s_waitcnt lgkmcnt(3)\n"
"ds_read_b64_tr_b16 a[150:151], v233 offset:43008\n"
"v_cvt_pk_bf16_f32 v159, v186, v187\n"
"v_mfma_f32_16x16x32_bf16 v[180:183], a[116:119], a[92:95], v[180:183]\n"
"v_exp_f32 v176, v176\n"
"v_mfma_f32_16x16x32_bf16 v[0:3], a[120:123], v[144:147], v[0:3]\n"
"v_exp_f32 v177, v177\n"
"v_mfma_f32_16x16x32_bf16 v[0:3], a[124:127], v[148:151], v[0:3]\n"
"v_exp_f32 v178, v178\n"
"v_mfma_f32_16x16x32_bf16 v[4:7], a[132:135], v[148:151], v[4:7]\n"
"v_exp_f32 v179, v179\n"
"v_mfma_f32_16x16x32_bf16 v[4:7], a[128:131], v[144:147], v[4:7]\n"
"ds_read_b64_tr_b16 a[152:153], v230 offset:25600\n"
"v_cvt_pk_bf16_f32 v164, v164, v165\n"
"v_mfma_f32_16x16x32_bf16 v[8:11], a[136:139], v[144:147], v[8:11]\n"
"buffer_load_dwordx4 v245, s[48:51], 0 offen offset:128 lds\n"
"v_mfma_f32_16x16x32_bf16 v[8:11], a[140:143], v[148:151], v[8:11]\n"
"s_add_u32 m0, s72, 17152\n"
"ds_read_b64_tr_b16 a[154:155], v230 offset:31744\n"
"v_cvt_pk_bf16_f32 v165, v166, v167\n"
"v_smfmac_f32_16x16x64_bf16 a[216:219], a[224:227], v[144:151], v237\n"
"ds_read_b64_tr_b16 a[156:157], v230 offset:37888\n"
"v_cvt_pk_bf16_f32 v166, v176, v177\n"
"v_mfma_f32_16x16x32_bf16 v[56:59], a[140:143], v[156:159], v[56:59]\n"
"ds_read_b64_tr_b16 a[158:159], v230 offset:44032\n"
"v_cvt_pk_bf16_f32 v167, v178, v179\n"
"v_mfma_f32_16x16x32_bf16 v[56:59], a[136:139], v[152:155], v[56:59]\n"
"v_exp_f32 v172, v172\n"
"v_mfma_f32_16x16x32_bf16 v[52:55], a[128:131], v[152:155], v[52:55]\n"
"v_exp_f32 v173, v173\n"
"v_mfma_f32_16x16x32_bf16 v[52:55], a[132:135], v[156:159], v[52:55]\n"
"v_exp_f32 v174, v174\n"
"v_mfma_f32_16x16x32_bf16 v[48:51], a[124:127], v[156:159], v[48:51]\n"
"v_exp_f32 v175, v175\n"
"v_mfma_f32_16x16x32_bf16 v[48:51], a[120:123], v[152:155], v[48:51]\n"
"ds_read_b64_tr_b16 a[160:161], v231 offset:25600\n"
"v_cvt_pk_bf16_f32 v172, v172, v173\n"
"v_smfmac_f32_16x16x64_bf16 a[216:219], a[228:231], v[152:159], v237\n"
"buffer_load_dwordx4 v245, s[48:51], 0 offen offset:256 lds\n"
"v_mfma_f32_16x16x32_bf16 v[96:99], a[120:123], v[160:163], v[96:99]\n"
"s_add_u32 m0, s72, 18432\n"
"ds_read_b64_tr_b16 a[162:163], v231 offset:31744\n"
"v_cvt_pk_bf16_f32 v173, v174, v175\n"
"v_mfma_f32_16x16x32_bf16 v[96:99], a[124:127], v[164:167], v[96:99]\n"
"v_exp_f32 v180, v180\n"
"v_mfma_f32_16x16x32_bf16 v[100:103], a[132:135], v[164:167], v[100:103]\n"
"v_exp_f32 v181, v181\n"
"v_mfma_f32_16x16x32_bf16 v[100:103], a[128:131], v[160:163], v[100:103]\n"
"v_exp_f32 v182, v182\n"
"v_mfma_f32_16x16x32_bf16 v[104:107], a[136:139], v[160:163], v[104:107]\n"
"v_exp_f32 v183, v183\n"
"v_mfma_f32_16x16x32_bf16 v[104:107], a[140:143], v[164:167], v[104:107]\n"
"ds_read_b64_tr_b16 a[164:165], v231 offset:37888\n"
"v_cvt_pk_bf16_f32 v174, v180, v181\n"
"v_smfmac_f32_16x16x64_bf16 a[220:223], a[224:227], v[160:167], v237\n"
"ds_read_b64_tr_b16 a[166:167], v231 offset:44032\n"
"v_cvt_pk_bf16_f32 v175, v182, v183\n"
"s_nop 1\n"
"v_mfma_f32_16x16x32_bf16 a[176:179], a[140:143], v[172:175], a[176:179]\n"
"buffer_load_dwordx4 v246, s[48:51], 0 offen offset:0 lds\n"
"v_mfma_f32_16x16x32_bf16 a[176:179], a[136:139], v[168:171], a[176:179]\n"
"s_add_u32 m0, s72, 19328\n"
"ds_read_b64_tr_b16 a[96:97], v232 offset:25600\n"
"v_mfma_f32_16x16x32_bf16 a[172:175], a[128:131], v[168:171], a[172:175]\n"
"ds_read_b64_tr_b16 a[98:99], v232 offset:31744\n"
"v_mfma_f32_16x16x32_bf16 a[172:175], a[132:135], v[172:175], a[172:175]\n"
"ds_read_b64_tr_b16 a[100:101], v232 offset:37888\n"
"v_mfma_f32_16x16x32_bf16 a[168:171], a[124:127], v[172:175], a[168:171]\n"
"buffer_load_dwordx4 v246, s[48:51], 0 offen offset:128 lds\n"
"v_mfma_f32_16x16x32_bf16 a[168:171], a[120:123], v[168:171], a[168:171]\n"
"ds_read_b64_tr_b16 a[102:103], v232 offset:44032\n"
"s_add_u32 m0, s72, 20224\n"
"v_smfmac_f32_16x16x64_bf16 a[220:223], a[228:231], v[168:175], v237\n"
"s_waitcnt lgkmcnt(4)\n"
"ds_read_b64_tr_b16 a[104:105], v233 offset:25600\n"
"v_mfma_f32_16x16x32_bf16 v[12:15], a[144:147], v[144:147], v[12:15]\n"
"ds_read_b64_tr_b16 a[106:107], v233 offset:31744\n"
"v_mfma_f32_16x16x32_bf16 v[12:15], a[148:151], v[148:151], v[12:15]\n"
"ds_read_b64_tr_b16 a[108:109], v233 offset:37888\n"
"v_mfma_f32_16x16x32_bf16 v[16:19], a[156:159], v[148:151], v[16:19]\n"
"ds_read_b64_tr_b16 a[110:111], v233 offset:44032\n"
"v_mfma_f32_16x16x32_bf16 v[16:19], a[152:155], v[144:147], v[16:19]\n"
"v_mfma_f32_16x16x32_bf16 v[20:23], a[160:163], v[144:147], v[20:23]\n"
"buffer_load_dwordx4 v246, s[48:51], 0 offen offset:256 lds\n"
"v_mfma_f32_16x16x32_bf16 v[20:23], a[164:167], v[148:151], v[20:23]\n"
"s_add_u32 m0, s72, 21504\n"
"v_mfma_f32_16x16x32_bf16 v[68:71], a[164:167], v[156:159], v[68:71]\n"
"v_mfma_f32_16x16x32_bf16 v[68:71], a[160:163], v[152:155], v[68:71]\n"
"v_mfma_f32_16x16x32_bf16 v[64:67], a[152:155], v[152:155], v[64:67]\n"
"v_mfma_f32_16x16x32_bf16 v[64:67], a[156:159], v[156:159], v[64:67]\n"
"v_mfma_f32_16x16x32_bf16 v[60:63], a[148:151], v[156:159], v[60:63]\n"
"v_mfma_f32_16x16x32_bf16 v[60:63], a[144:147], v[152:155], v[60:63]\n"
"buffer_load_dwordx4 v247, s[48:51], 0 offen offset:0 lds\n"
"v_mfma_f32_16x16x32_bf16 v[108:111], a[144:147], v[160:163], v[108:111]\n"
"s_add_u32 m0, s72, 22400\n"
"v_mfma_f32_16x16x32_bf16 v[108:111], a[148:151], v[164:167], v[108:111]\n"
"v_mfma_f32_16x16x32_bf16 v[112:115], a[156:159], v[164:167], v[112:115]\n"
"v_mfma_f32_16x16x32_bf16 v[112:115], a[152:155], v[160:163], v[112:115]\n"
"v_mfma_f32_16x16x32_bf16 v[116:119], a[160:163], v[160:163], v[116:119]\n"
"s_waitcnt vmcnt(13) lgkmcnt(0)\n"
"s_barrier\n"
"ds_read_b64_tr_b16 a[112:113], v230 offset:26624\n"
"v_mfma_f32_16x16x32_bf16 v[116:119], a[164:167], v[164:167], v[116:119]\n"
"ds_read_b64_tr_b16 a[114:115], v230 offset:32768\n"
"v_mfma_f32_16x16x32_bf16 a[188:191], a[164:167], v[172:175], a[188:191]\n"
"ds_read_b64_tr_b16 a[116:117], v230 offset:38912\n"
"v_mfma_f32_16x16x32_bf16 a[188:191], a[160:163], v[168:171], a[188:191]\n"
"buffer_load_dwordx4 v247, s[48:51], 0 offen offset:128 lds\n"
"v_mfma_f32_16x16x32_bf16 a[184:187], a[152:155], v[168:171], a[184:187]\n"
"ds_read_b64_tr_b16 a[118:119], v230 offset:45056\n"
"s_add_u32 m0, s97, 24576\n"
"v_mfma_f32_16x16x32_bf16 a[184:187], a[156:159], v[172:175], a[184:187]\n"
"buffer_load_dwordx4 v234, s[44:47], s66 offen offset:0 lds\n"
"v_mfma_f32_16x16x32_bf16 a[180:183], a[148:151], v[172:175], a[180:183]\n"
"ds_read_b64_tr_b16 a[164:165], v233 offset:38912\n"
"s_add_u32 m0, s97, 36864\n"
"v_mfma_f32_16x16x32_bf16 a[180:183], a[144:147], v[168:171], a[180:183]\n"
"ds_read_b64_tr_b16 a[166:167], v233 offset:45056\n"
"v_mfma_f32_16x16x32_bf16 v[24:27], a[96:99], v[144:147], v[24:27]\n"
"ds_read_b64_tr_b16 a[160:161], v233 offset:26624\n"
"v_mfma_f32_16x16x32_bf16 v[24:27], a[100:103], v[148:151], v[24:27]\n"
"ds_read_b64_tr_b16 a[162:163], v233 offset:32768\n"
"v_mfma_f32_16x16x32_bf16 v[28:31], a[108:111], v[148:151], v[28:31]\n"
"ds_read_b64_tr_b16 a[152:153], v232 offset:26624\n"
"v_mfma_f32_16x16x32_bf16 v[28:31], a[104:107], v[144:147], v[28:31]\n"
"s_waitcnt lgkmcnt(5)\n"
"ds_read_b64_tr_b16 a[154:155], v232 offset:32768\n"
"v_mfma_f32_16x16x32_bf16 v[32:35], a[112:115], v[144:147], v[32:35]\n"
"buffer_load_dwordx4 v234, s[44:47], s77 offen offset:0 lds\n"
"v_mfma_f32_16x16x32_bf16 v[32:35], a[116:119], v[148:151], v[32:35]\n"
"ds_read_b64_tr_b16 a[156:157], v232 offset:38912\n"
"s_add_u32 m0, s97, 25472\n"
"v_mfma_f32_16x16x32_bf16 v[80:83], a[116:119], v[156:159], v[80:83]\n"
"ds_read_b64_tr_b16 a[158:159], v232 offset:45056\n"
"v_mfma_f32_16x16x32_bf16 v[80:83], a[112:115], v[152:155], v[80:83]\n"
"ds_read_b64_tr_b16 a[148:149], v231 offset:38912\n"
"v_mfma_f32_16x16x32_bf16 v[76:79], a[104:107], v[152:155], v[76:79]\n"
"ds_read_b64_tr_b16 a[150:151], v231 offset:45056\n"
"v_mfma_f32_16x16x32_bf16 v[76:79], a[108:111], v[156:159], v[76:79]\n"
"ds_read_b64_tr_b16 a[144:145], v231 offset:26624\n"
"v_mfma_f32_16x16x32_bf16 v[72:75], a[100:103], v[156:159], v[72:75]\n"
"ds_read_b64_tr_b16 a[146:147], v231 offset:32768\n"
"v_mfma_f32_16x16x32_bf16 v[72:75], a[96:99], v[152:155], v[72:75]\n"
"ds_read_b128 a[120:123], v228 offset:6144\n"
"v_mfma_f32_16x16x32_bf16 v[120:123], a[96:99], v[160:163], v[120:123]\n"
"buffer_load_dwordx4 v234, s[44:47], s66 offen offset:128 lds\n"
"v_mfma_f32_16x16x32_bf16 v[120:123], a[100:103], v[164:167], v[120:123]\n"
"s_add_u32 m0, s72, 23296\n"
"ds_read_b128 a[124:127], v229 offset:6144\n"
"v_mfma_f32_16x16x32_bf16 v[124:127], a[108:111], v[164:167], v[124:127]\n"
"ds_read_b128 a[128:131], v228 offset:7168\n"
"v_mfma_f32_16x16x32_bf16 v[124:127], a[104:107], v[160:163], v[124:127]\n"
"ds_read_b128 a[132:135], v229 offset:7168\n"
"v_mfma_f32_16x16x32_bf16 v[128:131], a[112:115], v[160:163], v[128:131]\n"
"ds_read_b128 a[136:139], v228 offset:8192\n"
"v_mfma_f32_16x16x32_bf16 v[128:131], a[116:119], v[164:167], v[128:131]\n"
"buffer_load_dwordx4 v247, s[48:51], 0 offen offset:256 lds\n"
"v_mfma_f32_16x16x32_bf16 a[200:203], a[116:119], v[172:175], a[200:203]\n"
"s_add_u32 m0, s97, 37760\n"
"ds_read_b128 a[140:143], v229 offset:8192\n"
"v_mfma_f32_16x16x32_bf16 a[200:203], a[112:115], v[168:171], a[200:203]\n"
"ds_read_b128 a[116:119], v229 offset:2048\n"
"v_mfma_f32_16x16x32_bf16 a[196:199], a[104:107], v[168:171], a[196:199]\n"
"ds_read_b128 a[112:115], v228 offset:2048\n"
"v_mfma_f32_16x16x32_bf16 a[196:199], a[108:111], v[172:175], a[196:199]\n"
"ds_read_b128 a[104:107], v228 offset:1024\n"
"v_mfma_f32_16x16x32_bf16 a[192:195], a[100:103], v[172:175], a[192:195]\n"
"buffer_load_dwordx4 v234, s[44:47], s77 offen offset:128 lds\n"
"v_mfma_f32_16x16x32_bf16 a[192:195], a[96:99], v[168:171], a[192:195]\n"
"s_waitcnt lgkmcnt(9)\n"
"ds_read_b128 a[108:111], v229 offset:1024\n"
"s_add_u32 m0, s97, 26368\n"
"v_mfma_f32_16x16x32_bf16 v[36:39], a[144:147], v[144:147], v[36:39]\n"
"ds_read_b128 a[100:103], v229 offset:0\n"
"v_mfma_f32_16x16x32_bf16 v[36:39], a[148:151], v[148:151], v[36:39]\n"
"ds_read_b128 a[96:99], v228 offset:0\n"
"v_mfma_f32_16x16x32_bf16 v[40:43], a[156:159], v[148:151], v[40:43]\n"
"v_mfma_f32_16x16x32_bf16 v[40:43], a[152:155], v[144:147], v[40:43]\n"
"v_mfma_f32_16x16x32_bf16 v[44:47], a[160:163], v[144:147], v[44:47]\n"
"v_mfma_f32_16x16x32_bf16 v[44:47], a[164:167], v[148:151], v[44:47]\n"
"v_mfma_f32_16x16x32_bf16 v[92:95], a[164:167], v[156:159], v[92:95]\n"
"v_mfma_f32_16x16x32_bf16 v[92:95], a[160:163], v[152:155], v[92:95]\n"
"v_mfma_f32_16x16x32_bf16 v[88:91], a[152:155], v[152:155], v[88:91]\n"
"v_mfma_f32_16x16x32_bf16 v[88:91], a[156:159], v[156:159], v[88:91]\n"
"v_mfma_f32_16x16x32_bf16 v[84:87], a[148:151], v[156:159], v[84:87]\n"
"s_waitcnt vmcnt(13) lgkmcnt(0)\n"
"s_barrier\n"
"v_mfma_f32_16x16x32_bf16 v[84:87], a[144:147], v[152:155], v[84:87]\n"
"v_mfma_f32_16x16x32_bf16 v[132:135], a[144:147], v[160:163], v[132:135]\n"
"v_mfma_f32_16x16x32_bf16 v[132:135], a[148:151], v[164:167], v[132:135]\n"
"v_mfma_f32_16x16x32_bf16 v[136:139], a[156:159], v[164:167], v[136:139]\n"
"buffer_load_dwordx4 v234, s[44:47], s66 offen offset:256 lds\n"
"v_mfma_f32_16x16x32_bf16 v[136:139], a[152:155], v[160:163], v[136:139]\n"
"s_add_u32 m0, s97, 38656\n"
"s_mul_i32 s66, s67, 24576\n"
"v_mfma_f32_16x16x32_bf16 v[140:143], a[160:163], v[160:163], v[140:143]\n"
"v_mfma_f32_16x16x32_bf16 v[140:143], a[164:167], v[164:167], v[140:143]\n"
"v_mfma_f32_16x16x32_bf16 a[212:215], a[164:167], v[172:175], a[212:215]\n"
"v_mfma_f32_16x16x32_bf16 a[212:215], a[160:163], v[168:171], a[212:215]\n"
"v_mfma_f32_16x16x32_bf16 a[208:211], a[152:155], v[168:171], a[208:211]\n"
"v_mfma_f32_16x16x32_bf16 a[208:211], a[156:159], v[172:175], a[208:211]\n"
"buffer_load_dwordx4 v234, s[44:47], s77 offen offset:256 lds\n"
"v_mfma_f32_16x16x32_bf16 a[204:207], a[148:151], v[172:175], a[204:207]\n"
"v_mfma_f32_16x16x32_bf16 a[204:207], a[144:147], v[168:171], a[204:207]\n"
"s_setpc_b64 s[62:63]\n"
"L_TS0:\n"
"v_accvgpr_mov_b32 a232, a0\n"
"v_accvgpr_mov_b32 a233, a1\n"
"v_accvgpr_mov_b32 a234, a2\n"
"v_accvgpr_mov_b32 a235, a3\n"
"v_accvgpr_mov_b32 a236, a4\n"
"v_accvgpr_mov_b32 a237, a5\n"
"v_accvgpr_mov_b32 a238, a6\n"
"v_accvgpr_mov_b32 a239, a7\n"
"v_accvgpr_mov_b32 a240, a8\n"
"v_accvgpr_mov_b32 a241, a9\n"
"v_accvgpr_mov_b32 a242, a10\n"
"v_accvgpr_mov_b32 a243, a11\n"
"v_accvgpr_mov_b32 a244, a12\n"
"v_accvgpr_mov_b32 a245, a13\n"
"v_accvgpr_mov_b32 a246, a14\n"
"v_accvgpr_mov_b32 a247, a15\n"
"v_accvgpr_mov_b32 a248, a16\n"
"v_accvgpr_mov_b32 a249, a17\n"
"v_accvgpr_mov_b32 a250, a18\n"
"v_accvgpr_mov_b32 a251, a19\n"
"v_accvgpr_mov_b32 a252, a20\n"
"v_accvgpr_mov_b32 a253, a21\n"
"v_accvgpr_mov_b32 a254, a22\n"
"v_accvgpr_mov_b32 a255, a23\n"
"s_branch L_A1\n"
".p2align 5\n"
"L_W0TG0:\n"
"s_nop 7\n"
"s_branch L_W0FLW\n"
"L_TG0:\n"
"s_mov_b64 s[56:57], s[12:13]\n"
"s_mov_b32 s58, 786432\n"
"s_add_u32 s92, s89, 0\n"
"buffer_load_dwordx4 a[232:235], v234, s[56:59], s92 offen offset:0\n"
"buffer_load_dwordx4 a[236:239], v234, s[56:59], s92 offen offset:3072\n"
"buffer_load_dwordx4 a[240:243], v234, s[56:59], s92 offen offset:128\n"
"buffer_load_dwordx4 a[244:247], v234, s[56:59], s92 offen offset:3200\n"
"buffer_load_dwordx4 a[248:251], v234, s[56:59], s92 offen offset:256\n"
"buffer_load_dwordx4 a[252:255], v234, s[56:59], s92 offen offset:3328\n"
"s_branch L_B1\n"
".p2align 5\n"
"L_W0FLW:\n"
"s_nop 7\n"
"s_branch L_W0D3\n"
"L_W1FLW:\n"
"s_nop 7\n"
"s_branch L_W1EA\n"
"L_FLW:\n"
"s_waitcnt vmcnt(0)\n"
"s_barrier\n"
"L_FL:\n"
"v_readlane_b32 s61, v235, s60\n"
"v_mbcnt_lo_u32_b32 v144, -1, 0\n"
"v_mbcnt_hi_u32_b32 v144, -1, v144\n"
"s_mov_b64 s[56:57], s[30:31]\n"
"s_mov_b32 s58, 4\n"
"v_lshlrev_b32 v144, 4, v144\n"
"s_cmp_eq_u32 %3, 0\n"
"s_cselect_b32 s92, 0, 1073741824\n"
"buffer_atomic_add v237, v144, s[56:59], s92 offen sc1\n"
"s_add_u32 s60, s60, 1\n"
"s_and_b32 s98, s61, 0x3ffff\n"
"s_add_u32 s62, s64, s98\n"
"s_addc_u32 s63, s65, 0\n"
"s_lshr_b32 s67, s61, 18\n"
"s_mul_i32 s66, s67, 24576\n"
"s_setpc_b64 s[62:63]\n"
".p2align 12\n"
".p2align 5\n"
"L_W0A1:\n"
"s_nop 7\n"
"s_branch L_TABDONE\n"
"L_W1A1:\n"
"s_nop 7\n"
"s_branch L_W1TS2\n"
"L_W2A1:\n"
"s_nop 7\n"
"s_branch L_W2TS2\n"
"L_A1:\n"
"v_mfma_f32_16x16x32_bf16 v[144:147], a[96:99], a[0:3], v[188:191]\n"
"v_readlane_b32 s61, v235, s60\n"
"s_add_u32 s60, s60, 1\n"
"v_mfma_f32_16x16x32_bf16 v[144:147], a[100:103], a[4:7], v[144:147]\n"
"s_and_b32 s98, s61, 0x3ffff\n"
"s_add_u32 s62, s64, s98\n"
"s_addc_u32 s63, s65, 0\n"
"ds_bpermute_b32 v244, v250, v248 offset:0\n"
"v_mfma_f32_16x16x32_bf16 v[144:147], a[104:107], a[8:11], v[144:147]\n"
"s_add_u32 s77, s66, 12288\n"
"ds_bpermute_b32 v245, v250, v248 offset:4\n"
"ds_bpermute_b32 v246, v250, v249 offset:0\n"
"s_add_u32 m0, s97, 0\n"
"v_mfma_f32_16x16x32_bf16 v[144:147], a[108:111], a[12:15], v[144:147]\n"
"s_lshr_b32 s67, s61, 18\n"
"ds_bpermute_b32 v247, v250, v249 offset:4\n"
"v_add_u32 v250, 8, v250\n"
"v_mfma_f32_16x16x32_bf16 v[144:147], a[112:115], a[16:19], v[144:147]\n"
"ds_read_b128 a[144:147], v228 offset:12288\n"
"v_mov_b32 v251, v252\n"
"v_mfma_f32_16x16x32_bf16 v[144:147], a[116:119], a[20:23], v[144:147]\n"
"buffer_load_dwordx4 v234, s[40:43], s66 offen offset:0 lds\n"
"v_mfma_f32_16x16x32_bf16 v[176:179], a[120:123], a[0:3], v[188:191]\n"
"ds_read_b128 a[148:151], v229 offset:12288\n"
"s_add_u32 m0, s97, 896\n"
"v_mfma_f32_16x16x32_bf16 v[176:179], a[124:127], a[4:7], v[176:179]\n"
"ds_read_b128 a[152:155], v228 offset:13312\n"
"v_mov_b32 v252, v253\n"
"v_mfma_f32_16x16x32_bf16 v[176:179], a[128:131], a[8:11], v[176:179]\n"
"ds_read_b128 a[156:159], v229 offset:13312\n"
"v_mov_b32 v253, v254\n"
"v_mfma_f32_16x16x32_bf16 v[176:179], a[132:135], a[12:15], v[176:179]\n"
"ds_read_b128 a[160:163], v228 offset:14336\n"
"v_mfma_f32_16x16x32_bf16 v[176:179], a[136:139], a[16:19], v[176:179]\n"
"v_exp_f32 v144, v144\n"
"v_mfma_f32_16x16x32_bf16 v[176:179], a[140:143], a[20:23], v[176:179]\n"
"s_waitcnt lgkmcnt(5)\n"
"v_and_or_b32 v244, v234, s75, v244\n"
"v_and_or_b32 v245, v234, s75, v245\n"
"v_mfma_f32_16x16x32_bf16 v[152:155], a[96:99], a[24:27], v[192:195]\n"
"v_exp_f32 v145, v145\n"
"v_mfma_f32_16x16x32_bf16 v[152:155], a[100:103], a[28:31], v[152:155]\n"
"v_exp_f32 v146, v146\n"
"v_mfma_f32_16x16x32_bf16 v[152:155], a[104:107], a[32:35], v[152:155]\n"
"v_exp_f32 v147, v147\n"
"v_mfma_f32_16x16x32_bf16 v[152:155], a[108:111], a[36:39], v[152:155]\n"
"buffer_load_dwordx4 v234, s[40:43], s66 offen offset:128 lds\n"
"v_mfma_f32_16x16x32_bf16 v[152:155], a[112:115], a[40:43], v[152:155]\n"
"s_add_u32 m0, s97, 1792\n"
"ds_read_b128 a[164:167], v229 offset:14336\n"
"v_cvt_pk_bf16_f32 v144, v144, v145\n"
"v_mfma_f32_16x16x32_bf16 v[152:155], a[116:119], a[44:47], v[152:155]\n"
"v_exp_f32 v176, v176\n"
"v_mfma_f32_16x16x32_bf16 v[180:183], a[120:123], a[24:27], v[192:195]\n"
"v_exp_f32 v177, v177\n"
"v_mfma_f32_16x16x32_bf16 v[180:183], a[124:127], a[28:31], v[180:183]\n"
"v_exp_f32 v178, v178\n"
"v_mfma_f32_16x16x32_bf16 v[180:183], a[128:131], a[32:35], v[180:183]\n"
"v_exp_f32 v179, v179\n"
"v_mfma_f32_16x16x32_bf16 v[180:183], a[132:135], a[36:39], v[180:183]\n"
"v_exp_f32 v152, v152\n"
"v_mfma_f32_16x16x32_bf16 v[180:183], a[136:139], a[40:43], v[180:183]\n"
"v_exp_f32 v153, v153\n"
"v_mfma_f32_16x16x32_bf16 v[180:183], a[140:143], a[44:47], v[180:183]\n"
"v_exp_f32 v154, v154\n"
"v_mfma_f32_16x16x32_bf16 v[160:163], a[96:99], a[48:51], v[196:199]\n"
"v_exp_f32 v155, v155\n"
"v_mfma_f32_16x16x32_bf16 v[160:163], a[100:103], a[52:55], v[160:163]\n"
"buffer_load_dwordx4 v234, s[40:43], s66 offen offset:256 lds\n"
"v_mfma_f32_16x16x32_bf16 v[160:163], a[104:107], a[56:59], v[160:163]\n"
"s_add_u32 m0, s73, 0\n"
"v_cvt_pk_bf16_f32 v145, v146, v147\n"
"v_exp_f32 v180, v180\n"
"v_mfma_f32_16x16x32_bf16 v[160:163], a[108:111], a[60:63], v[160:163]\n"
"v_exp_f32 v181, v181\n"
"v_mfma_f32_16x16x32_bf16 v[160:163], a[112:115], a[64:67], v[160:163]\n"
"v_exp_f32 v182, v182\n"
"v_mfma_f32_16x16x32_bf16 v[160:163], a[116:119], a[68:71], v[160:163]\n"
"v_exp_f32 v183, v183\n"
"v_mfma_f32_16x16x32_bf16 v[184:187], a[120:123], a[48:51], v[196:199]\n"
"v_cvt_pk_bf16_f32 v146, v176, v177\n"
"v_cvt_pk_bf16_f32 v147, v178, v179\n"
"v_mfma_f32_16x16x32_bf16 v[184:187], a[124:127], a[52:55], v[184:187]\n"
"v_cvt_pk_bf16_f32 v152, v152, v153\n"
"v_cvt_pk_bf16_f32 v153, v154, v155\n"
"v_mfma_f32_16x16x32_bf16 v[184:187], a[128:131], a[56:59], v[184:187]\n"
"buffer_load_dwordx4 v244, s[48:51], 0 offen offset:0 lds\n"
"v_mfma_f32_16x16x32_bf16 v[184:187], a[132:135], a[60:63], v[184:187]\n"
"s_add_u32 m0, s73, 896\n"
"v_exp_f32 v160, v160\n"
"v_mfma_f32_16x16x32_bf16 v[184:187], a[136:139], a[64:67], v[184:187]\n"
"v_exp_f32 v161, v161\n"
"v_mfma_f32_16x16x32_bf16 v[184:187], a[140:143], a[68:71], v[184:187]\n"
"v_exp_f32 v162, v162\n"
"v_mfma_f32_16x16x32_bf16 v[168:171], a[96:99], a[72:75], v[200:203]\n"
"ds_read_b128 a[96:99], v228 offset:18432\n"
"v_exp_f32 v163, v163\n"
"v_mfma_f32_16x16x32_bf16 v[168:171], a[100:103], a[76:79], v[168:171]\n"
"ds_read_b128 a[100:103], v229 offset:18432\n"
"v_cvt_pk_bf16_f32 v154, v180, v181\n"
"v_mfma_f32_16x16x32_bf16 v[168:171], a[104:107], a[80:83], v[168:171]\n"
"v_exp_f32 v184, v184\n"
"v_mfma_f32_16x16x32_bf16 v[168:171], a[108:111], a[84:87], v[168:171]\n"
"v_exp_f32 v185, v185\n"
"v_mfma_f32_16x16x32_bf16 v[168:171], a[112:115], a[88:91], v[168:171]\n"
"v_exp_f32 v186, v186\n"
"v_mfma_f32_16x16x32_bf16 v[168:171], a[116:119], a[92:95], v[168:171]\n"
"ds_read_b128 a[104:107], v228 offset:19456\n"
"v_exp_f32 v187, v187\n"
"v_mfma_f32_16x16x32_bf16 v[176:179], a[120:123], a[72:75], v[200:203]\n"
"ds_read_b128 a[108:111], v229 offset:19456\n"
"v_cvt_pk_bf16_f32 v155, v182, v183\n"
"v_mfma_f32_16x16x32_bf16 v[176:179], a[124:127], a[76:79], v[176:179]\n"
"ds_read_b128 a[112:115], v228 offset:20480\n"
"v_cvt_pk_bf16_f32 v160, v160, v161\n"
"v_mfma_f32_16x16x32_bf16 v[176:179], a[128:131], a[80:83], v[176:179]\n"
"ds_read_b128 a[116:119], v229 offset:20480\n"
"v_cvt_pk_bf16_f32 v161, v162, v163\n"
"v_mfma_f32_16x16x32_bf16 v[176:179], a[132:135], a[84:87], v[176:179]\n"
"buffer_load_dwordx4 v244, s[48:51], 0 offen offset:128 lds\n"
"v_mfma_f32_16x16x32_bf16 v[176:179], a[136:139], a[88:91], v[176:179]\n"
"s_add_u32 m0, s73, 1792\n"
"v_exp_f32 v168, v168\n"
"v_mfma_f32_16x16x32_bf16 v[176:179], a[140:143], a[92:95], v[176:179]\n"
"s_waitcnt lgkmcnt(6)\n"
"v_exp_f32 v169, v169\n"
"v_mfma_f32_16x16x32_bf16 v[148:151], a[144:147], a[0:3], v[188:191]\n"
"v_exp_f32 v170, v170\n"
"v_mfma_f32_16x16x32_bf16 v[148:151], a[148:151], a[4:7], v[148:151]\n"
"v_exp_f32 v171, v171\n"
"v_mfma_f32_16x16x32_bf16 v[148:151], a[152:155], a[8:11], v[148:151]\n"
"buffer_load_dwordx4 v244, s[48:51], 0 offen offset:256 lds\n"
"v_mfma_f32_16x16x32_bf16 v[148:151], a[156:159], a[12:15], v[148:151]\n"
"s_add_u32 m0, s97, 12288\n"
"v_exp_f32 v176, v176\n"
"v_mfma_f32_16x16x32_bf16 v[148:151], a[160:163], a[16:19], v[148:151]\n"
"s_waitcnt vmcnt(8) lgkmcnt(0)\n"
"v_exp_f32 v177, v177\n"
"v_mfma_f32_16x16x32_bf16 v[148:151], a[164:167], a[20:23], v[148:151]\n"
"s_barrier\n"
"v_exp_f32 v178, v178\n"
"v_mfma_f32_16x16x32_bf16 v[180:183], a[96:99], a[0:3], v[188:191]\n"
"v_exp_f32 v179, v179\n"
"v_mfma_f32_16x16x32_bf16 v[180:183], a[100:103], a[4:7], v[180:183]\n"
"ds_read_b64_tr_b16 a[120:121], v230 offset:24576\n"
"v_cvt_pk_bf16_f32 v162, v184, v185\n"
"v_mfma_f32_16x16x32_bf16 v[180:183], a[104:107], a[8:11], v[180:183]\n"
"ds_read_b64_tr_b16 a[122:123], v230 offset:30720\n"
"v_cvt_pk_bf16_f32 v163, v186, v187\n"
"v_mfma_f32_16x16x32_bf16 v[180:183], a[108:111], a[12:15], v[180:183]\n"
"ds_read_b64_tr_b16 a[124:125], v230 offset:36864\n"
"v_cvt_pk_bf16_f32 v168, v168, v169\n"
"v_mfma_f32_16x16x32_bf16 v[180:183], a[112:115], a[16:19], v[180:183]\n"
"ds_read_b64_tr_b16 a[126:127], v230 offset:43008\n"
"v_cvt_pk_bf16_f32 v169, v170, v171\n"
"v_mfma_f32_16x16x32_bf16 v[180:183], a[116:119], a[20:23], v[180:183]\n"
"ds_read_b64_tr_b16 a[128:129], v231 offset:24576\n"
"v_cvt_pk_bf16_f32 v170, v176, v177\n"
"v_mfma_f32_16x16x32_bf16 v[156:159], a[144:147], a[24:27], v[192:195]\n"
"ds_read_b64_tr_b16 a[130:131], v231 offset:30720\n"
"v_cvt_pk_bf16_f32 v171, v178, v179\n"
"v_mfma_f32_16x16x32_bf16 v[156:159], a[148:151], a[28:31], v[156:159]\n"
"buffer_load_dwordx4 v234, s[40:43], s77 offen offset:0 lds\n"
"v_mfma_f32_16x16x32_bf16 v[156:159], a[152:155], a[32:35], v[156:159]\n"
"ds_read_b64_tr_b16 a[132:133], v231 offset:36864\n"
"s_add_u32 m0, s97, 13184\n"
"v_mfma_f32_16x16x32_bf16 v[156:159], a[156:159], a[36:39], v[156:159]\n"
"ds_read_b64_tr_b16 a[134:135], v231 offset:43008\n"
"v_and_or_b32 v246, v234, s75, v246\n"
"v_mfma_f32_16x16x32_bf16 v[156:159], a[160:163], a[40:43], v[156:159]\n"
"ds_read_b64_tr_b16 a[136:137], v232 offset:24576\n"
"v_and_or_b32 v247, v234, s75, v247\n"
"v_mfma_f32_16x16x32_bf16 v[156:159], a[164:167], a[44:47], v[156:159]\n"
"v_exp_f32 v148, v148\n"
"v_mfma_f32_16x16x32_bf16 v[184:187], a[96:99], a[24:27], v[192:195]\n"
"v_exp_f32 v149, v149\n"
"v_mfma_f32_16x16x32_bf16 v[184:187], a[100:103], a[28:31], v[184:187]\n"
"v_exp_f32 v150, v150\n"
"v_mfma_f32_16x16x32_bf16 v[184:187], a[104:107], a[32:35], v[184:187]\n"
"v_exp_f32 v151, v151\n"
"v_mfma_f32_16x16x32_bf16 v[184:187], a[108:111], a[36:39], v[184:187]\n"
"ds_read_b64_tr_b16 a[138:139], v232 offset:30720\n"
"v_cvt_pk_bf16_f32 v148, v148, v149\n"
"v_mfma_f32_16x16x32_bf16 v[184:187], a[112:115], a[40:43], v[184:187]\n"
"ds_read_b64_tr_b16 a[140:141], v232 offset:36864\n"
"v_cvt_pk_bf16_f32 v149, v150, v151\n"
"v_mfma_f32_16x16x32_bf16 v[184:187], a[116:119], a[44:47], v[184:187]\n"
"buffer_load_dwordx4 v234, s[40:43], s77 offen offset:128 lds\n"
"v_mfma_f32_16x16x32_bf16 v[164:167], a[144:147], a[48:51], v[196:199]\n"
"s_add_u32 m0, s97, 14080\n"
"v_exp_f32 v180, v180\n"
"v_mfma_f32_16x16x32_bf16 v[164:167], a[148:151], a[52:55], v[164:167]\n"
"v_exp_f32 v181, v181\n"
"v_mfma_f32_16x16x32_bf16 v[164:167], a[152:155], a[56:59], v[164:167]\n"
"v_exp_f32 v182, v182\n"
"v_mfma_f32_16x16x32_bf16 v[164:167], a[156:159], a[60:63], v[164:167]\n"
"v_exp_f32 v183, v183\n"
"v_mfma_f32_16x16x32_bf16 v[164:167], a[160:163], a[64:67], v[164:167]\n"
"ds_read_b64_tr_b16 a[142:143], v232 offset:43008\n"
"v_cvt_pk_bf16_f32 v150, v180, v181\n"
"v_mfma_f32_16x16x32_bf16 v[164:167], a[164:167], a[68:71], v[164:167]\n"
"v_exp_f32 v156, v156\n"
"v_mfma_f32_16x16x32_bf16 v[176:179], a[96:99], a[48:51], v[196:199]\n"
"v_exp_f32 v157, v157\n"
"v_mfma_f32_16x16x32_bf16 v[176:179], a[100:103], a[52:55], v[176:179]\n"
"v_exp_f32 v158, v158\n"
"v_mfma_f32_16x16x32_bf16 v[176:179], a[104:107], a[56:59], v[176:179]\n"
"v_exp_f32 v159, v159\n"
"v_mfma_f32_16x16x32_bf16 v[176:179], a[108:111], a[60:63], v[176:179]\n"
"buffer_load_dwordx4 v234, s[40:43], s77 offen offset:256 lds\n"
"v_mfma_f32_16x16x32_bf16 v[176:179], a[112:115], a[64:67], v[176:179]\n"
"s_add_u32 m0, s73, 3072\n"
"v_exp_f32 v184, v184\n"
"v_mfma_f32_16x16x32_bf16 v[176:179], a[116:119], a[68:71], v[176:179]\n"
"v_exp_f32 v185, v185\n"
"v_mfma_f32_16x16x32_bf16 v[172:175], a[144:147], a[72:75], v[200:203]\n"
"v_exp_f32 v186, v186\n"
"v_mfma_f32_16x16x32_bf16 v[172:175], a[148:151], a[76:79], v[172:175]\n"
"v_exp_f32 v187, v187\n"
"v_mfma_f32_16x16x32_bf16 v[172:175], a[152:155], a[80:83], v[172:175]\n"
"v_exp_f32 v164, v164\n"
"v_mfma_f32_16x16x32_bf16 v[172:175], a[156:159], a[84:87], v[172:175]\n"
"v_exp_f32 v165, v165\n"
"v_mfma_f32_16x16x32_bf16 v[172:175], a[160:163], a[88:91], v[172:175]\n"
"v_exp_f32 v166, v166\n"
"v_mfma_f32_16x16x32_bf16 v[172:175], a[164:167], a[92:95], v[172:175]\n"
"v_cvt_pk_bf16_f32 v151, v182, v183\n"
"v_exp_f32 v167, v167\n"
"v_mfma_f32_16x16x32_bf16 v[180:183], a[96:99], a[72:75], v[200:203]\n"
"ds_read_b64_tr_b16 a[144:145], v233 offset:24576\n"
"v_cvt_pk_bf16_f32 v156, v156, v157\n"
"v_mfma_f32_16x16x32_bf16 v[180:183], a[100:103], a[76:79], v[180:183]\n"
"buffer_load_dwordx4 v245, s[48:51], 0 offen offset:0 lds\n"
"v_mfma_f32_16x16x32_bf16 v[180:183], a[104:107], a[80:83], v[180:183]\n"
"ds_read_b64_tr_b16 a[146:147], v233 offset:30720\n"
"s_add_u32 m0, s73, 3968\n"
"v_mfma_f32_16x16x32_bf16 v[180:183], a[108:111], a[84:87], v[180:183]\n"
"ds_read_b64_tr_b16 a[148:149], v233 offset:36864\n"
"v_cvt_pk_bf16_f32 v157, v158, v159\n"
"v_mfma_f32_16x16x32_bf16 v[180:183], a[112:115], a[88:91], v[180:183]\n"
"ds_read_b64_tr_b16 a[150:151], v233 offset:43008\n"
"s_waitcnt lgkmcnt(4)\n"
"v_mfma_f32_16x16x32_bf16 v[180:183], a[116:119], a[92:95], v[180:183]\n"
"v_exp_f32 v176, v176\n"
"v_mfma_f32_16x16x32_bf16 v[0:3], a[120:123], v[144:147], v[0:3]\n"
"s_waitcnt vmcnt(13)\n"
"v_exp_f32 v177, v177\n"
"v_mfma_f32_16x16x32_bf16 v[0:3], a[124:127], v[148:151], v[0:3]\n"
"v_exp_f32 v178, v178\n"
"v_mfma_f32_16x16x32_bf16 v[4:7], a[132:135], v[148:151], v[4:7]\n"
"v_exp_f32 v179, v179\n"
"v_mfma_f32_16x16x32_bf16 v[4:7], a[128:131], v[144:147], v[4:7]\n"
"ds_read_b64_tr_b16 a[152:153], v230 offset:25600\n"
"v_cvt_pk_bf16_f32 v158, v184, v185\n"
"v_mfma_f32_16x16x32_bf16 v[8:11], a[136:139], v[144:147], v[8:11]\n"
"buffer_load_dwordx4 v245, s[48:51], 0 offen offset:128 lds\n"
"v_mfma_f32_16x16x32_bf16 v[8:11], a[140:143], v[148:151], v[8:11]\n"
"s_add_u32 m0, s73, 4864\n"
"ds_read_b64_tr_b16 a[154:155], v230 offset:31744\n"
"v_cvt_pk_bf16_f32 v159, v186, v187\n"
"v_smfmac_f32_16x16x64_bf16 a[216:219], a[224:227], v[144:151], v237\n"
"ds_read_b64_tr_b16 a[156:157], v230 offset:37888\n"
"v_cvt_pk_bf16_f32 v164, v164, v165\n"
"v_mfma_f32_16x16x32_bf16 v[56:59], a[140:143], v[156:159], v[56:59]\n"
"ds_read_b64_tr_b16 a[158:159], v230 offset:44032\n"
"v_cvt_pk_bf16_f32 v165, v166, v167\n"
"v_mfma_f32_16x16x32_bf16 v[56:59], a[136:139], v[152:155], v[56:59]\n"
"v_exp_f32 v172, v172\n"
"v_mfma_f32_16x16x32_bf16 v[52:55], a[128:131], v[152:155], v[52:55]\n"
"v_exp_f32 v173, v173\n"
"v_mfma_f32_16x16x32_bf16 v[52:55], a[132:135], v[156:159], v[52:55]\n"
"v_exp_f32 v174, v174\n"
"v_mfma_f32_16x16x32_bf16 v[48:51], a[124:127], v[156:159], v[48:51]\n"
"ds_read_b128 v[212:215], v238 offset:0\n"
"v_cvt_pk_bf16_f32 v166, v176, v177\n"
"v_mfma_f32_16x16x32_bf16 v[48:51], a[120:123], v[152:155], v[48:51]\n"
"ds_read_b128 v[216:219], v239 offset:0\n"
"v_cvt_pk_bf16_f32 v167, v178, v179\n"
"v_smfmac_f32_16x16x64_bf16 a[216:219], a[228:231], v[152:159], v237\n"
"buffer_load_dwordx4 v245, s[48:51], 0 offen offset:256 lds\n"
"v_mfma_f32_16x16x32_bf16 v[96:99], a[120:123], v[160:163], v[96:99]\n"
"s_add_u32 m0, s73, 6144\n"
"ds_read_b128 v[220:223], v238 offset:1024\n"
"v_cvt_pk_bf16_f32 v172, v172, v173\n"
"v_mfma_f32_16x16x32_bf16 v[96:99], a[124:127], v[164:167], v[96:99]\n"
"v_exp_f32 v175, v175\n"
"v_mfma_f32_16x16x32_bf16 v[100:103], a[132:135], v[164:167], v[100:103]\n"
"ds_read_b64_tr_b16 a[160:161], v231 offset:25600\n"
"v_cvt_pk_bf16_f32 v173, v174, v175\n"
"v_mfma_f32_16x16x32_bf16 v[100:103], a[128:131], v[160:163], v[100:103]\n"
"ds_read_b64_tr_b16 a[162:163], v231 offset:31744\n"
"v_mfma_f32_16x16x32_bf16 v[104:107], a[136:139], v[160:163], v[104:107]\n"
"v_exp_f32 v180, v180\n"
"v_mfma_f32_16x16x32_bf16 v[104:107], a[140:143], v[164:167], v[104:107]\n"
"v_exp_f32 v181, v181\n"
"v_smfmac_f32_16x16x64_bf16 a[220:223], a[224:227], v[160:167], v237\n"
"s_waitcnt lgkmcnt(2)\n"
"v_exp_f32 v182, v182\n"
"v_mfma_f32_16x16x32_bf16 v[204:207], v[212:215], a[232:235], 0\n"
"v_exp_f32 v183, v183\n"
"v_mfma_f32_16x16x32_bf16 v[204:207], v[216:219], a[236:239], v[204:207]\n"
"buffer_load_dwordx4 v246, s[48:51], 0 offen offset:0 lds\n"
"v_mfma_f32_16x16x32_bf16 v[204:207], v[220:223], a[240:243], v[204:207]\n"
"s_add_u32 m0, s73, 7040\n"
"ds_read_b64_tr_b16 a[164:165], v231 offset:37888\n"
"v_cvt_pk_bf16_f32 v174, v180, v181\n"
"v_cvt_pk_bf16_f32 v175, v182, v183\n"
"s_nop 1\n"
"v_mfma_f32_16x16x32_bf16 a[176:179], a[140:143], v[172:175], a[176:179]\n"
"ds_read_b64_tr_b16 a[166:167], v231 offset:44032\n"
"v_mfma_f32_16x16x32_bf16 a[176:179], a[136:139], v[168:171], a[176:179]\n"
"ds_read_b64_tr_b16 a[96:97], v232 offset:25600\n"
"v_mfma_f32_16x16x32_bf16 a[172:175], a[128:131], v[168:171], a[172:175]\n"
"ds_read_b128 v[212:215], v239 offset:1024\n"
"v_mfma_f32_16x16x32_bf16 a[172:175], a[132:135], v[172:175], a[172:175]\n"
"buffer_load_dwordx4 v246, s[48:51], 0 offen offset:128 lds\n"
"v_mfma_f32_16x16x32_bf16 a[168:171], a[124:127], v[172:175], a[168:171]\n"
"ds_read_b128 v[216:219], v238 offset:2048\n"
"s_add_u32 m0, s73, 7936\n"
"v_mfma_f32_16x16x32_bf16 a[168:171], a[120:123], v[168:171], a[168:171]\n"
"ds_read_b128 v[220:223], v239 offset:2048\n"
"v_smfmac_f32_16x16x64_bf16 a[220:223], a[228:231], v[168:175], v237\n"
"s_waitcnt lgkmcnt(4)\n"
"ds_read_b64_tr_b16 a[98:99], v232 offset:31744\n"
"v_mfma_f32_16x16x32_bf16 v[12:15], a[144:147], v[144:147], v[12:15]\n"
"ds_read_b64_tr_b16 a[100:101], v232 offset:37888\n"
"v_mfma_f32_16x16x32_bf16 v[12:15], a[148:151], v[148:151], v[12:15]\n"
"ds_read_b64_tr_b16 a[102:103], v232 offset:44032\n"
"v_mfma_f32_16x16x32_bf16 v[16:19], a[156:159], v[148:151], v[16:19]\n"
"ds_read_b64_tr_b16 a[104:105], v233 offset:25600\n"
"v_mfma_f32_16x16x32_bf16 v[16:19], a[152:155], v[144:147], v[16:19]\n"
"ds_read_b64_tr_b16 a[106:107], v233 offset:31744\n"
"v_mfma_f32_16x16x32_bf16 v[20:23], a[160:163], v[144:147], v[20:23]\n"
"buffer_load_dwordx4 v246, s[48:51], 0 offen offset:256 lds\n"
"v_mfma_f32_16x16x32_bf16 v[20:23], a[164:167], v[148:151], v[20:23]\n"
"ds_read_b64_tr_b16 a[108:109], v233 offset:37888\n"
"s_add_u32 m0, s73, 9216\n"
"v_mfma_f32_16x16x32_bf16 v[68:71], a[164:167], v[156:159], v[68:71]\n"
"ds_read_b64_tr_b16 a[110:111], v233 offset:44032\n"
"v_mfma_f32_16x16x32_bf16 v[68:71], a[160:163], v[152:155], v[68:71]\n"
"v_mfma_f32_16x16x32_bf16 v[64:67], a[152:155], v[152:155], v[64:67]\n"
"v_mfma_f32_16x16x32_bf16 v[64:67], a[156:159], v[156:159], v[64:67]\n"
"v_mfma_f32_16x16x32_bf16 v[60:63], a[148:151], v[156:159], v[60:63]\n"
"s_waitcnt lgkmcnt(7)\n"
"v_mfma_f32_16x16x32_bf16 v[60:63], a[144:147], v[152:155], v[60:63]\n"
"v_mfma_f32_16x16x32_bf16 v[204:207], v[212:215], a[244:247], v[204:207]\n"
"buffer_load_dwordx4 v247, s[48:51], 0 offen offset:0 lds\n"
"v_mfma_f32_16x16x32_bf16 v[204:207], v[216:219], a[248:251], v[204:207]\n"
"ds_read_b128 v[212:215], v238 offset:6144\n"
"s_add_u32 m0, s73, 10112\n"
"v_mfma_f32_16x16x32_bf16 v[204:207], v[220:223], a[252:255], v[204:207]\n"
"ds_read_b128 v[216:219], v239 offset:6144\n"
"v_mfma_f32_16x16x32_bf16 v[108:111], a[144:147], v[160:163], v[108:111]\n"
"ds_read_b128 v[220:223], v238 offset:7168\n"
"v_mfma_f32_16x16x32_bf16 v[108:111], a[148:151], v[164:167], v[108:111]\n"
"v_mfma_f32_16x16x32_bf16 v[112:115], a[156:159], v[164:167], v[112:115]\n"
"v_mfma_f32_16x16x32_bf16 v[112:115], a[152:155], v[160:163], v[112:115]\n"
"buffer_load_dwordx4 v247, s[48:51], 0 offen offset:128 lds\n"
"v_mfma_f32_16x16x32_bf16 v[116:119], a[160:163], v[160:163], v[116:119]\n"
"s_waitcnt vmcnt(14) lgkmcnt(3)\n"
"s_barrier\n"
"ds_read_b64_tr_b16 a[112:113], v230 offset:26624\n"
"v_mfma_f32_16x16x32_bf16 v[116:119], a[164:167], v[164:167], v[116:119]\n"
"ds_read_b64_tr_b16 a[114:115], v230 offset:32768\n"
"s_add_u32 m0, s97, 24576\n"
"v_mfma_f32_16x16x32_bf16 a[188:191], a[164:167], v[172:175], a[188:191]\n"
"ds_read_b64_tr_b16 a[116:117], v230 offset:38912\n"
"v_cndmask_b32 v204, v204, v205, s[82:83]\n"
"v_mfma_f32_16x16x32_bf16 a[188:191], a[160:163], v[168:171], a[188:191]\n"
"ds_read_b64_tr_b16 a[118:119], v230 offset:45056\n"
"v_cndmask_b32 v206, v206, v207, s[82:83]\n"
"v_mfma_f32_16x16x32_bf16 a[184:187], a[152:155], v[168:171], a[184:187]\n"
"buffer_load_dwordx4 v234, s[44:47], s66 offen offset:0 lds\n"
"v_mfma_f32_16x16x32_bf16 a[184:187], a[156:159], v[172:175], a[184:187]\n"
"s_add_u32 m0, s97, 36864\n"
"ds_read_b64_tr_b16 a[164:165], v233 offset:38912\n"
"v_cndmask_b32 v204, v204, v206, s[84:85]\n"
"v_mfma_f32_16x16x32_bf16 a[180:183], a[148:151], v[172:175], a[180:183]\n"
"ds_read_b64_tr_b16 a[166:167], v233 offset:45056\n"
"v_mfma_f32_16x16x32_bf16 a[180:183], a[144:147], v[168:171], a[180:183]\n"
"ds_read_b64_tr_b16 a[160:161], v233 offset:26624\n"
"v_mfma_f32_16x16x32_bf16 v[24:27], a[96:99], v[144:147], v[24:27]\n"
"ds_read_b64_tr_b16 a[162:163], v233 offset:32768\n"
"v_mfma_f32_16x16x32_bf16 v[24:27], a[100:103], v[148:151], v[24:27]\n"
"ds_read_b64_tr_b16 a[152:153], v232 offset:26624\n"
"v_mfma_f32_16x16x32_bf16 v[28:31], a[108:111], v[148:151], v[28:31]\n"
"ds_read_b64_tr_b16 a[154:155], v232 offset:32768\n"
"v_mfma_f32_16x16x32_bf16 v[28:31], a[104:107], v[144:147], v[28:31]\n"
"s_waitcnt lgkmcnt(6)\n"
"ds_read_b64_tr_b16 a[156:157], v232 offset:38912\n"
"v_mfma_f32_16x16x32_bf16 v[32:35], a[112:115], v[144:147], v[32:35]\n"
"buffer_load_dwordx4 v234, s[44:47], s77 offen offset:0 lds\n"
"v_mfma_f32_16x16x32_bf16 v[32:35], a[116:119], v[148:151], v[32:35]\n"
"s_add_u32 m0, s97, 25472\n"
"ds_read_b64_tr_b16 a[158:159], v232 offset:45056\n"
"v_mfma_f32_16x16x32_bf16 v[208:211], v[212:215], a[232:235], 0\n"
"ds_read_b128 v[212:215], v239 offset:7168\n"
"v_mfma_f32_16x16x32_bf16 v[208:211], v[216:219], a[236:239], v[208:211]\n"
"ds_read_b128 v[216:219], v238 offset:8192\n"
"v_subrev_u32 v238, s74, v238\n"
"v_mfma_f32_16x16x32_bf16 v[208:211], v[220:223], a[240:243], v[208:211]\n"
"ds_read_b128 v[220:223], v239 offset:8192\n"
"v_subrev_u32 v239, s74, v239\n"
"v_mfma_f32_16x16x32_bf16 v[80:83], a[116:119], v[156:159], v[80:83]\n"
"ds_read_b64_tr_b16 a[148:149], v231 offset:38912\n"
"v_mfma_f32_16x16x32_bf16 v[80:83], a[112:115], v[152:155], v[80:83]\n"
"ds_read_b64_tr_b16 a[150:151], v231 offset:45056\n"
"v_mfma_f32_16x16x32_bf16 v[76:79], a[104:107], v[152:155], v[76:79]\n"
"ds_read_b64_tr_b16 a[144:145], v231 offset:26624\n"
"v_mfma_f32_16x16x32_bf16 v[76:79], a[108:111], v[156:159], v[76:79]\n"
"ds_read_b64_tr_b16 a[146:147], v231 offset:32768\n"
"v_mfma_f32_16x16x32_bf16 v[72:75], a[100:103], v[156:159], v[72:75]\n"
"buffer_load_dwordx4 v234, s[44:47], s66 offen offset:128 lds\n"
"v_mfma_f32_16x16x32_bf16 v[72:75], a[96:99], v[152:155], v[72:75]\n"
"s_add_u32 m0, s73, 11008\n"
"ds_read_b128 a[120:123], v228 offset:6144\n"
"s_add_u32 s73, s73, s74\n"
"v_mfma_f32_16x16x32_bf16 v[120:123], a[96:99], v[160:163], v[120:123]\n"
"ds_read_b128 a[124:127], v229 offset:6144\n"
"s_sub_u32 s74, 0, s74\n"
"v_mfma_f32_16x16x32_bf16 v[120:123], a[100:103], v[164:167], v[120:123]\n"
"ds_read_b128 a[128:131], v228 offset:7168\n"
"v_mfma_f32_16x16x32_bf16 v[124:127], a[108:111], v[164:167], v[124:127]\n"
"ds_read_b128 a[132:135], v229 offset:7168\n"
"v_mfma_f32_16x16x32_bf16 v[124:127], a[104:107], v[160:163], v[124:127]\n"
"buffer_load_dwordx4 v247, s[48:51], 0 offen offset:256 lds\n"
"v_mfma_f32_16x16x32_bf16 v[128:131], a[112:115], v[160:163], v[128:131]\n"
"s_waitcnt lgkmcnt(8)\n"
"s_add_u32 m0, s97, 37760\n"
"ds_read_b128 a[136:139], v228 offset:8192\n"
"v_mfma_f32_16x16x32_bf16 v[128:131], a[116:119], v[164:167], v[128:131]\n"
"ds_read_b128 a[140:143], v229 offset:8192\n"
"v_mfma_f32_16x16x32_bf16 v[208:211], v[212:215], a[244:247], v[208:211]\n"
"v_mfma_f32_16x16x32_bf16 v[208:211], v[216:219], a[248:251], v[208:211]\n"
"v_mfma_f32_16x16x32_bf16 v[208:211], v[220:223], a[252:255], v[208:211]\n"
"v_mfma_f32_16x16x32_bf16 a[200:203], a[116:119], v[172:175], a[200:203]\n"
"buffer_load_dwordx4 v234, s[44:47], s77 offen offset:128 lds\n"
"v_mfma_f32_16x16x32_bf16 a[200:203], a[112:115], v[168:171], a[200:203]\n"
"ds_read_b128 a[116:119], v229 offset:2048\n"
"s_add_u32 m0, s97, 26368\n"
"v_mfma_f32_16x16x32_bf16 a[196:199], a[104:107], v[168:171], a[196:199]\n"
"ds_read_b128 a[112:115], v228 offset:2048\n"
"v_mfma_f32_16x16x32_bf16 a[196:199], a[108:111], v[172:175], a[196:199]\n"
"ds_read_b128 a[104:107], v228 offset:1024\n"
"v_cndmask_b32 v208, v208, v209, s[82:83]\n"
"v_mfma_f32_16x16x32_bf16 a[192:195], a[100:103], v[172:175], a[192:195]\n"
"s_waitcnt lgkmcnt(9)\n"
"ds_read_b128 a[108:111], v229 offset:1024\n"
"v_cndmask_b32 v210, v210, v211, s[82:83]\n"
"v_mfma_f32_16x16x32_bf16 a[192:195], a[96:99], v[168:171], a[192:195]\n"
"ds_read_b128 a[100:103], v229 offset:0\n"
"v_cndmask_b32 v208, v208, v210, s[84:85]\n"
"v_mfma_f32_16x16x32_bf16 v[36:39], a[144:147], v[144:147], v[36:39]\n"
"v_max_f32 v224, v204, v208\n"
"v_sub_f32 v204, v204, v224\n"
"v_mfma_f32_16x16x32_bf16 v[36:39], a[148:151], v[148:151], v[36:39]\n"
"ds_read_b128 a[96:99], v228 offset:0\n"
"v_sub_f32 v208, v208, v224\n"
"v_mfma_f32_16x16x32_bf16 v[40:43], a[156:159], v[148:151], v[40:43]\n"
"v_exp_f32 v204, v204\n"
"v_mfma_f32_16x16x32_bf16 v[40:43], a[152:155], v[144:147], v[40:43]\n"
"v_exp_f32 v208, v208\n"
"v_mfma_f32_16x16x32_bf16 v[44:47], a[160:163], v[144:147], v[44:47]\n"
"v_add_f32 v224, v204, v208\n"
"v_mfma_f32_16x16x32_bf16 v[44:47], a[164:167], v[148:151], v[44:47]\n"
"v_rcp_f32 v224, v224\n"
"v_mfma_f32_16x16x32_bf16 v[92:95], a[164:167], v[156:159], v[92:95]\n"
"v_mul_f32 v224, s80, v224\n"
"v_mul_f32 v204, v204, v224\n"
"v_mfma_f32_16x16x32_bf16 v[92:95], a[160:163], v[152:155], v[92:95]\n"
"v_mul_f32 v208, v208, v224\n"
"v_cvt_pk_bf16_f32 v254, v204, v208\n"
"v_mfma_f32_16x16x32_bf16 v[88:91], a[152:155], v[152:155], v[88:91]\n"
"v_mfma_f32_16x16x32_bf16 v[88:91], a[156:159], v[156:159], v[88:91]\n"
"v_mfma_f32_16x16x32_bf16 v[84:87], a[148:151], v[156:159], v[84:87]\n"
"s_waitcnt vmcnt(13) lgkmcnt(0)\n"
"s_barrier\n"
"v_mfma_f32_16x16x32_bf16 v[84:87], a[144:147], v[152:155], v[84:87]\n"
"v_mfma_f32_16x16x32_bf16 v[132:135], a[144:147], v[160:163], v[132:135]\n"
"buffer_load_dwordx4 v234, s[44:47], s66 offen offset:256 lds\n"
"v_mfma_f32_16x16x32_bf16 v[132:135], a[148:151], v[164:167], v[132:135]\n"
"s_add_u32 m0, s97, 38656\n"
"s_mul_i32 s66, s67, 24576\n"
"v_mfma_f32_16x16x32_bf16 v[136:139], a[156:159], v[164:167], v[136:139]\n"
"v_mfma_f32_16x16x32_bf16 v[136:139], a[152:155], v[160:163], v[136:139]\n"
"v_mfma_f32_16x16x32_bf16 v[140:143], a[160:163], v[160:163], v[140:143]\n"
"v_mfma_f32_16x16x32_bf16 v[140:143], a[164:167], v[164:167], v[140:143]\n"
"v_mfma_f32_16x16x32_bf16 a[212:215], a[164:167], v[172:175], a[212:215]\n"
"v_mfma_f32_16x16x32_bf16 a[212:215], a[160:163], v[168:171], a[212:215]\n"
"v_mfma_f32_16x16x32_bf16 a[208:211], a[152:155], v[168:171], a[208:211]\n"
"buffer_load_dwordx4 v234, s[44:47], s77 offen offset:256 lds\n"
"v_mfma_f32_16x16x32_bf16 a[208:211], a[156:159], v[172:175], a[208:211]\n"
"v_mfma_f32_16x16x32_bf16 a[204:207], a[148:151], v[172:175], a[204:207]\n"
"v_mfma_f32_16x16x32_bf16 a[204:207], a[144:147], v[168:171], a[204:207]\n"
"s_setpc_b64 s[62:63]\n"
"L_TS1:\n"
"v_accvgpr_mov_b32 a232, a24\n"
"v_accvgpr_mov_b32 a233, a25\n"
"v_accvgpr_mov_b32 a234, a26\n"
"v_accvgpr_mov_b32 a235, a27\n"
"v_accvgpr_mov_b32 a236, a28\n"
"v_accvgpr_mov_b32 a237, a29\n"
"v_accvgpr_mov_b32 a238, a30\n"
"v_accvgpr_mov_b32 a239, a31\n"
"v_accvgpr_mov_b32 a240, a32\n"
"v_accvgpr_mov_b32 a241, a33\n"
"v_accvgpr_mov_b32 a242, a34\n"
"v_accvgpr_mov_b32 a243, a35\n"
"v_accvgpr_mov_b32 a244, a36\n"
"v_accvgpr_mov_b32 a245, a37\n"
"v_accvgpr_mov_b32 a246, a38\n"
"v_accvgpr_mov_b32 a247, a39\n"
"v_accvgpr_mov_b32 a248, a40\n"
"v_accvgpr_mov_b32 a249, a41\n"
"v_accvgpr_mov_b32 a250, a42\n"
"v_accvgpr_mov_b32 a251, a43\n"
"v_accvgpr_mov_b32 a252, a44\n"
"v_accvgpr_mov_b32 a253, a45\n"
"v_accvgpr_mov_b32 a254, a46\n"
"v_accvgpr_mov_b32 a255, a47\n"
"s_branch L_A1\n"
".p2align 5\n"
"L_W1TS2:\n"
"s_nop 7\n"
"s_branch L_W1TS3\n"
"L_W2TS2:\n"
"s_nop 7\n"
"s_branch L_W2TS3\n"
"L_TS2:\n"
"v_accvgpr_mov_b32 a232, a48\n"
"v_accvgpr_mov_b32 a233, a49\n"
"v_accvgpr_mov_b32 a234, a50\n"
"v_accvgpr_mov_b32 a235, a51\n"
"v_accvgpr_mov_b32 a236, a52\n"
"v_accvgpr_mov_b32 a237, a53\n"
"v_accvgpr_mov_b32 a238, a54\n"
"v_accvgpr_mov_b32 a239, a55\n"
"v_accvgpr_mov_b32 a240, a56\n"
"v_accvgpr_mov_b32 a241, a57\n"
"v_accvgpr_mov_b32 a242, a58\n"
"v_accvgpr_mov_b32 a243, a59\n"
"v_accvgpr_mov_b32 a244, a60\n"
"v_accvgpr_mov_b32 a245, a61\n"
"v_accvgpr_mov_b32 a246, a62\n"
"v_accvgpr_mov_b32 a247, a63\n"
"v_accvgpr_mov_b32 a248, a64\n"
"v_accvgpr_mov_b32 a249, a65\n"
"v_accvgpr_mov_b32 a250, a66\n"
"v_accvgpr_mov_b32 a251, a67\n"
"v_accvgpr_mov_b32 a252, a68\n"
"v_accvgpr_mov_b32 a253, a69\n"
"v_accvgpr_mov_b32 a254, a70\n"
"v_accvgpr_mov_b32 a255, a71\n"
"s_branch L_A1\n"
".p2align 5\n"
"L_W1TS3:\n"
"s_nop 7\n"
"s_branch L_TABDONE\n"
"L_W2TS3:\n"
"s_nop 7\n"
"s_branch L_TABDONE\n"
"L_TS3:\n"
"s_mov_b64 s[48:49], s[20:21]\n"
"v_subrev_u32 v250, 32, v250\n"
"v_accvgpr_mov_b32 a232, a72\n"
"v_accvgpr_mov_b32 a233, a73\n"
"v_accvgpr_mov_b32 a234, a74\n"
"v_accvgpr_mov_b32 a235, a75\n"
"v_accvgpr_mov_b32 a236, a76\n"
"v_accvgpr_mov_b32 a237, a77\n"
"v_accvgpr_mov_b32 a238, a78\n"
"v_accvgpr_mov_b32 a239, a79\n"
"v_accvgpr_mov_b32 a240, a80\n"
"v_accvgpr_mov_b32 a241, a81\n"
"v_accvgpr_mov_b32 a242, a82\n"
"v_accvgpr_mov_b32 a243, a83\n"
"v_accvgpr_mov_b32 a244, a84\n"
"v_accvgpr_mov_b32 a245, a85\n"
"v_accvgpr_mov_b32 a246, a86\n"
"v_accvgpr_mov_b32 a247, a87\n"
"v_accvgpr_mov_b32 a248, a88\n"
"v_accvgpr_mov_b32 a249, a89\n"
"v_accvgpr_mov_b32 a250, a90\n"
"v_accvgpr_mov_b32 a251, a91\n"
"v_accvgpr_mov_b32 a252, a92\n"
"v_accvgpr_mov_b32 a253, a93\n"
"v_accvgpr_mov_b32 a254, a94\n"
"v_accvgpr_mov_b32 a255, a95\n"
"s_branch L_A1\n"
".p2align 12\n"
".p2align 5\n"
"L_W0B1:\n"
"s_nop 7\n"
"s_branch L_W0TG2\n"
"L_B1:\n"
"v_mfma_f32_16x16x32_bf16 v[144:147], a[96:99], a[0:3], v[188:191]\n"
"v_readlane_b32 s61, v235, s60\n"
"s_add_u32 s60, s60, 1\n"
"v_mfma_f32_16x16x32_bf16 v[144:147], a[100:103], a[4:7], v[144:147]\n"
"s_and_b32 s98, s61, 0x3ffff\n"
"s_add_u32 s62, s64, s98\n"
"s_addc_u32 s63, s65, 0\n"
"ds_bpermute_b32 v244, v250, v248 offset:0\n"
"v_mfma_f32_16x16x32_bf16 v[144:147], a[104:107], a[8:11], v[144:147]\n"
"s_add_u32 s77, s66, 12288\n"
"ds_bpermute_b32 v245, v250, v248 offset:4\n"
"ds_bpermute_b32 v246, v250, v249 offset:0\n"
"s_add_u32 m0, s97, 0\n"
"v_mfma_f32_16x16x32_bf16 v[144:147], a[108:111], a[12:15], v[144:147]\n"
"s_lshr_b32 s67, s61, 18\n"
"ds_bpermute_b32 v247, v250, v249 offset:4\n"
"ds_read_b128 a[144:147], v228 offset:12288\n"
"v_add_u32 v250, 8, v250\n"
"v_mfma_f32_16x16x32_bf16 v[144:147], a[112:115], a[16:19], v[144:147]\n"
"ds_read_b128 a[148:151], v229 offset:12288\n"
"v_mov_b32 v251, v252\n"
"v_mfma_f32_16x16x32_bf16 v[144:147], a[116:119], a[20:23], v[144:147]\n"
"buffer_load_dwordx4 v234, s[40:43], s66 offen offset:0 lds\n"
"v_mfma_f32_16x16x32_bf16 v[176:179], a[120:123], a[0:3], v[188:191]\n"
"ds_read_b128 a[152:155], v228 offset:13312\n"
"s_add_u32 m0, s97, 896\n"
"v_mfma_f32_16x16x32_bf16 v[176:179], a[124:127], a[4:7], v[176:179]\n"
"ds_read_b128 a[156:159], v229 offset:13312\n"
"v_mov_b32 v252, v253\n"
"v_mfma_f32_16x16x32_bf16 v[176:179], a[128:131], a[8:11], v[176:179]\n"
"ds_read_b128 a[160:163], v228 offset:14336\n"
"v_mov_b32 v253, v254\n"
"v_mfma_f32_16x16x32_bf16 v[176:179], a[132:135], a[12:15], v[176:179]\n"
"ds_read_b128 a[164:167], v229 offset:14336\n"
"v_mfma_f32_16x16x32_bf16 v[176:179], a[136:139], a[16:19], v[176:179]\n"
"v_exp_f32 v144, v144\n"
"v_mfma_f32_16x16x32_bf16 v[176:179], a[140:143], a[20:23], v[176:179]\n"
"s_waitcnt lgkmcnt(6)\n"
"v_and_or_b32 v244, v234, s75, v244\n"
"v_and_or_b32 v245, v234, s75, v245\n"
"v_and_or_b32 v246, v234, s75, v246\n"
"v_mfma_f32_16x16x32_bf16 v[152:155], a[96:99], a[24:27], v[192:195]\n"
"v_exp_f32 v145, v145\n"
"v_mfma_f32_16x16x32_bf16 v[152:155], a[100:103], a[28:31], v[152:155]\n"
"v_exp_f32 v146, v146\n"
"v_mfma_f32_16x16x32_bf16 v[152:155], a[104:107], a[32:35], v[152:155]\n"
"v_exp_f32 v147, v147\n"
"v_mfma_f32_16x16x32_bf16 v[152:155], a[108:111], a[36:39], v[152:155]\n"
"buffer_load_dwordx4 v234, s[40:43], s66 offen offset:128 lds\n"
"v_mfma_f32_16x16x32_bf16 v[152:155], a[112:115], a[40:43], v[152:155]\n"
"s_add_u32 m0, s97, 1792\n"
"v_exp_f32 v176, v176\n"
"v_mfma_f32_16x16x32_bf16 v[152:155], a[116:119], a[44:47], v[152:155]\n"
"v_exp_f32 v177, v177\n"
"v_mfma_f32_16x16x32_bf16 v[180:183], a[120:123], a[24:27], v[192:195]\n"
"v_exp_f32 v178, v178\n"
"v_mfma_f32_16x16x32_bf16 v[180:183], a[124:127], a[28:31], v[180:183]\n"
"v_exp_f32 v179, v179\n"
"v_mfma_f32_16x16x32_bf16 v[180:183], a[128:131], a[32:35], v[180:183]\n"
"v_cvt_pk_bf16_f32 v144, v144, v145\n"
"v_cvt_pk_bf16_f32 v145, v146, v147\n"
"v_mfma_f32_16x16x32_bf16 v[180:183], a[132:135], a[36:39], v[180:183]\n"
"v_exp_f32 v152, v152\n"
"v_mfma_f32_16x16x32_bf16 v[180:183], a[136:139], a[40:43], v[180:183]\n"
"v_exp_f32 v153, v153\n"
"v_mfma_f32_16x16x32_bf16 v[180:183], a[140:143], a[44:47], v[180:183]\n"
"v_exp_f32 v154, v154\n"
"v_mfma_f32_16x16x32_bf16 v[160:163], a[96:99], a[48:51], v[196:199]\n"
"v_exp_f32 v155, v155\n"
"v_mfma_f32_16x16x32_bf16 v[160:163], a[100:103], a[52:55], v[160:163]\n"
"buffer_load_dwordx4 v234, s[40:43], s66 offen offset:256 lds\n"
"v_mfma_f32_16x16x32_bf16 v[160:163], a[104:107], a[56:59], v[160:163]\n"
"s_add_u32 m0, s73, 0\n"
"v_cvt_pk_bf16_f32 v146, v176, v177\n"
"v_exp_f32 v180, v180\n"
"v_mfma_f32_16x16x32_bf16 v[160:163], a[108:111], a[60:63], v[160:163]\n"
"v_exp_f32 v181, v181\n"
"v_mfma_f32_16x16x32_bf16 v[160:163], a[112:115], a[64:67], v[160:163]\n"
"v_exp_f32 v182, v182\n"
"v_mfma_f32_16x16x32_bf16 v[160:163], a[116:119], a[68:71], v[160:163]\n"
"v_exp_f32 v183, v183\n"
"v_mfma_f32_16x16x32_bf16 v[184:187], a[120:123], a[48:51], v[196:199]\n"
"v_cvt_pk_bf16_f32 v147, v178, v179\n"
"v_cvt_pk_bf16_f32 v152, v152, v153\n"
"v_mfma_f32_16x16x32_bf16 v[184:187], a[124:127], a[52:55], v[184:187]\n"
"v_cvt_pk_bf16_f32 v153, v154, v155\n"
"v_cvt_pk_bf16_f32 v154, v180, v181\n"
"v_mfma_f32_16x16x32_bf16 v[184:187], a[128:131], a[56:59], v[184:187]\n"
"buffer_load_dwordx4 v244, s[48:51], 0 offen offset:0 lds\n"
"v_mfma_f32_16x16x32_bf16 v[184:187], a[132:135], a[60:63], v[184:187]\n"
"s_add_u32 m0, s73, 896\n"
"v_exp_f32 v160, v160\n"
"v_mfma_f32_16x16x32_bf16 v[184:187], a[136:139], a[64:67], v[184:187]\n"
"v_exp_f32 v161, v161\n"
"v_mfma_f32_16x16x32_bf16 v[184:187], a[140:143], a[68:71], v[184:187]\n"
"v_exp_f32 v162, v162\n"
"v_mfma_f32_16x16x32_bf16 v[168:171], a[96:99], a[72:75], v[200:203]\n"
"ds_read_b128 a[96:99], v228 offset:18432\n"
"v_exp_f32 v163, v163\n"
"v_mfma_f32_16x16x32_bf16 v[168:171], a[100:103], a[76:79], v[168:171]\n"
"ds_read_b128 a[100:103], v229 offset:18432\n"
"v_cvt_pk_bf16_f32 v155, v182, v183\n"
"v_mfma_f32_16x16x32_bf16 v[168:171], a[104:107], a[80:83], v[168:171]\n"
"v_exp_f32 v184, v184\n"
"v_mfma_f32_16x16x32_bf16 v[168:171], a[108:111], a[84:87], v[168:171]\n"
"v_exp_f32 v185, v185\n"
"v_mfma_f32_16x16x32_bf16 v[168:171], a[112:115], a[88:91], v[168:171]\n"
"ds_read_b128 a[104:107], v228 offset:19456\n"
"v_exp_f32 v186, v186\n"
"v_mfma_f32_16x16x32_bf16 v[168:171], a[116:119], a[92:95], v[168:171]\n"
"ds_read_b128 a[108:111], v229 offset:19456\n"
"v_exp_f32 v187, v187\n"
"v_mfma_f32_16x16x32_bf16 v[176:179], a[120:123], a[72:75], v[200:203]\n"
"ds_read_b128 a[112:115], v228 offset:20480\n"
"v_cvt_pk_bf16_f32 v160, v160, v161\n"
"v_mfma_f32_16x16x32_bf16 v[176:179], a[124:127], a[76:79], v[176:179]\n"
"ds_read_b128 a[116:119], v229 offset:20480\n"
"v_cvt_pk_bf16_f32 v161, v162, v163\n"
"v_mfma_f32_16x16x32_bf16 v[176:179], a[128:131], a[80:83], v[176:179]\n"
"buffer_load_dwordx4 v244, s[48:51], 0 offen offset:128 lds\n"
"v_mfma_f32_16x16x32_bf16 v[176:179], a[132:135], a[84:87], v[176:179]\n"
"s_add_u32 m0, s73, 1792\n"
"v_exp_f32 v168, v168\n"
"v_mfma_f32_16x16x32_bf16 v[176:179], a[136:139], a[88:91], v[176:179]\n"
"s_waitcnt lgkmcnt(6)\n"
"v_exp_f32 v169, v169\n"
"v_mfma_f32_16x16x32_bf16 v[176:179], a[140:143], a[92:95], v[176:179]\n"
"v_exp_f32 v170, v170\n"
"v_mfma_f32_16x16x32_bf16 v[148:151], a[144:147], a[0:3], v[188:191]\n"
"v_exp_f32 v171, v171\n"
"v_mfma_f32_16x16x32_bf16 v[148:151], a[148:151], a[4:7], v[148:151]\n"
"v_cvt_pk_bf16_f32 v162, v184, v185\n"
"v_cvt_pk_bf16_f32 v163, v186, v187\n"
"v_mfma_f32_16x16x32_bf16 v[148:151], a[152:155], a[8:11], v[148:151]\n"
"buffer_load_dwordx4 v244, s[48:51], 0 offen offset:256 lds\n"
"v_mfma_f32_16x16x32_bf16 v[148:151], a[156:159], a[12:15], v[148:151]\n"
"s_add_u32 m0, s97, 12288\n"
"v_exp_f32 v176, v176\n"
"v_mfma_f32_16x16x32_bf16 v[148:151], a[160:163], a[16:19], v[148:151]\n"
"s_waitcnt vmcnt(14) lgkmcnt(0)\n"
"v_exp_f32 v177, v177\n"
"v_mfma_f32_16x16x32_bf16 v[148:151], a[164:167], a[20:23], v[148:151]\n"
"s_barrier\n"
"v_exp_f32 v178, v178\n"
"v_mfma_f32_16x16x32_bf16 v[180:183], a[96:99], a[0:3], v[188:191]\n"
"ds_read_b64_tr_b16 a[120:121], v230 offset:24576\n"
"v_exp_f32 v179, v179\n"
"v_mfma_f32_16x16x32_bf16 v[180:183], a[100:103], a[4:7], v[180:183]\n"
"ds_read_b64_tr_b16 a[122:123], v230 offset:30720\n"
"v_cvt_pk_bf16_f32 v168, v168, v169\n"
"v_mfma_f32_16x16x32_bf16 v[180:183], a[104:107], a[8:11], v[180:183]\n"
"ds_read_b64_tr_b16 a[124:125], v230 offset:36864\n"
"v_cvt_pk_bf16_f32 v169, v170, v171\n"
"v_mfma_f32_16x16x32_bf16 v[180:183], a[108:111], a[12:15], v[180:183]\n"
"ds_read_b64_tr_b16 a[126:127], v230 offset:43008\n"
"v_cvt_pk_bf16_f32 v170, v176, v177\n"
"v_mfma_f32_16x16x32_bf16 v[180:183], a[112:115], a[16:19], v[180:183]\n"
"ds_read_b64_tr_b16 a[128:129], v231 offset:24576\n"
"v_cvt_pk_bf16_f32 v171, v178, v179\n"
"v_mfma_f32_16x16x32_bf16 v[180:183], a[116:119], a[20:23], v[180:183]\n"
"ds_read_b64_tr_b16 a[130:131], v231 offset:30720\n"
"v_and_or_b32 v247, v234, s75, v247\n"
"v_mfma_f32_16x16x32_bf16 v[156:159], a[144:147], a[24:27], v[192:195]\n"
"ds_read_b64_tr_b16 a[132:133], v231 offset:36864\n"
"v_mfma_f32_16x16x32_bf16 v[156:159], a[148:151], a[28:31], v[156:159]\n"
"buffer_load_dwordx4 v234, s[40:43], s77 offen offset:0 lds\n"
"v_mfma_f32_16x16x32_bf16 v[156:159], a[152:155], a[32:35], v[156:159]\n"
"ds_read_b64_tr_b16 a[134:135], v231 offset:43008\n"
"s_add_u32 m0, s97, 13184\n"
"v_mfma_f32_16x16x32_bf16 v[156:159], a[156:159], a[36:39], v[156:159]\n"
"ds_read_b64_tr_b16 a[136:137], v232 offset:24576\n"
"v_mfma_f32_16x16x32_bf16 v[156:159], a[160:163], a[40:43], v[156:159]\n"
"ds_read_b64_tr_b16 a[138:139], v232 offset:30720\n"
"v_mfma_f32_16x16x32_bf16 v[156:159], a[164:167], a[44:47], v[156:159]\n"
"v_exp_f32 v148, v148\n"
"v_mfma_f32_16x16x32_bf16 v[184:187], a[96:99], a[24:27], v[192:195]\n"
"v_exp_f32 v149, v149\n"
"v_mfma_f32_16x16x32_bf16 v[184:187], a[100:103], a[28:31], v[184:187]\n"
"v_exp_f32 v150, v150\n"
"v_mfma_f32_16x16x32_bf16 v[184:187], a[104:107], a[32:35], v[184:187]\n"
"v_exp_f32 v151, v151\n"
"v_mfma_f32_16x16x32_bf16 v[184:187], a[108:111], a[36:39], v[184:187]\n"
"ds_read_b64_tr_b16 a[140:141], v232 offset:36864\n"
"v_cvt_pk_bf16_f32 v148, v148, v149\n"
"v_mfma_f32_16x16x32_bf16 v[184:187], a[112:115], a[40:43], v[184:187]\n"
"ds_read_b64_tr_b16 a[142:143], v232 offset:43008\n"
"v_cvt_pk_bf16_f32 v149, v150, v151\n"
"v_mfma_f32_16x16x32_bf16 v[184:187], a[116:119], a[44:47], v[184:187]\n"
"buffer_load_dwordx4 v234, s[40:43], s77 offen offset:128 lds\n"
"v_mfma_f32_16x16x32_bf16 v[164:167], a[144:147], a[48:51], v[196:199]\n"
"s_add_u32 m0, s97, 14080\n"
"v_exp_f32 v180, v180\n"
"v_mfma_f32_16x16x32_bf16 v[164:167], a[148:151], a[52:55], v[164:167]\n"
"v_exp_f32 v181, v181\n"
"v_mfma_f32_16x16x32_bf16 v[164:167], a[152:155], a[56:59], v[164:167]\n"
"v_exp_f32 v182, v182\n"
"v_mfma_f32_16x16x32_bf16 v[164:167], a[156:159], a[60:63], v[164:167]\n"
"v_exp_f32 v183, v183\n"
"v_mfma_f32_16x16x32_bf16 v[164:167], a[160:163], a[64:67], v[164:167]\n"
"v_exp_f32 v156, v156\n"
"v_mfma_f32_16x16x32_bf16 v[164:167], a[164:167], a[68:71], v[164:167]\n"
"v_exp_f32 v157, v157\n"
"v_mfma_f32_16x16x32_bf16 v[176:179], a[96:99], a[48:51], v[196:199]\n"
"v_exp_f32 v158, v158\n"
"v_mfma_f32_16x16x32_bf16 v[176:179], a[100:103], a[52:55], v[176:179]\n"
"v_exp_f32 v159, v159\n"
"v_mfma_f32_16x16x32_bf16 v[176:179], a[104:107], a[56:59], v[176:179]\n"
"v_exp_f32 v184, v184\n"
"v_mfma_f32_16x16x32_bf16 v[176:179], a[108:111], a[60:63], v[176:179]\n"
"buffer_load_dwordx4 v234, s[40:43], s77 offen offset:256 lds\n"
"v_mfma_f32_16x16x32_bf16 v[176:179], a[112:115], a[64:67], v[176:179]\n"
"s_add_u32 m0, s73, 3072\n"
"v_exp_f32 v185, v185\n"
"v_mfma_f32_16x16x32_bf16 v[176:179], a[116:119], a[68:71], v[176:179]\n"
"v_exp_f32 v186, v186\n"
"v_mfma_f32_16x16x32_bf16 v[172:175], a[144:147], a[72:75], v[200:203]\n"
"v_exp_f32 v187, v187\n"
"v_mfma_f32_16x16x32_bf16 v[172:175], a[148:151], a[76:79], v[172:175]\n"
"v_exp_f32 v164, v164\n"
"v_mfma_f32_16x16x32_bf16 v[172:175], a[152:155], a[80:83], v[172:175]\n"
"v_exp_f32 v165, v165\n"
"v_mfma_f32_16x16x32_bf16 v[172:175], a[156:159], a[84:87], v[172:175]\n"
"v_exp_f32 v166, v166\n"
"v_mfma_f32_16x16x32_bf16 v[172:175], a[160:163], a[88:91], v[172:175]\n"
"v_exp_f32 v167, v167\n"
"v_mfma_f32_16x16x32_bf16 v[172:175], a[164:167], a[92:95], v[172:175]\n"
"ds_read_b64_tr_b16 a[144:145], v233 offset:24576\n"
"v_cvt_pk_bf16_f32 v150, v180, v181\n"
"v_cvt_pk_bf16_f32 v151, v182, v183\n"
"v_mfma_f32_16x16x32_bf16 v[180:183], a[96:99], a[72:75], v[200:203]\n"
"ds_read_b64_tr_b16 a[146:147], v233 offset:30720\n"
"v_cvt_pk_bf16_f32 v156, v156, v157\n"
"v_mfma_f32_16x16x32_bf16 v[180:183], a[100:103], a[76:79], v[180:183]\n"
"buffer_load_dwordx4 v245, s[48:51], 0 offen offset:0 lds\n"
"v_mfma_f32_16x16x32_bf16 v[180:183], a[104:107], a[80:83], v[180:183]\n"
"s_add_u32 m0, s73, 3968\n"
"ds_read_b64_tr_b16 a[148:149], v233 offset:36864\n"
"v_cvt_pk_bf16_f32 v157, v158, v159\n"
"v_mfma_f32_16x16x32_bf16 v[180:183], a[108:111], a[84:87], v[180:183]\n"
"ds_read_b64_tr_b16 a[150:151], v233 offset:43008\n"
"v_cvt_pk_bf16_f32 v158, v184, v185\n"
"v_mfma_f32_16x16x32_bf16 v[180:183], a[112:115], a[88:91], v[180:183]\n"
"s_waitcnt lgkmcnt(4)\n"
"v_exp_f32 v176, v176\n"
"v_mfma_f32_16x16x32_bf16 v[180:183], a[116:119], a[92:95], v[180:183]\n"
"v_exp_f32 v177, v177\n"
"v_mfma_f32_16x16x32_bf16 v[0:3], a[120:123], v[144:147], v[0:3]\n"
"s_waitcnt vmcnt(19)\n"
"v_exp_f32 v178, v178\n"
"v_mfma_f32_16x16x32_bf16 v[0:3], a[124:127], v[148:151], v[0:3]\n"
"v_exp_f32 v179, v179\n"
"v_mfma_f32_16x16x32_bf16 v[4:7], a[132:135], v[148:151], v[4:7]\n"
"ds_read_b64_tr_b16 a[152:153], v230 offset:25600\n"
"v_cvt_pk_bf16_f32 v159, v186, v187\n"
"v_mfma_f32_16x16x32_bf16 v[4:7], a[128:131], v[144:147], v[4:7]\n"
"ds_read_b64_tr_b16 a[154:155], v230 offset:31744\n"
"v_cvt_pk_bf16_f32 v164, v164, v165\n"
"v_mfma_f32_16x16x32_bf16 v[8:11], a[136:139], v[144:147], v[8:11]\n"
"buffer_load_dwordx4 v245, s[48:51], 0 offen offset:128 lds\n"
"v_mfma_f32_16x16x32_bf16 v[8:11], a[140:143], v[148:151], v[8:11]\n"
"s_add_u32 m0, s73, 4864\n"
"ds_read_b64_tr_b16 a[156:157], v230 offset:37888\n"
"v_cvt_pk_bf16_f32 v165, v166, v167\n"
"v_smfmac_f32_16x16x64_bf16 a[216:219], a[224:227], v[144:151], v237\n"
"ds_read_b64_tr_b16 a[158:159], v230 offset:44032\n"
"v_cvt_pk_bf16_f32 v166, v176, v177\n"
"v_mfma_f32_16x16x32_bf16 v[56:59], a[140:143], v[156:159], v[56:59]\n"
"ds_read_b64_tr_b16 a[160:161], v231 offset:25600\n"
"v_cvt_pk_bf16_f32 v167, v178, v179\n"
"v_mfma_f32_16x16x32_bf16 v[56:59], a[136:139], v[152:155], v[56:59]\n"
"ds_read_b64_tr_b16 a[162:163], v231 offset:31744\n"
"v_mfma_f32_16x16x32_bf16 v[52:55], a[128:131], v[152:155], v[52:55]\n"
"v_exp_f32 v172, v172\n"
"v_mfma_f32_16x16x32_bf16 v[52:55], a[132:135], v[156:159], v[52:55]\n"
"v_exp_f32 v173, v173\n"
"v_mfma_f32_16x16x32_bf16 v[48:51], a[124:127], v[156:159], v[48:51]\n"
"v_exp_f32 v174, v174\n"
"v_mfma_f32_16x16x32_bf16 v[48:51], a[120:123], v[152:155], v[48:51]\n"
"v_exp_f32 v175, v175\n"
"v_smfmac_f32_16x16x64_bf16 a[216:219], a[228:231], v[152:159], v237\n"
"buffer_load_dwordx4 v245, s[48:51], 0 offen offset:256 lds\n"
"v_mfma_f32_16x16x32_bf16 v[96:99], a[120:123], v[160:163], v[96:99]\n"
"s_add_u32 m0, s73, 6144\n"
"ds_read_b64_tr_b16 a[164:165], v231 offset:37888\n"
"v_cvt_pk_bf16_f32 v172, v172, v173\n"
"v_mfma_f32_16x16x32_bf16 v[96:99], a[124:127], v[164:167], v[96:99]\n"
"ds_read_b64_tr_b16 a[166:167], v231 offset:44032\n"
"v_cvt_pk_bf16_f32 v173, v174, v175\n"
"v_mfma_f32_16x16x32_bf16 v[100:103], a[132:135], v[164:167], v[100:103]\n"
"s_waitcnt vmcnt(12)\n"
"v_exp_f32 v180, v180\n"
"v_mfma_f32_16x16x32_bf16 v[100:103], a[128:131], v[160:163], v[100:103]\n"
"v_exp_f32 v181, v181\n"
"v_mfma_f32_16x16x32_bf16 v[104:107], a[136:139], v[160:163], v[104:107]\n"
"v_exp_f32 v182, v182\n"
"v_mfma_f32_16x16x32_bf16 v[104:107], a[140:143], v[164:167], v[104:107]\n"
"v_exp_f32 v183, v183\n"
"v_smfmac_f32_16x16x64_bf16 a[220:223], a[224:227], v[160:167], v237\n"
"ds_read_b64_tr_b16 a[96:97], v232 offset:25600\n"
"v_cvt_pk_bf16_f32 v174, v180, v181\n"
"v_cvt_pk_bf16_f32 v175, v182, v183\n"
"s_nop 1\n"
"v_mfma_f32_16x16x32_bf16 a[176:179], a[140:143], v[172:175], a[176:179]\n"
"ds_read_b64_tr_b16 a[98:99], v232 offset:31744\n"
"v_mfma_f32_16x16x32_bf16 a[176:179], a[136:139], v[168:171], a[176:179]\n"
"buffer_load_dwordx4 v246, s[48:51], 0 offen offset:0 lds\n"
"v_mfma_f32_16x16x32_bf16 a[172:175], a[128:131], v[168:171], a[172:175]\n"
"ds_read_b64_tr_b16 a[100:101], v232 offset:37888\n"
"s_add_u32 m0, s73, 7040\n"
"v_mfma_f32_16x16x32_bf16 a[172:175], a[132:135], v[172:175], a[172:175]\n"
"ds_read_b64_tr_b16 a[102:103], v232 offset:44032\n"
"v_mfma_f32_16x16x32_bf16 a[168:171], a[124:127], v[172:175], a[168:171]\n"
"ds_read_b64_tr_b16 a[104:105], v233 offset:25600\n"
"v_mfma_f32_16x16x32_bf16 a[168:171], a[120:123], v[168:171], a[168:171]\n"
"ds_read_b64_tr_b16 a[106:107], v233 offset:31744\n"
"s_waitcnt lgkmcnt(6)\n"
"v_smfmac_f32_16x16x64_bf16 a[220:223], a[228:231], v[168:175], v237\n"
"ds_read_b64_tr_b16 a[108:109], v233 offset:37888\n"
"v_mfma_f32_16x16x32_bf16 v[12:15], a[144:147], v[144:147], v[12:15]\n"
"ds_read_b128 v[212:215], v238 offset:0\n"
"v_mfma_f32_16x16x32_bf16 v[12:15], a[148:151], v[148:151], v[12:15]\n"
"ds_read_b128 v[216:219], v239 offset:0\n"
"v_mfma_f32_16x16x32_bf16 v[16:19], a[156:159], v[148:151], v[16:19]\n"
"buffer_load_dwordx4 v246, s[48:51], 0 offen offset:128 lds\n"
"v_mfma_f32_16x16x32_bf16 v[16:19], a[152:155], v[144:147], v[16:19]\n"
"ds_read_b128 v[220:223], v238 offset:1024\n"
"s_add_u32 m0, s73, 7936\n"
"v_mfma_f32_16x16x32_bf16 v[20:23], a[160:163], v[144:147], v[20:23]\n"
"ds_write_b128 v240, a[232:235] offset:0\n"
"v_mfma_f32_16x16x32_bf16 v[20:23], a[164:167], v[148:151], v[20:23]\n"
"ds_write_b128 v240, a[236:239] offset:1024\n"
"v_mfma_f32_16x16x32_bf16 v[68:71], a[164:167], v[156:159], v[68:71]\n"
"ds_read_b128 a[232:235], v241\n"
"v_mfma_f32_16x16x32_bf16 v[68:71], a[160:163], v[152:155], v[68:71]\n"
"ds_read_b128 a[236:239], v242\n"
"v_mfma_f32_16x16x32_bf16 v[64:67], a[152:155], v[152:155], v[64:67]\n"
"ds_write_b128 v240, a[240:243] offset:0\n"
"v_mfma_f32_16x16x32_bf16 v[64:67], a[156:159], v[156:159], v[64:67]\n"
"ds_write_b128 v240, a[244:247] offset:1024\n"
"v_mfma_f32_16x16x32_bf16 v[60:63], a[148:151], v[156:159], v[60:63]\n"
"buffer_load_dwordx4 v246, s[48:51], 0 offen offset:256 lds\n"
"v_mfma_f32_16x16x32_bf16 v[60:63], a[144:147], v[152:155], v[60:63]\n"
"ds_read_b128 a[240:243], v241\n"
"s_add_u32 m0, s73, 9216\n"
"v_mfma_f32_16x16x32_bf16 v[108:111], a[144:147], v[160:163], v[108:111]\n"
"ds_read_b64_tr_b16 a[110:111], v233 offset:44032\n"
"v_mfma_f32_16x16x32_bf16 v[108:111], a[148:151], v[164:167], v[108:111]\n"
"ds_read_b128 a[244:247], v242\n"
"v_mfma_f32_16x16x32_bf16 v[112:115], a[156:159], v[164:167], v[112:115]\n"
"ds_write_b128 v240, a[248:251] offset:0\n"
"v_mfma_f32_16x16x32_bf16 v[112:115], a[152:155], v[160:163], v[112:115]\n"
"ds_write_b128 v240, a[252:255] offset:1024\n"
"v_mfma_f32_16x16x32_bf16 v[116:119], a[160:163], v[160:163], v[116:119]\n"
"ds_read_b128 a[248:251], v241\n"
"v_mfma_f32_16x16x32_bf16 v[116:119], a[164:167], v[164:167], v[116:119]\n"
"s_waitcnt lgkmcnt(5)\n"
"ds_read_b128 a[252:255], v242\n"
"v_mfma_f32_16x16x32_bf16 v[204:207], v[212:215], a[232:235], 0\n"
"buffer_load_dwordx4 v247, s[48:51], 0 offen offset:0 lds\n"
"v_mfma_f32_16x16x32_bf16 v[204:207], v[216:219], a[236:239], v[204:207]\n"
"ds_read_b128 v[212:215], v239 offset:1024\n"
"s_add_u32 m0, s73, 10112\n"
"v_mfma_f32_16x16x32_bf16 v[204:207], v[220:223], a[240:243], v[204:207]\n"
"s_waitcnt vmcnt(13) lgkmcnt(6)\n"
"s_barrier\n"
"ds_read_b64_tr_b16 a[112:113], v230 offset:26624\n"
"v_mfma_f32_16x16x32_bf16 a[188:191], a[164:167], v[172:175], a[188:191]\n"
"ds_read_b64_tr_b16 a[114:115], v230 offset:32768\n"
"v_mfma_f32_16x16x32_bf16 a[188:191], a[160:163], v[168:171], a[188:191]\n"
"ds_read_b64_tr_b16 a[116:117], v230 offset:38912\n"
"v_mfma_f32_16x16x32_bf16 a[184:187], a[152:155], v[168:171], a[184:187]\n"
"ds_read_b64_tr_b16 a[118:119], v230 offset:45056\n"
"v_mfma_f32_16x16x32_bf16 a[184:187], a[156:159], v[172:175], a[184:187]\n"
"buffer_load_dwordx4 v247, s[48:51], 0 offen offset:128 lds\n"
"v_mfma_f32_16x16x32_bf16 a[180:183], a[148:151], v[172:175], a[180:183]\n"
"ds_read_b128 v[216:219], v238 offset:2048\n"
"s_add_u32 m0, s97, 24576\n"
"v_mfma_f32_16x16x32_bf16 a[180:183], a[144:147], v[168:171], a[180:183]\n"
"ds_read_b128 v[220:223], v239 offset:2048\n"
"v_mfma_f32_16x16x32_bf16 v[24:27], a[96:99], v[144:147], v[24:27]\n"
"buffer_load_dwordx4 v234, s[44:47], s66 offen offset:0 lds\n"
"v_mfma_f32_16x16x32_bf16 v[24:27], a[100:103], v[148:151], v[24:27]\n"
"ds_read_b64_tr_b16 a[164:165], v233 offset:38912\n"
"s_add_u32 m0, s97, 36864\n"
"v_mfma_f32_16x16x32_bf16 v[28:31], a[108:111], v[148:151], v[28:31]\n"
"ds_read_b64_tr_b16 a[166:167], v233 offset:45056\n"
"v_mfma_f32_16x16x32_bf16 v[28:31], a[104:107], v[144:147], v[28:31]\n"
"s_waitcnt lgkmcnt(4)\n"
"ds_read_b64_tr_b16 a[160:161], v233 offset:26624\n"
"v_mfma_f32_16x16x32_bf16 v[32:35], a[112:115], v[144:147], v[32:35]\n"
"ds_read_b64_tr_b16 a[162:163], v233 offset:32768\n"
"v_mfma_f32_16x16x32_bf16 v[32:35], a[116:119], v[148:151], v[32:35]\n"
"s_waitcnt lgkmcnt(4)\n"
"ds_read_b64_tr_b16 a[152:153], v232 offset:26624\n"
"v_mfma_f32_16x16x32_bf16 v[204:207], v[212:215], a[244:247], v[204:207]\n"
"ds_read_b128 v[212:215], v238 offset:6144\n"
"v_mfma_f32_16x16x32_bf16 v[204:207], v[216:219], a[248:251], v[204:207]\n"
"buffer_load_dwordx4 v234, s[44:47], s77 offen offset:0 lds\n"
"v_mfma_f32_16x16x32_bf16 v[204:207], v[220:223], a[252:255], v[204:207]\n"
"ds_read_b128 v[216:219], v239 offset:6144\n"
"s_add_u32 m0, s97, 25472\n"
"v_mfma_f32_16x16x32_bf16 v[80:83], a[116:119], v[156:159], v[80:83]\n"
"ds_read_b128 v[220:223], v238 offset:7168\n"
"v_mfma_f32_16x16x32_bf16 v[80:83], a[112:115], v[152:155], v[80:83]\n"
"ds_read_b64_tr_b16 a[154:155], v232 offset:32768\n"
"v_mfma_f32_16x16x32_bf16 v[76:79], a[104:107], v[152:155], v[76:79]\n"
"ds_read_b64_tr_b16 a[156:157], v232 offset:38912\n"
"v_cndmask_b32 v204, v204, v205, s[82:83]\n"
"v_mfma_f32_16x16x32_bf16 v[76:79], a[108:111], v[156:159], v[76:79]\n"
"ds_read_b64_tr_b16 a[158:159], v232 offset:45056\n"
"v_cndmask_b32 v206, v206, v207, s[82:83]\n"
"v_mfma_f32_16x16x32_bf16 v[72:75], a[100:103], v[156:159], v[72:75]\n"
"ds_read_b64_tr_b16 a[148:149], v231 offset:38912\n"
"v_cndmask_b32 v204, v204, v206, s[84:85]\n"
"v_mfma_f32_16x16x32_bf16 v[72:75], a[96:99], v[152:155], v[72:75]\n"
"ds_read_b64_tr_b16 a[150:151], v231 offset:45056\n"
"v_mfma_f32_16x16x32_bf16 v[120:123], a[96:99], v[160:163], v[120:123]\n"
"ds_read_b64_tr_b16 a[144:145], v231 offset:26624\n"
"v_mfma_f32_16x16x32_bf16 v[120:123], a[100:103], v[164:167], v[120:123]\n"
"buffer_load_dwordx4 v234, s[44:47], s66 offen offset:128 lds\n"
"v_mfma_f32_16x16x32_bf16 v[124:127], a[108:111], v[164:167], v[124:127]\n"
"s_add_u32 m0, s73, 11008\n"
"ds_read_b64_tr_b16 a[146:147], v231 offset:32768\n"
"s_add_u32 s73, s73, s74\n"
"v_mfma_f32_16x16x32_bf16 v[124:127], a[104:107], v[160:163], v[124:127]\n"
"ds_read_b128 a[120:123], v228 offset:6144\n"
"v_mfma_f32_16x16x32_bf16 v[128:131], a[112:115], v[160:163], v[128:131]\n"
"s_waitcnt lgkmcnt(8)\n"
"ds_read_b128 a[124:127], v229 offset:6144\n"
"v_mfma_f32_16x16x32_bf16 v[128:131], a[116:119], v[164:167], v[128:131]\n"
"ds_read_b128 a[128:131], v228 offset:7168\n"
"v_mfma_f32_16x16x32_bf16 v[208:211], v[212:215], a[232:235], 0\n"
"buffer_load_dwordx4 v247, s[48:51], 0 offen offset:256 lds\n"
"v_mfma_f32_16x16x32_bf16 v[208:211], v[216:219], a[236:239], v[208:211]\n"
"ds_read_b128 v[212:215], v239 offset:7168\n"
"s_add_u32 m0, s97, 37760\n"
"v_mfma_f32_16x16x32_bf16 v[208:211], v[220:223], a[240:243], v[208:211]\n"
"ds_read_b128 v[216:219], v238 offset:8192\n"
"v_subrev_u32 v238, s74, v238\n"
"v_mfma_f32_16x16x32_bf16 a[200:203], a[116:119], v[172:175], a[200:203]\n"
"ds_read_b128 v[220:223], v239 offset:8192\n"
"v_subrev_u32 v239, s74, v239\n"
"v_mfma_f32_16x16x32_bf16 a[200:203], a[112:115], v[168:171], a[200:203]\n"
"ds_read_b128 a[132:135], v229 offset:7168\n"
"s_sub_u32 s74, 0, s74\n"
"v_mfma_f32_16x16x32_bf16 a[196:199], a[104:107], v[168:171], a[196:199]\n"
"ds_read_b128 a[136:139], v228 offset:8192\n"
"v_mfma_f32_16x16x32_bf16 a[196:199], a[108:111], v[172:175], a[196:199]\n"
"buffer_load_dwordx4 v234, s[44:47], s77 offen offset:128 lds\n"
"v_mfma_f32_16x16x32_bf16 a[192:195], a[100:103], v[172:175], a[192:195]\n"
"s_waitcnt lgkmcnt(8)\n"
"ds_read_b128 a[140:143], v229 offset:8192\n"
"s_add_u32 m0, s97, 26368\n"
"v_mfma_f32_16x16x32_bf16 a[192:195], a[96:99], v[168:171], a[192:195]\n"
"ds_read_b128 a[116:119], v229 offset:2048\n"
"v_mfma_f32_16x16x32_bf16 v[36:39], a[144:147], v[144:147], v[36:39]\n"
"ds_read_b128 a[112:115], v228 offset:2048\n"
"v_mfma_f32_16x16x32_bf16 v[36:39], a[148:151], v[148:151], v[36:39]\n"
"ds_read_b128 a[104:107], v228 offset:1024\n"
"v_mfma_f32_16x16x32_bf16 v[40:43], a[156:159], v[148:151], v[40:43]\n"
"ds_read_b128 a[108:111], v229 offset:1024\n"
"v_mfma_f32_16x16x32_bf16 v[40:43], a[152:155], v[144:147], v[40:43]\n"
"ds_read_b128 a[100:103], v229 offset:0\n"
"v_mfma_f32_16x16x32_bf16 v[44:47], a[160:163], v[144:147], v[44:47]\n"
"s_waitcnt lgkmcnt(8)\n"
"ds_read_b128 a[96:99], v228 offset:0\n"
"v_mfma_f32_16x16x32_bf16 v[44:47], a[164:167], v[148:151], v[44:47]\n"
"v_mfma_f32_16x16x32_bf16 v[208:211], v[212:215], a[244:247], v[208:211]\n"
"v_mfma_f32_16x16x32_bf16 v[208:211], v[216:219], a[248:251], v[208:211]\n"
"v_mfma_f32_16x16x32_bf16 v[208:211], v[220:223], a[252:255], v[208:211]\n"
"v_mfma_f32_16x16x32_bf16 v[92:95], a[164:167], v[156:159], v[92:95]\n"
"v_mfma_f32_16x16x32_bf16 v[92:95], a[160:163], v[152:155], v[92:95]\n"
"v_mfma_f32_16x16x32_bf16 v[88:91], a[152:155], v[152:155], v[88:91]\n"
"v_mfma_f32_16x16x32_bf16 v[88:91], a[156:159], v[156:159], v[88:91]\n"
"v_mfma_f32_16x16x32_bf16 v[84:87], a[148:151], v[156:159], v[84:87]\n"
"s_waitcnt vmcnt(13) lgkmcnt(0)\n"
"s_barrier\n"
"v_mfma_f32_16x16x32_bf16 v[84:87], a[144:147], v[152:155], v[84:87]\n"
"v_mfma_f32_16x16x32_bf16 v[132:135], a[144:147], v[160:163], v[132:135]\n"
"v_cndmask_b32 v208, v208, v209, s[82:83]\n"
"v_cndmask_b32 v210, v210, v211, s[82:83]\n"
"v_mfma_f32_16x16x32_bf16 v[132:135], a[148:151], v[164:167], v[132:135]\n"
"buffer_load_dwordx4 v234, s[44:47], s66 offen offset:256 lds\n"
"v_mfma_f32_16x16x32_bf16 v[136:139], a[156:159], v[164:167], v[136:139]\n"
"s_add_u32 m0, s97, 38656\n"
"v_cndmask_b32 v208, v208, v210, s[84:85]\n"
"v_max_f32 v224, v204, v208\n"
"v_mfma_f32_16x16x32_bf16 v[136:139], a[152:155], v[160:163], v[136:139]\n"
"s_mul_i32 s66, s67, 24576\n"
"v_sub_f32 v204, v204, v224\n"
"v_sub_f32 v208, v208, v224\n"
"v_mfma_f32_16x16x32_bf16 v[140:143], a[160:163], v[160:163], v[140:143]\n"
"v_mul_f32 v204, s90, v204\n"
"v_mul_f32 v208, s90, v208\n"
"v_mfma_f32_16x16x32_bf16 v[140:143], a[164:167], v[164:167], v[140:143]\n"
"v_exp_f32 v204, v204\n"
"v_mfma_f32_16x16x32_bf16 a[212:215], a[164:167], v[172:175], a[212:215]\n"
"v_exp_f32 v208, v208\n"
"v_mfma_f32_16x16x32_bf16 a[212:215], a[160:163], v[168:171], a[212:215]\n"
"v_add_f32 v224, v204, v208\n"
"v_mfma_f32_16x16x32_bf16 a[208:211], a[152:155], v[168:171], a[208:211]\n"
"v_rcp_f32 v224, v224\n"
"v_mfma_f32_16x16x32_bf16 a[208:211], a[156:159], v[172:175], a[208:211]\n"
"v_mul_f32 v224, s80, v224\n"
"v_mul_f32 v204, v204, v224\n"
"v_mfma_f32_16x16x32_bf16 a[204:207], a[148:151], v[172:175], a[204:207]\n"
"v_mul_f32 v208, v208, v224\n"
"v_cvt_pk_bf16_f32 v254, v204, v208\n"
"v_mfma_f32_16x16x32_bf16 a[204:207], a[144:147], v[168:171], a[204:207]\n"
"buffer_load_dwordx4 v234, s[44:47], s77 offen offset:256 lds\n"
"s_setpc_b64 s[62:63]\n"
"L_TG1:\n"
"s_mov_b64 s[56:57], s[12:13]\n"
"s_mov_b32 s58, 786432\n"
"s_add_u32 s92, s89, 24576\n"
"buffer_load_dwordx4 a[232:235], v234, s[56:59], s92 offen offset:0\n"
"buffer_load_dwordx4 a[236:239], v234, s[56:59], s92 offen offset:3072\n"
"buffer_load_dwordx4 a[240:243], v234, s[56:59], s92 offen offset:128\n"
"buffer_load_dwordx4 a[244:247], v234, s[56:59], s92 offen offset:3200\n"
"buffer_load_dwordx4 a[248:251], v234, s[56:59], s92 offen offset:256\n"
"buffer_load_dwordx4 a[252:255], v234, s[56:59], s92 offen offset:3328\n"
"s_branch L_B1\n"
".p2align 5\n"
"L_W0TG2:\n"
"s_nop 7\n"
"s_branch L_W0TG3\n"
"L_TG2:\n"
"s_mov_b64 s[56:57], s[12:13]\n"
"s_mov_b32 s58, 786432\n"
"s_add_u32 s92, s89, 49152\n"
"buffer_load_dwordx4 a[232:235], v234, s[56:59], s92 offen offset:0\n"
"buffer_load_dwordx4 a[236:239], v234, s[56:59], s92 offen offset:3072\n"
"buffer_load_dwordx4 a[240:243], v234, s[56:59], s92 offen offset:128\n"
"buffer_load_dwordx4 a[244:247], v234, s[56:59], s92 offen offset:3200\n"
"buffer_load_dwordx4 a[248:251], v234, s[56:59], s92 offen offset:256\n"
"buffer_load_dwordx4 a[252:255], v234, s[56:59], s92 offen offset:3328\n"
"s_branch L_B1\n"
".p2align 5\n"
"L_W0TG3:\n"
"s_nop 7\n"
"s_branch L_W0D1\n"
"L_TG3:\n"
"s_mov_b64 s[56:57], s[12:13]\n"
"s_mov_b32 s58, 786432\n"
"s_add_u32 s92, s89, 73728\n"
"buffer_load_dwordx4 a[232:235], v234, s[56:59], s92 offen offset:0\n"
"buffer_load_dwordx4 a[236:239], v234, s[56:59], s92 offen offset:3072\n"
"buffer_load_dwordx4 a[240:243], v234, s[56:59], s92 offen offset:128\n"
"buffer_load_dwordx4 a[244:247], v234, s[56:59], s92 offen offset:3200\n"
"buffer_load_dwordx4 a[248:251], v234, s[56:59], s92 offen offset:256\n"
"buffer_load_dwordx4 a[252:255], v234, s[56:59], s92 offen offset:3328\n"
"s_mov_b64 s[48:49], s[20:21]\n"
"v_subrev_u32 v250, 32, v250\n"
"s_branch L_B1\n"
".p2align 12\n"
".p2align 5\n"
"L_W0VS:\n"
"s_nop 7\n"
"s_branch L_W0EPZ\n"
"L_W1VS:\n"
"s_nop 7\n"
"s_branch L_W1EPZ\n"
"L_W2VS:\n"
"s_nop 7\n"
"s_branch L_W2EPZ\n"
"L_VS:\n"
"v_mbcnt_lo_u32_b32 v146, -1, 0\n"
"v_mbcnt_hi_u32_b32 v146, -1, v146\n"
"s_add_u32 s92, s72, 12288\n"
"v_add_u32 v240, s92, v230\n"
"v_add_u32 v241, s92, v231\n"
"v_add_u32 v242, s92, v232\n"
"v_add_u32 v243, s92, v233\n"
"v_lshl_add_u32 v238, v146, 4, s72\n"
"v_mov_b32 v255, v238\n"
"v_lshrrev_b32 v144, 3, v146\n"
"v_and_b32 v145, 7, v146\n"
"v_xor_b32 v145, v145, v144\n"
"v_lshl_add_u32 v145, v145, 4, s97\n"
"v_mad_u32_u24 v239, v144, s99, v145\n"
"s_branch L_N2\n"
"L_N2:\n"
"v_mfma_f32_16x16x32_bf16 v[144:147], a[96:99], a[0:3], v[188:191]\n"
"v_readlane_b32 s61, v235, s60\n"
"s_add_u32 s60, s60, 1\n"
"v_mfma_f32_16x16x32_bf16 v[144:147], a[100:103], a[4:7], v[144:147]\n"
"s_and_b32 s98, s61, 0x3ffff\n"
"s_add_u32 s62, s64, s98\n"
"s_addc_u32 s63, s65, 0\n"
"v_mfma_f32_16x16x32_bf16 v[144:147], a[104:107], a[8:11], v[144:147]\n"
"s_add_u32 m0, s97, 0\n"
"v_and_b32 v212, 0xffff, v251\n"
"v_lshlrev_b32 v213, 16, v251\n"
"v_mfma_f32_16x16x32_bf16 v[144:147], a[108:111], a[12:15], v[144:147]\n"
"buffer_load_dwordx4 v234, s[40:43], s66 offen offset:0 lds\n"
"v_mfma_f32_16x16x32_bf16 v[144:147], a[112:115], a[16:19], v[144:147]\n"
"s_add_u32 m0, s97, 896\n"
"ds_read_b128 a[144:147], v228 offset:12288\n"
"v_lshrrev_b32 v214, 16, v251\n"
"v_mfma_f32_16x16x32_bf16 v[144:147], a[116:119], a[20:23], v[144:147]\n"
"s_add_u32 s77, s66, 12288\n"
"ds_read_b128 a[148:151], v229 offset:12288\n"
"v_and_b32 v215, 0xffff0000, v251\n"
"v_mfma_f32_16x16x32_bf16 v[176:179], a[120:123], a[0:3], v[188:191]\n"
"s_lshr_b32 s67, s61, 18\n"
"ds_read_b128 a[152:155], v228 offset:13312\n"
"v_cndmask_b32 v212, v212, v213, s[82:83]\n"
"v_mfma_f32_16x16x32_bf16 v[176:179], a[124:127], a[4:7], v[176:179]\n"
"ds_read_b128 a[156:159], v229 offset:13312\n"
"v_cndmask_b32 v214, v214, v215, s[82:83]\n"
"v_mfma_f32_16x16x32_bf16 v[176:179], a[128:131], a[8:11], v[176:179]\n"
"v_exp_f32 v144, v144\n"
"v_mfma_f32_16x16x32_bf16 v[176:179], a[132:135], a[12:15], v[176:179]\n"
"v_exp_f32 v145, v145\n"
"v_mfma_f32_16x16x32_bf16 v[176:179], a[136:139], a[16:19], v[176:179]\n"
"v_exp_f32 v146, v146\n"
"v_mfma_f32_16x16x32_bf16 v[176:179], a[140:143], a[20:23], v[176:179]\n"
"v_exp_f32 v147, v147\n"
"v_mfma_f32_16x16x32_bf16 v[152:155], a[96:99], a[24:27], v[192:195]\n"
"buffer_load_dwordx4 v234, s[40:43], s66 offen offset:128 lds\n"
"v_mfma_f32_16x16x32_bf16 v[152:155], a[100:103], a[28:31], v[152:155]\n"
"s_add_u32 m0, s97, 1792\n"
"ds_read_b128 a[160:163], v228 offset:14336\n"
"v_cvt_pk_bf16_f32 v144, v144, v145\n"
"v_mfma_f32_16x16x32_bf16 v[152:155], a[104:107], a[32:35], v[152:155]\n"
"ds_read_b128 a[164:167], v229 offset:14336\n"
"v_cvt_pk_bf16_f32 v145, v146, v147\n"
"v_mfma_f32_16x16x32_bf16 v[152:155], a[108:111], a[36:39], v[152:155]\n"
"v_exp_f32 v176, v176\n"
"v_mfma_f32_16x16x32_bf16 v[152:155], a[112:115], a[40:43], v[152:155]\n"
"v_exp_f32 v177, v177\n"
"v_mfma_f32_16x16x32_bf16 v[152:155], a[116:119], a[44:47], v[152:155]\n"
"v_exp_f32 v178, v178\n"
"v_mfma_f32_16x16x32_bf16 v[180:183], a[120:123], a[24:27], v[192:195]\n"
"v_exp_f32 v179, v179\n"
"v_mfma_f32_16x16x32_bf16 v[180:183], a[124:127], a[28:31], v[180:183]\n"
"v_cvt_pk_bf16_f32 v146, v176, v177\n"
"v_cvt_pk_bf16_f32 v147, v178, v179\n"
"v_mfma_f32_16x16x32_bf16 v[180:183], a[128:131], a[32:35], v[180:183]\n"
"v_cndmask_b32 v224, 0, v212, s[68:69]\n"
"v_cndmask_b32 v225, 0, v212, s[70:71]\n"
"v_mfma_f32_16x16x32_bf16 v[180:183], a[132:135], a[36:39], v[180:183]\n"
"v_exp_f32 v152, v152\n"
"v_mfma_f32_16x16x32_bf16 v[180:183], a[136:139], a[40:43], v[180:183]\n"
"buffer_load_dwordx4 v234, s[40:43], s66 offen offset:256 lds\n"
"v_mfma_f32_16x16x32_bf16 v[180:183], a[140:143], a[44:47], v[180:183]\n"
"s_add_u32 m0, s97, 12288\n"
"v_exp_f32 v153, v153\n"
"v_mfma_f32_16x16x32_bf16 v[160:163], a[96:99], a[48:51], v[196:199]\n"
"v_exp_f32 v154, v154\n"
"v_mfma_f32_16x16x32_bf16 v[160:163], a[100:103], a[52:55], v[160:163]\n"
"v_exp_f32 v155, v155\n"
"v_mfma_f32_16x16x32_bf16 v[160:163], a[104:107], a[56:59], v[160:163]\n"
"v_cvt_pk_bf16_f32 v152, v152, v153\n"
"v_cvt_pk_bf16_f32 v153, v154, v155\n"
"v_mfma_f32_16x16x32_bf16 v[160:163], a[108:111], a[60:63], v[160:163]\n"
"v_exp_f32 v180, v180\n"
"v_mfma_f32_16x16x32_bf16 v[160:163], a[112:115], a[64:67], v[160:163]\n"
"v_exp_f32 v181, v181\n"
"v_mfma_f32_16x16x32_bf16 v[160:163], a[116:119], a[68:71], v[160:163]\n"
"v_exp_f32 v182, v182\n"
"v_mfma_f32_16x16x32_bf16 v[184:187], a[120:123], a[48:51], v[196:199]\n"
"v_exp_f32 v183, v183\n"
"v_mfma_f32_16x16x32_bf16 v[184:187], a[124:127], a[52:55], v[184:187]\n"
"v_cvt_pk_bf16_f32 v154, v180, v181\n"
"v_cvt_pk_bf16_f32 v155, v182, v183\n"
"v_mfma_f32_16x16x32_bf16 v[184:187], a[128:131], a[56:59], v[184:187]\n"
"v_cndmask_b32 v226, 0, v214, s[68:69]\n"
"v_cndmask_b32 v227, 0, v214, s[70:71]\n"
"v_mfma_f32_16x16x32_bf16 v[184:187], a[132:135], a[60:63], v[184:187]\n"
"v_exp_f32 v160, v160\n"
"v_mfma_f32_16x16x32_bf16 v[184:187], a[136:139], a[64:67], v[184:187]\n"
"v_exp_f32 v161, v161\n"
"v_mfma_f32_16x16x32_bf16 v[184:187], a[140:143], a[68:71], v[184:187]\n"
"v_exp_f32 v162, v162\n"
"v_mfma_f32_16x16x32_bf16 v[168:171], a[96:99], a[72:75], v[200:203]\n"
"v_exp_f32 v163, v163\n"
"v_mfma_f32_16x16x32_bf16 v[168:171], a[100:103], a[76:79], v[168:171]\n"
"ds_read_b128 a[96:99], v228 offset:18432\n"
"v_cvt_pk_bf16_f32 v160, v160, v161\n"
"v_mfma_f32_16x16x32_bf16 v[168:171], a[104:107], a[80:83], v[168:171]\n"
"ds_read_b128 a[100:103], v229 offset:18432\n"
"v_cvt_pk_bf16_f32 v161, v162, v163\n"
"v_mfma_f32_16x16x32_bf16 v[168:171], a[108:111], a[84:87], v[168:171]\n"
"v_exp_f32 v184, v184\n"
"v_mfma_f32_16x16x32_bf16 v[168:171], a[112:115], a[88:91], v[168:171]\n"
"v_exp_f32 v185, v185\n"
"v_mfma_f32_16x16x32_bf16 v[168:171], a[116:119], a[92:95], v[168:171]\n"
"v_exp_f32 v186, v186\n"
"v_mfma_f32_16x16x32_bf16 v[176:179], a[120:123], a[72:75], v[200:203]\n"
"ds_read_b128 a[104:107], v228 offset:19456\n"
"v_cvt_pk_bf16_f32 v162, v184, v185\n"
"v_mfma_f32_16x16x32_bf16 v[176:179], a[124:127], a[76:79], v[176:179]\n"
"ds_read_b128 a[108:111], v229 offset:19456\n"
"v_mov_b32 v251, v252\n"
"v_mfma_f32_16x16x32_bf16 v[176:179], a[128:131], a[80:83], v[176:179]\n"
"ds_read_b128 a[112:115], v228 offset:20480\n"
"v_mov_b32 v252, v253\n"
"v_mfma_f32_16x16x32_bf16 v[176:179], a[132:135], a[84:87], v[176:179]\n"
"ds_read_b128 a[116:119], v229 offset:20480\n"
"v_mov_b32 v253, v254\n"
"v_mfma_f32_16x16x32_bf16 v[176:179], a[136:139], a[88:91], v[176:179]\n"
"s_waitcnt lgkmcnt(6)\n"
"v_exp_f32 v187, v187\n"
"v_mfma_f32_16x16x32_bf16 v[176:179], a[140:143], a[92:95], v[176:179]\n"
"v_exp_f32 v168, v168\n"
"v_mfma_f32_16x16x32_bf16 v[148:151], a[144:147], a[0:3], v[188:191]\n"
"v_exp_f32 v169, v169\n"
"v_mfma_f32_16x16x32_bf16 v[148:151], a[148:151], a[4:7], v[148:151]\n"
"v_exp_f32 v170, v170\n"
"v_mfma_f32_16x16x32_bf16 v[148:151], a[152:155], a[8:11], v[148:151]\n"
"v_exp_f32 v171, v171\n"
"v_mfma_f32_16x16x32_bf16 v[148:151], a[156:159], a[12:15], v[148:151]\n"
"v_exp_f32 v176, v176\n"
"v_mfma_f32_16x16x32_bf16 v[148:151], a[160:163], a[16:19], v[148:151]\n"
"v_exp_f32 v177, v177\n"
"v_mfma_f32_16x16x32_bf16 v[148:151], a[164:167], a[20:23], v[148:151]\n"
"s_waitcnt vmcnt(5) lgkmcnt(0)\n"
"s_barrier\n"
"ds_read_b64_tr_b16 a[120:121], v230 offset:24576\n"
"v_mfma_f32_16x16x32_bf16 v[180:183], a[96:99], a[0:3], v[188:191]\n"
"v_exp_f32 v178, v178\n"
"v_mfma_f32_16x16x32_bf16 v[180:183], a[100:103], a[4:7], v[180:183]\n"
"v_exp_f32 v179, v179\n"
"v_mfma_f32_16x16x32_bf16 v[180:183], a[104:107], a[8:11], v[180:183]\n"
"ds_read_b64_tr_b16 a[122:123], v230 offset:30720\n"
"v_cvt_pk_bf16_f32 v163, v186, v187\n"
"v_mfma_f32_16x16x32_bf16 v[180:183], a[108:111], a[12:15], v[180:183]\n"
"ds_read_b64_tr_b16 a[124:125], v230 offset:36864\n"
"v_cvt_pk_bf16_f32 v168, v168, v169\n"
"v_mfma_f32_16x16x32_bf16 v[180:183], a[112:115], a[16:19], v[180:183]\n"
"ds_read_b64_tr_b16 a[126:127], v230 offset:43008\n"
"v_cvt_pk_bf16_f32 v169, v170, v171\n"
"v_mfma_f32_16x16x32_bf16 v[180:183], a[116:119], a[20:23], v[180:183]\n"
"buffer_load_dwordx4 v234, s[40:43], s77 offen offset:0 lds\n"
"v_mfma_f32_16x16x32_bf16 v[156:159], a[144:147], a[24:27], v[192:195]\n"
"s_add_u32 m0, s97, 13184\n"
"ds_read_b64_tr_b16 a[128:129], v231 offset:24576\n"
"v_cvt_pk_bf16_f32 v170, v176, v177\n"
"v_mfma_f32_16x16x32_bf16 v[156:159], a[148:151], a[28:31], v[156:159]\n"
"ds_read_b64_tr_b16 a[130:131], v231 offset:30720\n"
"v_cvt_pk_bf16_f32 v171, v178, v179\n"
"v_mfma_f32_16x16x32_bf16 v[156:159], a[152:155], a[32:35], v[156:159]\n"
"ds_read_b64_tr_b16 a[132:133], v231 offset:36864\n"
"v_mfma_f32_16x16x32_bf16 v[156:159], a[156:159], a[36:39], v[156:159]\n"
"ds_read_b64_tr_b16 a[134:135], v231 offset:43008\n"
"v_mfma_f32_16x16x32_bf16 v[156:159], a[160:163], a[40:43], v[156:159]\n"
"v_exp_f32 v148, v148\n"
"v_mfma_f32_16x16x32_bf16 v[156:159], a[164:167], a[44:47], v[156:159]\n"
"v_exp_f32 v149, v149\n"
"v_mfma_f32_16x16x32_bf16 v[184:187], a[96:99], a[24:27], v[192:195]\n"
"v_exp_f32 v150, v150\n"
"v_mfma_f32_16x16x32_bf16 v[184:187], a[100:103], a[28:31], v[184:187]\n"
"v_exp_f32 v151, v151\n"
"v_mfma_f32_16x16x32_bf16 v[184:187], a[104:107], a[32:35], v[184:187]\n"
"ds_read_b64_tr_b16 a[136:137], v232 offset:24576\n"
"v_cvt_pk_bf16_f32 v148, v148, v149\n"
"v_mfma_f32_16x16x32_bf16 v[184:187], a[108:111], a[36:39], v[184:187]\n"
"buffer_load_dwordx4 v234, s[40:43], s77 offen offset:128 lds\n"
"v_mfma_f32_16x16x32_bf16 v[184:187], a[112:115], a[40:43], v[184:187]\n"
"s_add_u32 m0, s97, 14080\n"
"ds_read_b64_tr_b16 a[138:139], v232 offset:30720\n"
"v_cvt_pk_bf16_f32 v149, v150, v151\n"
"v_mfma_f32_16x16x32_bf16 v[184:187], a[116:119], a[44:47], v[184:187]\n"
"v_exp_f32 v180, v180\n"
"v_mfma_f32_16x16x32_bf16 v[164:167], a[144:147], a[48:51], v[196:199]\n"
"v_exp_f32 v181, v181\n"
"v_mfma_f32_16x16x32_bf16 v[164:167], a[148:151], a[52:55], v[164:167]\n"
"v_exp_f32 v182, v182\n"
"v_mfma_f32_16x16x32_bf16 v[164:167], a[152:155], a[56:59], v[164:167]\n"
"v_exp_f32 v183, v183\n"
"v_mfma_f32_16x16x32_bf16 v[164:167], a[156:159], a[60:63], v[164:167]\n"
"ds_read_b64_tr_b16 a[140:141], v232 offset:36864\n"
"v_cvt_pk_bf16_f32 v150, v180, v181\n"
"v_mfma_f32_16x16x32_bf16 v[164:167], a[160:163], a[64:67], v[164:167]\n"
"ds_read_b64_tr_b16 a[142:143], v232 offset:43008\n"
"v_cvt_pk_bf16_f32 v151, v182, v183\n"
"v_mfma_f32_16x16x32_bf16 v[164:167], a[164:167], a[68:71], v[164:167]\n"
"v_exp_f32 v156, v156\n"
"v_mfma_f32_16x16x32_bf16 v[176:179], a[96:99], a[48:51], v[196:199]\n"
"v_exp_f32 v157, v157\n"
"v_mfma_f32_16x16x32_bf16 v[176:179], a[100:103], a[52:55], v[176:179]\n"
"buffer_load_dwordx4 v234, s[40:43], s77 offen offset:256 lds\n"
"v_mfma_f32_16x16x32_bf16 v[176:179], a[104:107], a[56:59], v[176:179]\n"
"s_add_u32 m0, s97, 24576\n"
"v_exp_f32 v158, v158\n"
"v_mfma_f32_16x16x32_bf16 v[176:179], a[108:111], a[60:63], v[176:179]\n"
"v_exp_f32 v159, v159\n"
"v_mfma_f32_16x16x32_bf16 v[176:179], a[112:115], a[64:67], v[176:179]\n"
"v_exp_f32 v184, v184\n"
"v_mfma_f32_16x16x32_bf16 v[176:179], a[116:119], a[68:71], v[176:179]\n"
"v_exp_f32 v185, v185\n"
"v_mfma_f32_16x16x32_bf16 v[172:175], a[144:147], a[72:75], v[200:203]\n"
"v_exp_f32 v186, v186\n"
"v_mfma_f32_16x16x32_bf16 v[172:175], a[148:151], a[76:79], v[172:175]\n"
"v_exp_f32 v187, v187\n"
"v_mfma_f32_16x16x32_bf16 v[172:175], a[152:155], a[80:83], v[172:175]\n"
"v_exp_f32 v164, v164\n"
"v_mfma_f32_16x16x32_bf16 v[172:175], a[156:159], a[84:87], v[172:175]\n"
"v_exp_f32 v165, v165\n"
"v_mfma_f32_16x16x32_bf16 v[172:175], a[160:163], a[88:91], v[172:175]\n"
"v_exp_f32 v166, v166\n"
"v_mfma_f32_16x16x32_bf16 v[172:175], a[164:167], a[92:95], v[172:175]\n"
"v_exp_f32 v167, v167\n"
"v_mfma_f32_16x16x32_bf16 v[180:183], a[96:99], a[72:75], v[200:203]\n"
"ds_read_b64_tr_b16 a[144:145], v233 offset:24576\n"
"v_cvt_pk_bf16_f32 v156, v156, v157\n"
"v_mfma_f32_16x16x32_bf16 v[180:183], a[100:103], a[76:79], v[180:183]\n"
"ds_read_b64_tr_b16 a[146:147], v233 offset:30720\n"
"v_cvt_pk_bf16_f32 v157, v158, v159\n"
"v_mfma_f32_16x16x32_bf16 v[180:183], a[104:107], a[80:83], v[180:183]\n"
"ds_read_b64_tr_b16 a[148:149], v233 offset:36864\n"
"v_cvt_pk_bf16_f32 v158, v184, v185\n"
"v_mfma_f32_16x16x32_bf16 v[180:183], a[108:111], a[84:87], v[180:183]\n"
"ds_read_b64_tr_b16 a[150:151], v233 offset:43008\n"
"v_cvt_pk_bf16_f32 v159, v186, v187\n"
"v_mfma_f32_16x16x32_bf16 v[180:183], a[112:115], a[88:91], v[180:183]\n"
"s_waitcnt lgkmcnt(4)\n"
"v_exp_f32 v176, v176\n"
"v_mfma_f32_16x16x32_bf16 v[180:183], a[116:119], a[92:95], v[180:183]\n"
"v_exp_f32 v177, v177\n"
"v_mfma_f32_16x16x32_bf16 v[0:3], a[120:123], v[144:147], v[0:3]\n"
"s_waitcnt vmcnt(9)\n"
"v_exp_f32 v178, v178\n"
"v_mfma_f32_16x16x32_bf16 v[0:3], a[124:127], v[148:151], v[0:3]\n"
"v_exp_f32 v179, v179\n"
"v_mfma_f32_16x16x32_bf16 v[4:7], a[132:135], v[148:151], v[4:7]\n"
"ds_read_b64_tr_b16 a[152:153], v230 offset:25600\n"
"v_cvt_pk_bf16_f32 v164, v164, v165\n"
"v_mfma_f32_16x16x32_bf16 v[4:7], a[128:131], v[144:147], v[4:7]\n"
"ds_read_b64_tr_b16 a[154:155], v230 offset:31744\n"
"v_cvt_pk_bf16_f32 v165, v166, v167\n"
"v_mfma_f32_16x16x32_bf16 v[8:11], a[136:139], v[144:147], v[8:11]\n"
"ds_read_b64_tr_b16 a[156:157], v230 offset:37888\n"
"v_cvt_pk_bf16_f32 v166, v176, v177\n"
"v_mfma_f32_16x16x32_bf16 v[8:11], a[140:143], v[148:151], v[8:11]\n"
"ds_read_b64_tr_b16 a[158:159], v230 offset:44032\n"
"v_cvt_pk_bf16_f32 v167, v178, v179\n"
"v_smfmac_f32_16x16x64_bf16 a[216:219], a[224:227], v[144:151], v237\n"
"v_exp_f32 v172, v172\n"
"v_mfma_f32_16x16x32_bf16 v[56:59], a[140:143], v[156:159], v[56:59]\n"
"v_exp_f32 v173, v173\n"
"v_mfma_f32_16x16x32_bf16 v[56:59], a[136:139], v[152:155], v[56:59]\n"
"v_exp_f32 v174, v174\n"
"v_mfma_f32_16x16x32_bf16 v[52:55], a[128:131], v[152:155], v[52:55]\n"
"ds_read_b64_tr_b16 v[204:205], v240 offset:0\n"
"v_cvt_pk_bf16_f32 v172, v172, v173\n"
"v_mfma_f32_16x16x32_bf16 v[52:55], a[132:135], v[156:159], v[52:55]\n"
"ds_read_b64_tr_b16 v[206:207], v240 offset:6144\n"
"v_mfma_f32_16x16x32_bf16 v[48:51], a[124:127], v[156:159], v[48:51]\n"
"ds_read_b64_tr_b16 v[208:209], v241 offset:0\n"
"v_mfma_f32_16x16x32_bf16 v[48:51], a[120:123], v[152:155], v[48:51]\n"
"ds_read_b64_tr_b16 v[210:211], v241 offset:6144\n"
"v_smfmac_f32_16x16x64_bf16 a[216:219], a[228:231], v[152:159], v237\n"
"v_exp_f32 v175, v175\n"
"v_mfma_f32_16x16x32_bf16 v[96:99], a[120:123], v[160:163], v[96:99]\n"
"ds_read_b64_tr_b16 a[160:161], v231 offset:25600\n"
"v_cvt_pk_bf16_f32 v173, v174, v175\n"
"v_mfma_f32_16x16x32_bf16 v[96:99], a[124:127], v[164:167], v[96:99]\n"
"ds_read_b64_tr_b16 a[162:163], v231 offset:31744\n"
"v_mfma_f32_16x16x32_bf16 v[100:103], a[132:135], v[164:167], v[100:103]\n"
"v_exp_f32 v180, v180\n"
"v_mfma_f32_16x16x32_bf16 v[100:103], a[128:131], v[160:163], v[100:103]\n"
"v_exp_f32 v181, v181\n"
"v_mfma_f32_16x16x32_bf16 v[104:107], a[136:139], v[160:163], v[104:107]\n"
"v_exp_f32 v182, v182\n"
"v_mfma_f32_16x16x32_bf16 v[104:107], a[140:143], v[164:167], v[104:107]\n"
"v_exp_f32 v183, v183\n"
"v_smfmac_f32_16x16x64_bf16 a[220:223], a[224:227], v[160:167], v237\n"
"s_waitcnt lgkmcnt(2)\n"
"ds_read_b64_tr_b16 a[164:165], v231 offset:37888\n"
"v_cvt_pk_bf16_f32 v174, v180, v181\n"
"v_mfma_f32_16x16x32_bf16 v[212:215], v[204:207], v[224:227], 0\n"
"ds_read_b64_tr_b16 a[166:167], v231 offset:44032\n"
"v_cvt_pk_bf16_f32 v175, v182, v183\n"
"v_mfma_f32_16x16x32_bf16 v[216:219], v[208:211], v[224:227], 0\n"
"ds_read_b64_tr_b16 a[96:97], v232 offset:25600\n"
"v_mfma_f32_16x16x32_bf16 a[176:179], a[140:143], v[172:175], a[176:179]\n"
"ds_read_b64_tr_b16 a[98:99], v232 offset:31744\n"
"v_mfma_f32_16x16x32_bf16 a[176:179], a[136:139], v[168:171], a[176:179]\n"
"ds_read_b64_tr_b16 v[204:205], v242 offset:0\n"
"v_cvt_pk_bf16_f32 v212, v212, v213\n"
"v_mfma_f32_16x16x32_bf16 a[172:175], a[128:131], v[168:171], a[172:175]\n"
"ds_read_b64_tr_b16 v[206:207], v242 offset:6144\n"
"v_cvt_pk_bf16_f32 v213, v214, v215\n"
"v_mfma_f32_16x16x32_bf16 a[172:175], a[132:135], v[172:175], a[172:175]\n"
"ds_read_b64_tr_b16 v[208:209], v243 offset:0\n"
"v_cvt_pk_bf16_f32 v214, v216, v217\n"
"v_mfma_f32_16x16x32_bf16 a[168:171], a[124:127], v[172:175], a[168:171]\n"
"ds_read_b64_tr_b16 v[210:211], v243 offset:6144\n"
"v_cvt_pk_bf16_f32 v215, v218, v219\n"
"v_mfma_f32_16x16x32_bf16 a[168:171], a[120:123], v[168:171], a[168:171]\n"
"ds_read_b64_tr_b16 a[100:101], v232 offset:37888\n"
"v_smfmac_f32_16x16x64_bf16 a[220:223], a[228:231], v[168:175], v237\n"
"s_waitcnt lgkmcnt(7)\n"
"ds_read_b64_tr_b16 a[102:103], v232 offset:44032\n"
"v_mfma_f32_16x16x32_bf16 v[12:15], a[144:147], v[144:147], v[12:15]\n"
"ds_read_b64_tr_b16 a[104:105], v233 offset:25600\n"
"v_mfma_f32_16x16x32_bf16 v[12:15], a[148:151], v[148:151], v[12:15]\n"
"ds_read_b64_tr_b16 a[106:107], v233 offset:31744\n"
"v_mfma_f32_16x16x32_bf16 v[16:19], a[156:159], v[148:151], v[16:19]\n"
"ds_write_b64 v255, v[212:213] offset:0\n"
"v_mfma_f32_16x16x32_bf16 v[16:19], a[152:155], v[144:147], v[16:19]\n"
"v_mfma_f32_16x16x32_bf16 v[20:23], a[160:163], v[144:147], v[20:23]\n"
"v_mfma_f32_16x16x32_bf16 v[20:23], a[164:167], v[148:151], v[20:23]\n"
"s_waitcnt lgkmcnt(5)\n"
"ds_read_b64_tr_b16 a[108:109], v233 offset:37888\n"
"v_mfma_f32_16x16x32_bf16 v[220:223], v[204:207], v[224:227], 0\n"
"ds_read_b64_tr_b16 a[110:111], v233 offset:44032\n"
"v_mfma_f32_16x16x32_bf16 v[216:219], v[208:211], v[224:227], 0\n"
"ds_write_b64 v255, v[214:215] offset:8\n"
"v_mfma_f32_16x16x32_bf16 v[68:71], a[164:167], v[156:159], v[68:71]\n"
"ds_read_b64_tr_b16 v[204:205], v240 offset:1024\n"
"v_mfma_f32_16x16x32_bf16 v[68:71], a[160:163], v[152:155], v[68:71]\n"
"ds_read_b64_tr_b16 v[206:207], v240 offset:7168\n"
"v_mfma_f32_16x16x32_bf16 v[64:67], a[152:155], v[152:155], v[64:67]\n"
"ds_read_b64_tr_b16 v[208:209], v241 offset:1024\n"
"v_cvt_pk_bf16_f32 v220, v220, v221\n"
"v_mfma_f32_16x16x32_bf16 v[64:67], a[156:159], v[156:159], v[64:67]\n"
"ds_read_b64_tr_b16 v[210:211], v241 offset:7168\n"
"v_cvt_pk_bf16_f32 v221, v222, v223\n"
"v_mfma_f32_16x16x32_bf16 v[60:63], a[148:151], v[156:159], v[60:63]\n"
"ds_write_b64 v255, v[220:221] offset:3072\n"
"v_mfma_f32_16x16x32_bf16 v[60:63], a[144:147], v[152:155], v[60:63]\n"
"v_cvt_pk_bf16_f32 v222, v216, v217\n"
"v_cvt_pk_bf16_f32 v223, v218, v219\n"
"v_mfma_f32_16x16x32_bf16 v[108:111], a[144:147], v[160:163], v[108:111]\n"
"ds_write_b64 v255, v[222:223] offset:3080\n"
"v_mfma_f32_16x16x32_bf16 v[108:111], a[148:151], v[164:167], v[108:111]\n"
"v_mfma_f32_16x16x32_bf16 v[112:115], a[156:159], v[164:167], v[112:115]\n"
"v_mfma_f32_16x16x32_bf16 v[112:115], a[152:155], v[160:163], v[112:115]\n"
"v_mfma_f32_16x16x32_bf16 v[116:119], a[160:163], v[160:163], v[116:119]\n"
"s_waitcnt vmcnt(3) lgkmcnt(7)\n"
"s_barrier\n"
"ds_read_b64_tr_b16 a[112:113], v230 offset:26624\n"
"v_mfma_f32_16x16x32_bf16 v[116:119], a[164:167], v[164:167], v[116:119]\n"
"ds_read_b64_tr_b16 a[114:115], v230 offset:32768\n"
"ds_read_b64_tr_b16 a[116:117], v230 offset:38912\n"
"v_mfma_f32_16x16x32_bf16 a[188:191], a[164:167], v[172:175], a[188:191]\n"
"ds_read_b64_tr_b16 a[118:119], v230 offset:45056\n"
"ds_read_b128 a[120:123], v228 offset:6144\n"
"v_mfma_f32_16x16x32_bf16 a[188:191], a[160:163], v[168:171], a[188:191]\n"
"ds_read_b128 a[124:127], v229 offset:6144\n"
"ds_read_b128 a[128:131], v228 offset:7168\n"
"v_mfma_f32_16x16x32_bf16 a[184:187], a[152:155], v[168:171], a[184:187]\n"
"ds_read_b128 a[132:135], v229 offset:7168\n"
"v_mfma_f32_16x16x32_bf16 a[184:187], a[156:159], v[172:175], a[184:187]\n"
"ds_read_b64_tr_b16 a[164:165], v233 offset:38912\n"
"v_mfma_f32_16x16x32_bf16 a[180:183], a[148:151], v[172:175], a[180:183]\n"
"ds_read_b64_tr_b16 a[166:167], v233 offset:45056\n"
"s_waitcnt lgkmcnt(12)\n"
"v_mfma_f32_16x16x32_bf16 a[180:183], a[144:147], v[168:171], a[180:183]\n"
"ds_read_b64_tr_b16 a[160:161], v233 offset:26624\n"
"v_mfma_f32_16x16x32_bf16 v[212:215], v[204:207], v[224:227], 0\n"
"buffer_load_dwordx4 v234, s[44:47], s66 offen offset:0 lds\n"
"v_mfma_f32_16x16x32_bf16 v[216:219], v[208:211], v[224:227], 0\n"
"ds_read_b64_tr_b16 a[162:163], v233 offset:32768\n"
"s_add_u32 m0, s97, 36864\n"
"v_mfma_f32_16x16x32_bf16 v[24:27], a[96:99], v[144:147], v[24:27]\n"
"ds_read_b64_tr_b16 a[152:153], v232 offset:26624\n"
"s_waitcnt lgkmcnt(9)\n"
"v_mfma_f32_16x16x32_bf16 v[24:27], a[100:103], v[148:151], v[24:27]\n"
"ds_read_b64_tr_b16 a[154:155], v232 offset:32768\n"
"v_cvt_pk_bf16_f32 v212, v212, v213\n"
"v_mfma_f32_16x16x32_bf16 v[28:31], a[108:111], v[148:151], v[28:31]\n"
"ds_read_b64_tr_b16 v[204:205], v242 offset:1024\n"
"v_cvt_pk_bf16_f32 v213, v214, v215\n"
"v_mfma_f32_16x16x32_bf16 v[28:31], a[104:107], v[144:147], v[28:31]\n"
"ds_read_b64_tr_b16 v[206:207], v242 offset:7168\n"
"v_cvt_pk_bf16_f32 v214, v216, v217\n"
"v_mfma_f32_16x16x32_bf16 v[32:35], a[112:115], v[144:147], v[32:35]\n"
"ds_read_b64_tr_b16 v[208:209], v243 offset:1024\n"
"v_cvt_pk_bf16_f32 v215, v218, v219\n"
"v_mfma_f32_16x16x32_bf16 v[32:35], a[116:119], v[148:151], v[32:35]\n"
"ds_read_b64_tr_b16 v[210:211], v243 offset:7168\n"
"v_mfma_f32_16x16x32_bf16 v[80:83], a[116:119], v[156:159], v[80:83]\n"
"ds_read_b64_tr_b16 a[156:157], v232 offset:38912\n"
"v_mfma_f32_16x16x32_bf16 v[80:83], a[112:115], v[152:155], v[80:83]\n"
"buffer_load_dwordx4 v234, s[44:47], s77 offen offset:0 lds\n"
"v_mfma_f32_16x16x32_bf16 v[76:79], a[104:107], v[152:155], v[76:79]\n"
"ds_read_b64_tr_b16 a[158:159], v232 offset:45056\n"
"s_add_u32 m0, s97, 25472\n"
"v_mfma_f32_16x16x32_bf16 v[76:79], a[108:111], v[156:159], v[76:79]\n"
"ds_write_b64 v255, v[212:213] offset:1024\n"
"v_mfma_f32_16x16x32_bf16 v[72:75], a[100:103], v[156:159], v[72:75]\n"
"v_mfma_f32_16x16x32_bf16 v[72:75], a[96:99], v[152:155], v[72:75]\n"
"s_waitcnt lgkmcnt(3)\n"
"ds_read_b64_tr_b16 a[148:149], v231 offset:38912\n"
"v_mfma_f32_16x16x32_bf16 v[220:223], v[204:207], v[224:227], 0\n"
"ds_read_b64_tr_b16 a[150:151], v231 offset:45056\n"
"v_mfma_f32_16x16x32_bf16 v[216:219], v[208:211], v[224:227], 0\n"
"ds_write_b64 v255, v[214:215] offset:1032\n"
"v_mfma_f32_16x16x32_bf16 v[120:123], a[96:99], v[160:163], v[120:123]\n"
"buffer_load_dword v254, off, s[56:59], 0 sc1\n"
"v_mfma_f32_16x16x32_bf16 v[120:123], a[100:103], v[164:167], v[120:123]\n"
"ds_read_b64_tr_b16 v[204:205], v240 offset:2048\n"
"v_mfma_f32_16x16x32_bf16 v[124:127], a[108:111], v[164:167], v[124:127]\n"
"buffer_load_dwordx4 v234, s[44:47], s66 offen offset:128 lds\n"
"v_mfma_f32_16x16x32_bf16 v[124:127], a[104:107], v[160:163], v[124:127]\n"
"s_add_u32 m0, s97, 37760\n"
"ds_read_b64_tr_b16 v[206:207], v240 offset:8192\n"
"v_cvt_pk_bf16_f32 v220, v220, v221\n"
"v_mfma_f32_16x16x32_bf16 v[128:131], a[112:115], v[160:163], v[128:131]\n"
"ds_read_b64_tr_b16 v[208:209], v241 offset:2048\n"
"v_cvt_pk_bf16_f32 v221, v222, v223\n"
"v_mfma_f32_16x16x32_bf16 v[128:131], a[116:119], v[164:167], v[128:131]\n"
"ds_read_b64_tr_b16 v[210:211], v241 offset:8192\n"
"v_cvt_pk_bf16_f32 v222, v216, v217\n"
"v_mfma_f32_16x16x32_bf16 a[200:203], a[116:119], v[172:175], a[200:203]\n"
"ds_read_b64_tr_b16 a[144:145], v231 offset:26624\n"
"v_cvt_pk_bf16_f32 v223, v218, v219\n"
"v_mfma_f32_16x16x32_bf16 a[200:203], a[112:115], v[168:171], a[200:203]\n"
"ds_read_b64_tr_b16 a[146:147], v231 offset:32768\n"
"v_mfma_f32_16x16x32_bf16 a[196:199], a[104:107], v[168:171], a[196:199]\n"
"ds_write_b64 v255, v[220:221] offset:4096\n"
"v_mfma_f32_16x16x32_bf16 a[196:199], a[108:111], v[172:175], a[196:199]\n"
"v_mfma_f32_16x16x32_bf16 a[192:195], a[100:103], v[172:175], a[192:195]\n"
"ds_read_b128 a[136:139], v228 offset:8192\n"
"v_mfma_f32_16x16x32_bf16 a[192:195], a[96:99], v[168:171], a[192:195]\n"
"s_waitcnt lgkmcnt(4)\n"
"ds_read_b128 a[140:143], v229 offset:8192\n"
"v_mfma_f32_16x16x32_bf16 v[212:215], v[204:207], v[224:227], 0\n"
"ds_write_b64 v255, v[222:223] offset:4104\n"
"v_mfma_f32_16x16x32_bf16 v[216:219], v[208:211], v[224:227], 0\n"
"s_waitcnt lgkmcnt(4)\n"
"buffer_load_dwordx4 v234, s[44:47], s77 offen offset:128 lds\n"
"v_mfma_f32_16x16x32_bf16 v[36:39], a[144:147], v[144:147], v[36:39]\n"
"ds_read_b128 a[116:119], v229 offset:2048\n"
"s_add_u32 m0, s97, 26368\n"
"v_mfma_f32_16x16x32_bf16 v[36:39], a[148:151], v[148:151], v[36:39]\n"
"ds_read_b128 a[112:115], v228 offset:2048\n"
"v_cvt_pk_bf16_f32 v212, v212, v213\n"
"v_mfma_f32_16x16x32_bf16 v[40:43], a[156:159], v[148:151], v[40:43]\n"
"ds_read_b64_tr_b16 v[204:205], v242 offset:2048\n"
"v_cvt_pk_bf16_f32 v213, v214, v215\n"
"v_mfma_f32_16x16x32_bf16 v[40:43], a[152:155], v[144:147], v[40:43]\n"
"ds_read_b64_tr_b16 v[206:207], v242 offset:8192\n"
"v_cvt_pk_bf16_f32 v214, v216, v217\n"
"v_mfma_f32_16x16x32_bf16 v[44:47], a[160:163], v[144:147], v[44:47]\n"
"ds_read_b64_tr_b16 v[208:209], v243 offset:2048\n"
"v_cvt_pk_bf16_f32 v215, v218, v219\n"
"v_mfma_f32_16x16x32_bf16 v[44:47], a[164:167], v[148:151], v[44:47]\n"
"ds_read_b64_tr_b16 v[210:211], v243 offset:8192\n"
"v_mfma_f32_16x16x32_bf16 v[92:95], a[164:167], v[156:159], v[92:95]\n"
"ds_read_b128 a[104:107], v228 offset:1024\n"
"v_mfma_f32_16x16x32_bf16 v[92:95], a[160:163], v[152:155], v[92:95]\n"
"ds_read_b128 a[108:111], v229 offset:1024\n"
"v_mfma_f32_16x16x32_bf16 v[88:91], a[152:155], v[152:155], v[88:91]\n"
"ds_read_b128 a[100:103], v229 offset:0\n"
"v_mfma_f32_16x16x32_bf16 v[88:91], a[156:159], v[156:159], v[88:91]\n"
"ds_read_b128 a[96:99], v228 offset:0\n"
"v_mfma_f32_16x16x32_bf16 v[84:87], a[148:151], v[156:159], v[84:87]\n"
"ds_write_b64 v255, v[212:213] offset:2048\n"
"v_mfma_f32_16x16x32_bf16 v[84:87], a[144:147], v[152:155], v[84:87]\n"
"s_waitcnt lgkmcnt(5)\n"
"v_mfma_f32_16x16x32_bf16 v[220:223], v[204:207], v[224:227], 0\n"
"v_mfma_f32_16x16x32_bf16 v[216:219], v[208:211], v[224:227], 0\n"
"ds_write_b64 v255, v[214:215] offset:2056\n"
"v_mfma_f32_16x16x32_bf16 v[132:135], a[144:147], v[160:163], v[132:135]\n"
"v_mfma_f32_16x16x32_bf16 v[132:135], a[148:151], v[164:167], v[132:135]\n"
"v_mfma_f32_16x16x32_bf16 v[136:139], a[156:159], v[164:167], v[136:139]\n"
"v_mfma_f32_16x16x32_bf16 v[136:139], a[152:155], v[160:163], v[136:139]\n"
"s_waitcnt vmcnt(5) lgkmcnt(2)\n"
"s_barrier\n"
"v_mfma_f32_16x16x32_bf16 v[140:143], a[160:163], v[160:163], v[140:143]\n"
"buffer_load_dwordx4 v234, s[44:47], s66 offen offset:256 lds\n"
"v_mfma_f32_16x16x32_bf16 v[140:143], a[164:167], v[164:167], v[140:143]\n"
"s_add_u32 m0, s97, 38656\n"
"v_cvt_pk_bf16_f32 v220, v220, v221\n"
"v_cvt_pk_bf16_f32 v221, v222, v223\n"
"v_mfma_f32_16x16x32_bf16 a[212:215], a[164:167], v[172:175], a[212:215]\n"
"s_mul_i32 s66, s67, 24576\n"
"ds_write_b64 v255, v[220:221] offset:5120\n"
"v_mfma_f32_16x16x32_bf16 a[212:215], a[160:163], v[168:171], a[212:215]\n"
"v_cvt_pk_bf16_f32 v222, v216, v217\n"
"v_cvt_pk_bf16_f32 v223, v218, v219\n"
"v_mfma_f32_16x16x32_bf16 a[208:211], a[152:155], v[168:171], a[208:211]\n"
"buffer_load_dwordx4 v234, s[44:47], s77 offen offset:256 lds\n"
"v_mfma_f32_16x16x32_bf16 a[208:211], a[156:159], v[172:175], a[208:211]\n"
"ds_write_b64 v255, v[222:223] offset:5128\n"
"v_mfma_f32_16x16x32_bf16 a[204:207], a[148:151], v[172:175], a[204:207]\n"
"v_add_u32 v255, 6144, v255\n"
"v_mfma_f32_16x16x32_bf16 a[204:207], a[144:147], v[168:171], a[204:207]\n"
"s_setpc_b64 s[62:63]\n"
"L_ST:\n"
"ds_read_b128 a[232:235], v238 offset:12288\n"
"ds_read_b128 a[236:239], v238 offset:15360\n"
"ds_read_b128 a[240:243], v238 offset:13312\n"
"ds_read_b128 a[244:247], v238 offset:16384\n"
"ds_read_b128 a[248:251], v238 offset:14336\n"
"ds_read_b128 a[252:255], v238 offset:17408\n"
"s_branch L_GA\n"
".p2align 12\n"
".p2align 5\n"
"L_W0SW:\n"
"s_nop 7\n"
"s_branch L_W0D2\n"
"L_W1SW:\n"
"s_nop 7\n"
"s_branch L_TABDONE\n"
"L_SW:\n"
"s_mov_b64 s[52:53], s[12:13]\n"
"s_mov_b32 s96, s89\n"
"v_mbcnt_lo_u32_b32 v247, -1, 0\n"
"v_mbcnt_hi_u32_b32 v247, -1, v247\n"
"v_lshl_add_u32 v244, v247, 4, s76\n"
"v_bfe_u32 v246, v247, 3, 1\n"
"v_lshlrev_b32 v246, 11, v246\n"
"v_sub_u32 v246, v228, v246\n"
"v_add_u32 v245, s76, v246\n"
"buffer_load_dwordx4 v[204:207], v234, s[52:55], s96 offen offset:0\n"
"v_readlane_b32 s61, v235, s60\n"
"s_add_u32 s60, s60, 1\n"
"s_and_b32 s98, s61, 0x3ffff\n"
"s_add_u32 s62, s64, s98\n"
"s_addc_u32 s63, s65, 0\n"
"v_cvt_pk_bf16_f32 v0, v0, v1\n"
"v_cvt_pk_bf16_f32 v1, v2, v3\n"
"v_cvt_pk_bf16_f32 v2, v4, v5\n"
"v_cvt_pk_bf16_f32 v3, v6, v7\n"
"v_cvt_pk_bf16_f32 v4, v8, v9\n"
"v_cvt_pk_bf16_f32 v5, v10, v11\n"
"v_cvt_pk_bf16_f32 v6, v12, v13\n"
"v_cvt_pk_bf16_f32 v8, v16, v17\n"
"v_cvt_pk_bf16_f32 v10, v20, v21\n"
"v_cvt_pk_bf16_f32 v12, v24, v25\n"
"v_cvt_pk_bf16_f32 v13, v26, v27\n"
"v_cvt_pk_bf16_f32 v16, v32, v33\n"
"v_cvt_pk_bf16_f32 v17, v34, v35\n"
"v_cvt_pk_bf16_f32 v20, v40, v41\n"
"v_cvt_pk_bf16_f32 v21, v42, v43\n"
"v_cvt_pk_bf16_f32 v7, v14, v15\n"
"v_cvt_pk_bf16_f32 v9, v18, v19\n"
"v_cvt_pk_bf16_f32 v11, v22, v23\n"
"v_cvt_pk_bf16_f32 v14, v28, v29\n"
"v_cvt_pk_bf16_f32 v15, v30, v31\n"
"v_cvt_pk_bf16_f32 v18, v36, v37\n"
"v_cvt_pk_bf16_f32 v19, v38, v39\n"
"v_cvt_pk_bf16_f32 v22, v44, v45\n"
"v_cvt_pk_bf16_f32 v23, v46, v47\n"
"v_cvt_pk_bf16_f32 v48, v48, v49\n"
"v_cvt_pk_bf16_f32 v49, v50, v51\n"
"v_cvt_pk_bf16_f32 v50, v52, v53\n"
"v_cvt_pk_bf16_f32 v53, v58, v59\n"
"v_cvt_pk_bf16_f32 v58, v68, v69\n"
"v_cvt_pk_bf16_f32 v68, v88, v89\n"
"v_cvt_pk_bf16_f32 v69, v90, v91\n"
"v_cvt_pk_bf16_f32 v59, v70, v71\n"
"v_cvt_pk_bf16_f32 v70, v92, v93\n"
"v_cvt_pk_bf16_f32 v71, v94, v95\n"
"v_accvgpr_read_b32 v200, a216\n"
"v_accvgpr_read_b32 v196, a217\n"
"v_accvgpr_read_b32 v201, a218\n"
"v_accvgpr_read_b32 v197, a219\n"
"v_accvgpr_read_b32 v202, a220\n"
"v_accvgpr_read_b32 v198, a221\n"
"v_accvgpr_read_b32 v203, a222\n"
"v_accvgpr_read_b32 v199, a223\n"
"v_lshlrev_b32 v247, 4, v247\n"
"s_mov_b64 s[56:57], s[28:29]\n"
"buffer_load_dwordx4 v[212:215], v234, s[52:55], s96 offen offset:128\n"
"s_mov_b32 s58, 25600\n"
"v_add_f32 v200, v200, v196\n"
"v_add_f32 v201, v201, v197\n"
"v_add_f32 v202, v202, v198\n"
"v_add_f32 v203, v203, v199\n"
"s_mov_b32 s92, 24576\n"
"v_cvt_pk_bf16_f32 v51, v54, v55\n"
"v_cvt_pk_bf16_f32 v52, v56, v57\n"
"v_cvt_pk_bf16_f32 v54, v60, v61\n"
"v_cvt_pk_bf16_f32 v55, v62, v63\n"
"v_cvt_pk_bf16_f32 v56, v64, v65\n"
"v_cvt_pk_bf16_f32 v57, v66, v67\n"
"v_cvt_pk_bf16_f32 v60, v72, v73\n"
"v_cvt_pk_bf16_f32 v61, v74, v75\n"
"v_cvt_pk_bf16_f32 v62, v76, v77\n"
"buffer_load_dwordx4 v[220:223], v234, s[52:55], s96 offen offset:256\n"
"v_cvt_pk_bf16_f32 v63, v78, v79\n"
"v_cvt_pk_bf16_f32 v64, v80, v81\n"
"v_cvt_pk_bf16_f32 v65, v82, v83\n"
"v_cvt_pk_bf16_f32 v66, v84, v85\n"
"v_cvt_pk_bf16_f32 v67, v86, v87\n"
"v_cvt_pk_bf16_f32 v96, v96, v97\n"
"v_cvt_pk_bf16_f32 v97, v98, v99\n"
"v_cvt_pk_bf16_f32 v98, v100, v101\n"
"v_cvt_pk_bf16_f32 v99, v102, v103\n"
"v_cvt_pk_bf16_f32 v100, v104, v105\n"
"v_cvt_pk_bf16_f32 v101, v106, v107\n"
"v_cvt_pk_bf16_f32 v102, v108, v109\n"
"v_cvt_pk_bf16_f32 v103, v110, v111\n"
"v_cvt_pk_bf16_f32 v104, v112, v113\n"
"v_cvt_pk_bf16_f32 v105, v114, v115\n"
"buffer_load_dwordx4 v[208:211], v234, s[52:55], s96 offen offset:3072\n"
"v_cvt_pk_bf16_f32 v106, v116, v117\n"
"v_cvt_pk_bf16_f32 v107, v118, v119\n"
"v_xor_b32 v246, 64, v245\n"
"v_cvt_pk_bf16_f32 v108, v120, v121\n"
"v_cvt_pk_bf16_f32 v109, v122, v123\n"
"v_cvt_pk_bf16_f32 v110, v124, v125\n"
"v_cvt_pk_bf16_f32 v111, v126, v127\n"
"v_add_u32 v168, 147456, v228\n"
"v_add_u32 v169, 147456, v229\n"
"v_accvgpr_read_b32 v188, a168\n"
"v_accvgpr_read_b32 v189, a169\n"
"v_accvgpr_read_b32 v190, a170\n"
"v_accvgpr_read_b32 v191, a171\n"
"v_accvgpr_read_b32 v192, a172\n"
"v_accvgpr_read_b32 v193, a173\n"
"buffer_load_dwordx4 v[216:219], v234, s[52:55], s96 offen offset:3200\n"
"ds_read_b128 a[144:147], v168 offset:0\n"
"ds_read_b128 a[148:151], v169 offset:0\n"
"v_accvgpr_read_b32 v194, a174\n"
"v_accvgpr_read_b32 v195, a175\n"
"v_cvt_pk_bf16_f32 v120, v188, v189\n"
"v_cvt_pk_bf16_f32 v121, v190, v191\n"
"v_cvt_pk_bf16_f32 v122, v192, v193\n"
"v_cvt_pk_bf16_f32 v123, v194, v195\n"
"v_mov_b32 v170, 0\n"
"v_mov_b32 v171, 0\n"
"v_accvgpr_read_b32 v188, a176\n"
"v_accvgpr_read_b32 v189, a177\n"
"v_accvgpr_read_b32 v190, a178\n"
"v_accvgpr_read_b32 v191, a179\n"
"buffer_load_dwordx4 v[224:227], v234, s[52:55], s96 offen offset:3328\n"
"ds_read_b128 a[152:155], v168 offset:1024\n"
"s_add_u32 s96, s96, 24576\n"
"ds_read_b128 a[156:159], v169 offset:1024\n"
"v_accvgpr_read_b32 v192, a180\n"
"v_accvgpr_read_b32 v193, a181\n"
"v_accvgpr_read_b32 v194, a182\n"
"v_accvgpr_read_b32 v195, a183\n"
"v_cvt_pk_bf16_f32 v124, v188, v189\n"
"v_cvt_pk_bf16_f32 v125, v190, v191\n"
"v_cvt_pk_bf16_f32 v126, v192, v193\n"
"v_cvt_pk_bf16_f32 v127, v194, v195\n"
"v_cvt_pk_bf16_f32 v112, v128, v129\n"
"v_cvt_pk_bf16_f32 v113, v130, v131\n"
"v_accvgpr_read_b32 v188, a184\n"
"v_accvgpr_read_b32 v189, a185\n"
"buffer_load_dwordx4 v[144:147], v234, s[52:55], s96 offen offset:0\n"
"ds_read_b128 a[160:163], v168 offset:2048\n"
"ds_read_b128 a[164:167], v169 offset:2048\n"
"v_mov_b32 v168, 0\n"
"v_mov_b32 v169, 0\n"
"v_accvgpr_read_b32 v190, a186\n"
"v_accvgpr_read_b32 v191, a187\n"
"v_mfma_f32_32x32x16_bf16 a[168:183], v[168:171], v[168:171], 0\n"
"s_lshr_b32 s67, s61, 18\n"
"v_accvgpr_read_b32 v192, a188\n"
"v_accvgpr_read_b32 v193, a189\n"
"v_accvgpr_read_b32 v194, a190\n"
"v_accvgpr_read_b32 v195, a191\n"
"v_cvt_pk_bf16_f32 v128, v188, v189\n"
"v_cvt_pk_bf16_f32 v129, v190, v191\n"
"buffer_load_dwordx4 v[152:155], v234, s[52:55], s96 offen offset:128\n"
"v_cvt_pk_bf16_f32 v130, v192, v193\n"
"v_cvt_pk_bf16_f32 v131, v194, v195\n"
"v_accvgpr_read_b32 v188, a192\n"
"v_accvgpr_read_b32 v189, a193\n"
"v_accvgpr_read_b32 v190, a194\n"
"v_accvgpr_read_b32 v191, a195\n"
"v_accvgpr_read_b32 v192, a196\n"
"v_accvgpr_read_b32 v193, a197\n"
"v_accvgpr_read_b32 v194, a198\n"
"v_accvgpr_read_b32 v195, a199\n"
"v_mfma_f32_32x32x16_bf16 a[184:199], v[168:171], v[168:171], 0\n"
"s_mul_i32 s66, s67, 24576\n"
"v_cvt_pk_bf16_f32 v114, v132, v133\n"
"v_cvt_pk_bf16_f32 v115, v134, v135\n"
"v_cvt_pk_bf16_f32 v132, v188, v189\n"
"buffer_load_dwordx4 v[160:163], v234, s[52:55], s96 offen offset:256\n"
"v_cvt_pk_bf16_f32 v133, v190, v191\n"
"v_cvt_pk_bf16_f32 v134, v192, v193\n"
"v_cvt_pk_bf16_f32 v135, v194, v195\n"
"v_cvt_pk_bf16_f32 v116, v136, v137\n"
"v_cvt_pk_bf16_f32 v117, v138, v139\n"
"v_accvgpr_read_b32 v188, a200\n"
"v_accvgpr_read_b32 v189, a201\n"
"v_accvgpr_read_b32 v190, a202\n"
"v_accvgpr_read_b32 v191, a203\n"
"v_accvgpr_read_b32 v192, a204\n"
"v_accvgpr_read_b32 v193, a205\n"
"v_accvgpr_read_b32 v194, a206\n"
"v_accvgpr_read_b32 v195, a207\n"
"v_cvt_pk_bf16_f32 v136, v188, v189\n"
"v_cvt_pk_bf16_f32 v137, v190, v191\n"
"buffer_load_dwordx4 v[148:151], v234, s[52:55], s96 offen offset:3072\n"
"v_cvt_pk_bf16_f32 v138, v192, v193\n"
"v_cvt_pk_bf16_f32 v139, v194, v195\n"
"v_accvgpr_read_b32 v188, a208\n"
"v_accvgpr_read_b32 v189, a209\n"
"v_accvgpr_read_b32 v190, a210\n"
"v_accvgpr_read_b32 v191, a211\n"
"v_accvgpr_read_b32 v192, a212\n"
"v_accvgpr_read_b32 v193, a213\n"
"v_accvgpr_read_b32 v194, a214\n"
"v_accvgpr_read_b32 v195, a215\n"
"v_mfma_f32_32x32x16_bf16 a[200:215], v[168:171], v[168:171], 0\n"
"v_cvt_pk_bf16_f32 v118, v140, v141\n"
"v_cvt_pk_bf16_f32 v140, v188, v189\n"
"v_cvt_pk_bf16_f32 v141, v190, v191\n"
"buffer_load_dwordx4 v[156:159], v234, s[52:55], s96 offen offset:3200\n"
"v_mfma_f32_16x16x32_bf16 a[216:219], v[168:171], v[168:171], 0\n"
"v_cvt_pk_bf16_f32 v119, v142, v143\n"
"v_cvt_pk_bf16_f32 v142, v192, v193\n"
"v_mfma_f32_16x16x32_bf16 a[220:223], v[168:171], v[168:171], 0\n"
"v_cvt_pk_bf16_f32 v143, v194, v195\n"
"buffer_load_dwordx4 v[164:167], v234, s[52:55], s96 offen offset:3328\n"
"s_add_u32 s96, s96, 24576\n"
"s_waitcnt vmcnt(8)\n"
"v_lshlrev_b32 v172, 16, v204\n"
"v_and_b32 v173, s78, v204\n"
"buffer_load_dwordx4 v[24:27], v234, s[52:55], s96 offen offset:0\n"
"v_lshlrev_b32 v174, 16, v205\n"
"v_and_b32 v175, s78, v205\n"
"v_lshlrev_b32 v176, 16, v206\n"
"v_and_b32 v177, s78, v206\n"
"v_lshlrev_b32 v178, 16, v207\n"
"v_and_b32 v179, s78, v207\n"
"v_pk_mul_f32 v[172:173], v[172:173], s[90:91]\n"
"v_pk_mul_f32 v[174:175], v[174:175], s[90:91]\n"
"v_pk_mul_f32 v[176:177], v[176:177], s[90:91]\n"
"v_pk_mul_f32 v[178:179], v[178:179], s[90:91]\n"
"v_cvt_pk_bf16_f32 v204, v172, v173\n"
"v_cvt_pk_bf16_f32 v205, v174, v175\n"
"v_cvt_pk_bf16_f32 v206, v176, v177\n"
"v_cvt_pk_bf16_f32 v207, v178, v179\n"
"v_lshlrev_b32 v172, 16, v208\n"
"buffer_load_dwordx4 v[32:35], v234, s[52:55], s96 offen offset:128\n"
"ds_write_b128 v244, v[204:207] offset:0\n"
"v_and_b32 v173, s78, v208\n"
"v_lshlrev_b32 v174, 16, v209\n"
"v_and_b32 v175, s78, v209\n"
"v_lshlrev_b32 v176, 16, v210\n"
"v_and_b32 v177, s78, v210\n"
"v_lshlrev_b32 v178, 16, v211\n"
"v_and_b32 v179, s78, v211\n"
"v_pk_mul_f32 v[172:173], v[172:173], s[90:91]\n"
"v_pk_mul_f32 v[174:175], v[174:175], s[90:91]\n"
"buffer_load_dwordx4 v[40:43], v234, s[52:55], s96 offen offset:256\n"
"v_pk_mul_f32 v[176:177], v[176:177], s[90:91]\n"
"v_pk_mul_f32 v[178:179], v[178:179], s[90:91]\n"
"v_cvt_pk_bf16_f32 v208, v172, v173\n"
"v_cvt_pk_bf16_f32 v209, v174, v175\n"
"v_cvt_pk_bf16_f32 v210, v176, v177\n"
"v_cvt_pk_bf16_f32 v211, v178, v179\n"
"s_waitcnt vmcnt(10)\n"
"ds_write_b128 v244, v[208:211] offset:1024\n"
"v_lshlrev_b32 v172, 16, v212\n"
"buffer_load_dwordx4 v[28:31], v234, s[52:55], s96 offen offset:3072\n"
"v_and_b32 v173, s78, v212\n"
"v_lshlrev_b32 v174, 16, v213\n"
"v_and_b32 v175, s78, v213\n"
"v_lshlrev_b32 v176, 16, v214\n"
"v_and_b32 v177, s78, v214\n"
"v_lshlrev_b32 v178, 16, v215\n"
"v_and_b32 v179, s78, v215\n"
"v_pk_mul_f32 v[172:173], v[172:173], s[90:91]\n"
"v_pk_mul_f32 v[174:175], v[174:175], s[90:91]\n"
"v_pk_mul_f32 v[176:177], v[176:177], s[90:91]\n"
"v_pk_mul_f32 v[178:179], v[178:179], s[90:91]\n"
"v_cvt_pk_bf16_f32 v212, v172, v173\n"
"v_cvt_pk_bf16_f32 v213, v174, v175\n"
"v_cvt_pk_bf16_f32 v214, v176, v177\n"
"v_cvt_pk_bf16_f32 v215, v178, v179\n"
"buffer_load_dwordx4 v[36:39], v234, s[52:55], s96 offen offset:3200\n"
"ds_read_b128 a[0:3], v245\n"
"ds_read_b128 a[4:7], v246\n"
"v_lshlrev_b32 v172, 16, v216\n"
"v_and_b32 v173, s78, v216\n"
"v_lshlrev_b32 v174, 16, v217\n"
"v_and_b32 v175, s78, v217\n"
"v_lshlrev_b32 v176, 16, v218\n"
"v_and_b32 v177, s78, v218\n"
"v_lshlrev_b32 v178, 16, v219\n"
"v_and_b32 v179, s78, v219\n"
"v_pk_mul_f32 v[172:173], v[172:173], s[90:91]\n"
"v_pk_mul_f32 v[174:175], v[174:175], s[90:91]\n"
"v_pk_mul_f32 v[176:177], v[176:177], s[90:91]\n"
"v_pk_mul_f32 v[178:179], v[178:179], s[90:91]\n"
"buffer_load_dwordx4 v[44:47], v234, s[52:55], s96 offen offset:3328\n"
"ds_write_b128 v244, v[212:215] offset:0\n"
"s_add_u32 s96, s96, 24576\n"
"v_cvt_pk_bf16_f32 v216, v172, v173\n"
"v_cvt_pk_bf16_f32 v217, v174, v175\n"
"v_cvt_pk_bf16_f32 v218, v176, v177\n"
"v_cvt_pk_bf16_f32 v219, v178, v179\n"
"s_waitcnt vmcnt(12)\n"
"v_lshlrev_b32 v172, 16, v220\n"
"v_and_b32 v173, s78, v220\n"
"v_lshlrev_b32 v174, 16, v221\n"
"buffer_load_dwordx4 v[88:91], v234, s[52:55], s96 offen offset:256\n"
"ds_write_b128 v244, v[216:219] offset:1024\n"
"v_and_b32 v175, s78, v221\n"
"v_lshlrev_b32 v176, 16, v222\n"
"v_and_b32 v177, s78, v222\n"
"v_lshlrev_b32 v178, 16, v223\n"
"v_and_b32 v179, s78, v223\n"
"v_pk_mul_f32 v[172:173], v[172:173], s[90:91]\n"
"v_pk_mul_f32 v[174:175], v[174:175], s[90:91]\n"
"v_pk_mul_f32 v[176:177], v[176:177], s[90:91]\n"
"v_pk_mul_f32 v[178:179], v[178:179], s[90:91]\n"
"buffer_load_dwordx4 v[92:95], v234, s[52:55], s96 offen offset:3328\n"
"ds_read_b128 a[8:11], v245\n"
"ds_read_b128 a[12:15], v246\n"
"v_cvt_pk_bf16_f32 v220, v172, v173\n"
"v_cvt_pk_bf16_f32 v221, v174, v175\n"
"v_cvt_pk_bf16_f32 v222, v176, v177\n"
"v_cvt_pk_bf16_f32 v223, v178, v179\n"
"v_lshlrev_b32 v172, 16, v224\n"
"v_and_b32 v173, s78, v224\n"
"v_lshlrev_b32 v174, 16, v225\n"
"v_and_b32 v175, s78, v225\n"
"v_lshlrev_b32 v176, 16, v226\n"
"v_and_b32 v177, s78, v226\n"
"v_lshlrev_b32 v178, 16, v227\n"
"v_and_b32 v179, s78, v227\n"
"buffer_store_dwordx4 v[200:203], v247, s[56:59], s92 offen offset:0 sc1\n"
"ds_write_b128 v244, v[220:223] offset:0\n"
"s_mov_b32 s92, 0\n"
"v_pk_mul_f32 v[172:173], v[172:173], s[90:91]\n"
"v_pk_mul_f32 v[174:175], v[174:175], s[90:91]\n"
"v_pk_mul_f32 v[176:177], v[176:177], s[90:91]\n"
"v_pk_mul_f32 v[178:179], v[178:179], s[90:91]\n"
"v_cvt_pk_bf16_f32 v224, v172, v173\n"
"v_cvt_pk_bf16_f32 v225, v174, v175\n"
"v_cvt_pk_bf16_f32 v226, v176, v177\n"
"v_cvt_pk_bf16_f32 v227, v178, v179\n"
"s_waitcnt vmcnt(11)\n"
"buffer_load_dwordx4 v[72:75], v234, s[52:55], s96 offen offset:0\n"
"ds_write_b128 v244, v[224:227] offset:1024\n"
"v_lshlrev_b32 v172, 16, v144\n"
"v_and_b32 v173, s78, v144\n"
"v_lshlrev_b32 v174, 16, v145\n"
"v_and_b32 v175, s78, v145\n"
"v_lshlrev_b32 v176, 16, v146\n"
"v_and_b32 v177, s78, v146\n"
"v_lshlrev_b32 v178, 16, v147\n"
"v_and_b32 v179, s78, v147\n"
"v_pk_mul_f32 v[172:173], v[172:173], s[90:91]\n"
"buffer_store_dwordx4 v[0:3], v247, s[56:59], s92 offen offset:0 sc1\n"
"ds_read_b128 a[16:19], v245\n"
"ds_read_b128 a[20:23], v246\n"
"v_pk_mul_f32 v[174:175], v[174:175], s[90:91]\n"
"v_pk_mul_f32 v[176:177], v[176:177], s[90:91]\n"
"v_pk_mul_f32 v[178:179], v[178:179], s[90:91]\n"
"v_cvt_pk_bf16_f32 v144, v172, v173\n"
"v_cvt_pk_bf16_f32 v145, v174, v175\n"
"v_cvt_pk_bf16_f32 v146, v176, v177\n"
"v_cvt_pk_bf16_f32 v147, v178, v179\n"
"v_lshlrev_b32 v172, 16, v148\n"
"v_and_b32 v173, s78, v148\n"
"v_lshlrev_b32 v174, 16, v149\n"
"v_and_b32 v175, s78, v149\n"
"v_lshlrev_b32 v176, 16, v150\n"
"v_and_b32 v177, s78, v150\n"
"v_lshlrev_b32 v178, 16, v151\n"
"buffer_store_dwordx4 v[4:7], v247, s[56:59], s92 offen offset:1024 sc1\n"
"ds_write_b128 v244, v[144:147] offset:0\n"
"v_and_b32 v179, s78, v151\n"
"v_pk_mul_f32 v[172:173], v[172:173], s[90:91]\n"
"v_pk_mul_f32 v[174:175], v[174:175], s[90:91]\n"
"v_pk_mul_f32 v[176:177], v[176:177], s[90:91]\n"
"v_pk_mul_f32 v[178:179], v[178:179], s[90:91]\n"
"v_cvt_pk_bf16_f32 v148, v172, v173\n"
"v_cvt_pk_bf16_f32 v149, v174, v175\n"
"v_cvt_pk_bf16_f32 v150, v176, v177\n"
"v_cvt_pk_bf16_f32 v151, v178, v179\n"
"s_waitcnt vmcnt(13)\n"
"buffer_load_dwordx4 v[76:79], v234, s[52:55], s96 offen offset:3072\n"
"ds_write_b128 v244, v[148:151] offset:1024\n"
"v_lshlrev_b32 v172, 16, v152\n"
"v_and_b32 v173, s78, v152\n"
"v_lshlrev_b32 v174, 16, v153\n"
"v_and_b32 v175, s78, v153\n"
"v_lshlrev_b32 v176, 16, v154\n"
"v_and_b32 v177, s78, v154\n"
"v_lshlrev_b32 v178, 16, v155\n"
"v_and_b32 v179, s78, v155\n"
"v_pk_mul_f32 v[172:173], v[172:173], s[90:91]\n"
"v_pk_mul_f32 v[174:175], v[174:175], s[90:91]\n"
"buffer_store_dwordx4 v[8:11], v247, s[56:59], s92 offen offset:2048 sc1\n"
"ds_read_b128 a[24:27], v245\n"
"s_mov_b32 s92, 3072\n"
"ds_read_b128 a[28:31], v246\n"
"v_pk_mul_f32 v[176:177], v[176:177], s[90:91]\n"
"v_pk_mul_f32 v[178:179], v[178:179], s[90:91]\n"
"v_cvt_pk_bf16_f32 v152, v172, v173\n"
"v_cvt_pk_bf16_f32 v153, v174, v175\n"
"v_cvt_pk_bf16_f32 v154, v176, v177\n"
"v_cvt_pk_bf16_f32 v155, v178, v179\n"
"v_lshlrev_b32 v172, 16, v156\n"
"v_and_b32 v173, s78, v156\n"
"v_lshlrev_b32 v174, 16, v157\n"
"v_and_b32 v175, s78, v157\n"
"v_lshlrev_b32 v176, 16, v158\n"
"v_and_b32 v177, s78, v158\n"
"v_lshlrev_b32 v178, 16, v159\n"
"v_and_b32 v179, s78, v159\n"
"buffer_store_dwordx4 v[12:15], v247, s[56:59], s92 offen offset:0 sc1\n"
"ds_write_b128 v244, v[152:155] offset:0\n"
"v_pk_mul_f32 v[172:173], v[172:173], s[90:91]\n"
"v_pk_mul_f32 v[174:175], v[174:175], s[90:91]\n"
"v_pk_mul_f32 v[176:177], v[176:177], s[90:91]\n"
"v_pk_mul_f32 v[178:179], v[178:179], s[90:91]\n"
"v_cvt_pk_bf16_f32 v156, v172, v173\n"
"v_cvt_pk_bf16_f32 v157, v174, v175\n"
"v_cvt_pk_bf16_f32 v158, v176, v177\n"
"v_cvt_pk_bf16_f32 v159, v178, v179\n"
"s_waitcnt vmcnt(15)\n"
"buffer_load_dwordx4 v[80:83], v234, s[52:55], s96 offen offset:128\n"
"ds_write_b128 v244, v[156:159] offset:1024\n"
"v_lshlrev_b32 v172, 16, v160\n"
"v_and_b32 v173, s78, v160\n"
"v_lshlrev_b32 v174, 16, v161\n"
"v_and_b32 v175, s78, v161\n"
"v_lshlrev_b32 v176, 16, v162\n"
"v_and_b32 v177, s78, v162\n"
"v_lshlrev_b32 v178, 16, v163\n"
"v_and_b32 v179, s78, v163\n"
"v_pk_mul_f32 v[172:173], v[172:173], s[90:91]\n"
"v_pk_mul_f32 v[174:175], v[174:175], s[90:91]\n"
"buffer_store_dwordx4 v[16:19], v247, s[56:59], s92 offen offset:1024 sc1\n"
"ds_read_b128 a[32:35], v245\n"
"ds_read_b128 a[36:39], v246\n"
"v_pk_mul_f32 v[176:177], v[176:177], s[90:91]\n"
"v_pk_mul_f32 v[178:179], v[178:179], s[90:91]\n"
"v_cvt_pk_bf16_f32 v160, v172, v173\n"
"v_cvt_pk_bf16_f32 v161, v174, v175\n"
"v_cvt_pk_bf16_f32 v162, v176, v177\n"
"v_cvt_pk_bf16_f32 v163, v178, v179\n"
"v_lshlrev_b32 v172, 16, v164\n"
"v_and_b32 v173, s78, v164\n"
"v_lshlrev_b32 v174, 16, v165\n"
"v_and_b32 v175, s78, v165\n"
"v_lshlrev_b32 v176, 16, v166\n"
"v_and_b32 v177, s78, v166\n"
"v_lshlrev_b32 v178, 16, v167\n"
"v_and_b32 v179, s78, v167\n"
"buffer_store_dwordx4 v[20:23], v247, s[56:59], s92 offen offset:2048 sc1\n"
"ds_write_b128 v244, v[160:163] offset:0\n"
"s_mov_b32 s92, 6144\n"
"v_pk_mul_f32 v[172:173], v[172:173], s[90:91]\n"
"v_pk_mul_f32 v[174:175], v[174:175], s[90:91]\n"
"v_pk_mul_f32 v[176:177], v[176:177], s[90:91]\n"
"v_pk_mul_f32 v[178:179], v[178:179], s[90:91]\n"
"s_waitcnt lgkmcnt(15)\n"
"v_mfma_f32_16x16x32_bf16 v[188:191], a[144:147], a[0:3], 0\n"
"v_cvt_pk_bf16_f32 v164, v172, v173\n"
"v_cvt_pk_bf16_f32 v165, v174, v175\n"
"v_cvt_pk_bf16_f32 v166, v176, v177\n"
"v_cvt_pk_bf16_f32 v167, v178, v179\n"
"v_mfma_f32_16x16x32_bf16 v[188:191], a[148:151], a[4:7], v[188:191]\n"
"s_waitcnt vmcnt(14)\n"
"buffer_load_dwordx4 v[84:87], v234, s[52:55], s96 offen offset:3200\n"
"ds_write_b128 v244, v[164:167] offset:1024\n"
"v_lshlrev_b32 v172, 16, v24\n"
"v_and_b32 v173, s78, v24\n"
"v_lshlrev_b32 v174, 16, v25\n"
"v_and_b32 v175, s78, v25\n"
"v_lshlrev_b32 v176, 16, v26\n"
"v_and_b32 v177, s78, v26\n"
"v_lshlrev_b32 v178, 16, v27\n"
"v_and_b32 v179, s78, v27\n"
"v_pk_mul_f32 v[172:173], v[172:173], s[90:91]\n"
"v_pk_mul_f32 v[174:175], v[174:175], s[90:91]\n"
"buffer_store_dwordx4 v[48:51], v247, s[56:59], s92 offen offset:0 sc1\n"
"ds_read_b128 a[40:43], v245\n"
"v_pk_mul_f32 v[176:177], v[176:177], s[90:91]\n"
"v_pk_mul_f32 v[178:179], v[178:179], s[90:91]\n"
"s_waitcnt lgkmcnt(15)\n"
"v_mfma_f32_16x16x32_bf16 v[188:191], a[152:155], a[8:11], v[188:191]\n"
"s_mov_b64 s[52:53], s[26:27]\n"
"ds_read_b128 a[44:47], v246\n"
"v_cvt_pk_bf16_f32 v24, v172, v173\n"
"v_cvt_pk_bf16_f32 v25, v174, v175\n"
"v_cvt_pk_bf16_f32 v26, v176, v177\n"
"v_cvt_pk_bf16_f32 v27, v178, v179\n"
"v_lshlrev_b32 v172, 16, v28\n"
"v_and_b32 v173, s78, v28\n"
"v_lshlrev_b32 v174, 16, v29\n"
"v_and_b32 v175, s78, v29\n"
"v_lshlrev_b32 v176, 16, v30\n"
"v_and_b32 v177, s78, v30\n"
"v_lshlrev_b32 v178, 16, v31\n"
"buffer_store_dwordx4 v[52:55], v247, s[56:59], s92 offen offset:1024 sc1\n"
"ds_write_b128 v244, v[24:27] offset:0\n"
"v_and_b32 v179, s78, v31\n"
"v_pk_mul_f32 v[172:173], v[172:173], s[90:91]\n"
"v_pk_mul_f32 v[174:175], v[174:175], s[90:91]\n"
"v_pk_mul_f32 v[176:177], v[176:177], s[90:91]\n"
"v_pk_mul_f32 v[178:179], v[178:179], s[90:91]\n"
"v_mfma_f32_16x16x32_bf16 v[188:191], a[156:159], a[12:15], v[188:191]\n"
"v_cvt_pk_bf16_f32 v28, v172, v173\n"
"v_cvt_pk_bf16_f32 v29, v174, v175\n"
"v_cvt_pk_bf16_f32 v30, v176, v177\n"
"v_cvt_pk_bf16_f32 v31, v178, v179\n"
"s_waitcnt vmcnt(16)\n"
"buffer_store_dwordx4 v[56:59], v247, s[56:59], s92 offen offset:2048 sc1\n"
"ds_write_b128 v244, v[28:31] offset:1024\n"
"s_mov_b32 s92, 9216\n"
"s_waitcnt lgkmcnt(15)\n"
"v_mfma_f32_16x16x32_bf16 v[188:191], a[160:163], a[16:19], v[188:191]\n"
"v_lshlrev_b32 v172, 16, v32\n"
"v_and_b32 v173, s78, v32\n"
"v_lshlrev_b32 v174, 16, v33\n"
"v_and_b32 v175, s78, v33\n"
"v_lshlrev_b32 v176, 16, v34\n"
"v_and_b32 v177, s78, v34\n"
"v_lshlrev_b32 v178, 16, v35\n"
"v_and_b32 v179, s78, v35\n"
"v_pk_mul_f32 v[172:173], v[172:173], s[90:91]\n"
"buffer_store_dwordx4 v[60:63], v247, s[56:59], s92 offen offset:0 sc1\n"
"ds_read_b128 a[48:51], v245\n"
"ds_read_b128 a[52:55], v246\n"
"v_pk_mul_f32 v[174:175], v[174:175], s[90:91]\n"
"v_pk_mul_f32 v[176:177], v[176:177], s[90:91]\n"
"v_pk_mul_f32 v[178:179], v[178:179], s[90:91]\n"
"v_cvt_pk_bf16_f32 v32, v172, v173\n"
"v_cvt_pk_bf16_f32 v33, v174, v175\n"
"v_cvt_pk_bf16_f32 v34, v176, v177\n"
"v_cvt_pk_bf16_f32 v35, v178, v179\n"
"s_waitcnt lgkmcnt(15)\n"
"v_mfma_f32_16x16x32_bf16 v[188:191], a[164:167], a[20:23], v[188:191]\n"
"ds_write_b128 v244, v[32:35] offset:0\n"
"buffer_store_dwordx4 v[64:67], v247, s[56:59], s92 offen offset:1024 sc1\n"
"v_lshlrev_b32 v172, 16, v36\n"
"v_and_b32 v173, s78, v36\n"
"v_lshlrev_b32 v174, 16, v37\n"
"v_and_b32 v175, s78, v37\n"
"v_lshlrev_b32 v176, 16, v38\n"
"v_and_b32 v177, s78, v38\n"
"v_lshlrev_b32 v178, 16, v39\n"
"v_and_b32 v179, s78, v39\n"
"v_pk_mul_f32 v[172:173], v[172:173], s[90:91]\n"
"v_pk_mul_f32 v[174:175], v[174:175], s[90:91]\n"
"v_pk_mul_f32 v[176:177], v[176:177], s[90:91]\n"
"v_pk_mul_f32 v[178:179], v[178:179], s[90:91]\n"
"v_cvt_pk_bf16_f32 v36, v172, v173\n"
"v_cvt_pk_bf16_f32 v37, v174, v175\n"
"v_cvt_pk_bf16_f32 v38, v176, v177\n"
"v_cvt_pk_bf16_f32 v39, v178, v179\n"
"s_waitcnt lgkmcnt(14)\n"
"v_mfma_f32_16x16x32_bf16 v[192:195], a[144:147], a[24:27], 0\n"
"s_waitcnt vmcnt(18)\n"
"ds_write_b128 v244, v[36:39] offset:1024\n"
"buffer_store_dwordx4 v[68:71], v247, s[56:59], s92 offen offset:2048 sc1\n"
"s_mov_b32 s92, 12288\n"
"ds_read_b128 a[56:59], v245\n"
"v_lshlrev_b32 v172, 16, v40\n"
"v_and_b32 v173, s78, v40\n"
"v_lshlrev_b32 v174, 16, v41\n"
"v_and_b32 v175, s78, v41\n"
"v_lshlrev_b32 v176, 16, v42\n"
"v_and_b32 v177, s78, v42\n"
"v_lshlrev_b32 v178, 16, v43\n"
"v_and_b32 v179, s78, v43\n"
"v_pk_mul_f32 v[172:173], v[172:173], s[90:91]\n"
"v_pk_mul_f32 v[174:175], v[174:175], s[90:91]\n"
"v_pk_mul_f32 v[176:177], v[176:177], s[90:91]\n"
"v_pk_mul_f32 v[178:179], v[178:179], s[90:91]\n"
"s_waitcnt lgkmcnt(15)\n"
"v_mfma_f32_16x16x32_bf16 v[192:195], a[148:151], a[28:31], v[192:195]\n"
"ds_read_b128 a[60:63], v246\n"
"buffer_store_dwordx4 v[96:99], v247, s[56:59], s92 offen offset:0 sc1\n"
"v_cvt_pk_bf16_f32 v40, v172, v173\n"
"v_cvt_pk_bf16_f32 v41, v174, v175\n"
"v_cvt_pk_bf16_f32 v42, v176, v177\n"
"v_cvt_pk_bf16_f32 v43, v178, v179\n"
"v_lshlrev_b32 v172, 16, v44\n"
"v_and_b32 v173, s78, v44\n"
"v_lshlrev_b32 v174, 16, v45\n"
"v_and_b32 v175, s78, v45\n"
"v_lshlrev_b32 v176, 16, v46\n"
"v_and_b32 v177, s78, v46\n"
"v_lshlrev_b32 v178, 16, v47\n"
"v_and_b32 v179, s78, v47\n"
"v_pk_mul_f32 v[172:173], v[172:173], s[90:91]\n"
"v_pk_mul_f32 v[174:175], v[174:175], s[90:91]\n"
"v_pk_mul_f32 v[176:177], v[176:177], s[90:91]\n"
"v_pk_mul_f32 v[178:179], v[178:179], s[90:91]\n"
"s_waitcnt lgkmcnt(13)\n"
"v_mfma_f32_16x16x32_bf16 v[192:195], a[152:155], a[32:35], v[192:195]\n"
"ds_write_b128 v244, v[40:43] offset:0\n"
"buffer_store_dwordx4 v[100:103], v247, s[56:59], s92 offen offset:1024 sc1\n"
"v_cvt_pk_bf16_f32 v44, v172, v173\n"
"v_cvt_pk_bf16_f32 v45, v174, v175\n"
"v_cvt_pk_bf16_f32 v46, v176, v177\n"
"v_cvt_pk_bf16_f32 v47, v178, v179\n"
"s_waitcnt lgkmcnt(13)\n"
"v_mfma_f32_16x16x32_bf16 v[192:195], a[156:159], a[36:39], v[192:195]\n"
"s_waitcnt vmcnt(14)\n"
"ds_write_b128 v244, v[44:47] offset:1024\n"
"s_waitcnt lgkmcnt(11)\n"
"v_mfma_f32_16x16x32_bf16 v[192:195], a[160:163], a[40:43], v[192:195]\n"
"s_waitcnt lgkmcnt(10)\n"
"v_lshlrev_b32 v172, 16, v72\n"
"v_and_b32 v173, s78, v72\n"
"v_mfma_f32_16x16x32_bf16 v[192:195], a[164:167], a[44:47], v[192:195]\n"
"v_lshlrev_b32 v174, 16, v73\n"
"v_and_b32 v175, s78, v73\n"
"v_mfma_f32_32x32x16_bf16 v[0:15], v[168:171], v[168:171], 0\n"
"buffer_store_dwordx4 v[104:107], v247, s[56:59], s92 offen offset:2048 sc1\n"
"ds_read_b128 a[64:67], v245\n"
"ds_read_b128 a[68:71], v246\n"
"s_mov_b32 s92, 15360\n"
"v_lshlrev_b32 v176, 16, v74\n"
"v_and_b32 v177, s78, v74\n"
"v_lshlrev_b32 v178, 16, v75\n"
"v_and_b32 v179, s78, v75\n"
"v_pk_mul_f32 v[172:173], v[172:173], s[90:91]\n"
"v_pk_mul_f32 v[174:175], v[174:175], s[90:91]\n"
"v_pk_mul_f32 v[176:177], v[176:177], s[90:91]\n"
"v_pk_mul_f32 v[178:179], v[178:179], s[90:91]\n"
"s_waitcnt lgkmcnt(9)\n"
"v_mfma_f32_16x16x32_bf16 v[196:199], a[144:147], a[48:51], 0\n"
"v_cvt_pk_bf16_f32 v72, v172, v173\n"
"v_cvt_pk_bf16_f32 v73, v174, v175\n"
"v_cvt_pk_bf16_f32 v74, v176, v177\n"
"v_cvt_pk_bf16_f32 v75, v178, v179\n"
"v_lshlrev_b32 v172, 16, v76\n"
"v_and_b32 v173, s78, v76\n"
"v_lshlrev_b32 v174, 16, v77\n"
"v_and_b32 v175, s78, v77\n"
"v_lshlrev_b32 v176, 16, v78\n"
"v_and_b32 v177, s78, v78\n"
"v_lshlrev_b32 v178, 16, v79\n"
"v_and_b32 v179, s78, v79\n"
"v_pk_mul_f32 v[172:173], v[172:173], s[90:91]\n"
"v_pk_mul_f32 v[174:175], v[174:175], s[90:91]\n"
"v_pk_mul_f32 v[176:177], v[176:177], s[90:91]\n"
"v_pk_mul_f32 v[178:179], v[178:179], s[90:91]\n"
"s_waitcnt lgkmcnt(8)\n"
"v_mfma_f32_16x16x32_bf16 v[196:199], a[148:151], a[52:55], v[196:199]\n"
"buffer_store_dwordx4 v[108:111], v247, s[56:59], s92 offen offset:0 sc1\n"
"ds_write_b128 v244, v[72:75] offset:0\n"
"v_cvt_pk_bf16_f32 v76, v172, v173\n"
"v_cvt_pk_bf16_f32 v77, v174, v175\n"
"v_cvt_pk_bf16_f32 v78, v176, v177\n"
"v_cvt_pk_bf16_f32 v79, v178, v179\n"
"s_waitcnt vmcnt(10)\n"
"s_waitcnt lgkmcnt(6)\n"
"v_mfma_f32_16x16x32_bf16 v[196:199], a[152:155], a[56:59], v[196:199]\n"
"ds_write_b128 v244, v[76:79] offset:1024\n"
"buffer_store_dwordx4 v[112:115], v247, s[56:59], s92 offen offset:1024 sc1\n"
"v_lshlrev_b32 v172, 16, v80\n"
"v_and_b32 v173, s78, v80\n"
"v_lshlrev_b32 v174, 16, v81\n"
"v_and_b32 v175, s78, v81\n"
"v_lshlrev_b32 v176, 16, v82\n"
"v_and_b32 v177, s78, v82\n"
"v_lshlrev_b32 v178, 16, v83\n"
"v_and_b32 v179, s78, v83\n"
"v_pk_mul_f32 v[172:173], v[172:173], s[90:91]\n"
"v_pk_mul_f32 v[174:175], v[174:175], s[90:91]\n"
"v_pk_mul_f32 v[176:177], v[176:177], s[90:91]\n"
"v_pk_mul_f32 v[178:179], v[178:179], s[90:91]\n"
"v_cvt_pk_bf16_f32 v80, v172, v173\n"
"v_cvt_pk_bf16_f32 v81, v174, v175\n"
"v_cvt_pk_bf16_f32 v82, v176, v177\n"
"v_cvt_pk_bf16_f32 v83, v178, v179\n"
"s_waitcnt lgkmcnt(6)\n"
"v_mfma_f32_16x16x32_bf16 v[196:199], a[156:159], a[60:63], v[196:199]\n"
"ds_read_b128 a[72:75], v245\n"
"buffer_store_dwordx4 v[116:119], v247, s[56:59], s92 offen offset:2048 sc1\n"
"ds_read_b128 a[76:79], v246\n"
"ds_write_b128 v244, v[80:83] offset:0\n"
"s_mov_b32 s92, 18432\n"
"v_lshlrev_b32 v172, 16, v84\n"
"v_and_b32 v173, s78, v84\n"
"v_lshlrev_b32 v174, 16, v85\n"
"v_and_b32 v175, s78, v85\n"
"v_lshlrev_b32 v176, 16, v86\n"
"v_and_b32 v177, s78, v86\n"
"v_lshlrev_b32 v178, 16, v87\n"
"v_and_b32 v179, s78, v87\n"
"v_pk_mul_f32 v[172:173], v[172:173], s[90:91]\n"
"v_pk_mul_f32 v[174:175], v[174:175], s[90:91]\n"
"v_pk_mul_f32 v[176:177], v[176:177], s[90:91]\n"
"v_pk_mul_f32 v[178:179], v[178:179], s[90:91]\n"
"v_cvt_pk_bf16_f32 v84, v172, v173\n"
"v_cvt_pk_bf16_f32 v85, v174, v175\n"
"v_cvt_pk_bf16_f32 v86, v176, v177\n"
"v_cvt_pk_bf16_f32 v87, v178, v179\n"
"s_waitcnt lgkmcnt(6)\n"
"v_mfma_f32_16x16x32_bf16 v[196:199], a[160:163], a[64:67], v[196:199]\n"
"s_waitcnt vmcnt(23)\n"
"buffer_store_dwordx4 v[120:123], v247, s[56:59], s92 offen offset:0 sc1\n"
"ds_write_b128 v244, v[84:87] offset:1024\n"
"v_lshlrev_b32 v172, 16, v88\n"
"v_and_b32 v173, s78, v88\n"
"v_lshlrev_b32 v174, 16, v89\n"
"v_and_b32 v175, s78, v89\n"
"v_lshlrev_b32 v176, 16, v90\n"
"v_and_b32 v177, s78, v90\n"
"v_lshlrev_b32 v178, 16, v91\n"
"v_and_b32 v179, s78, v91\n"
"v_pk_mul_f32 v[172:173], v[172:173], s[90:91]\n"
"v_pk_mul_f32 v[174:175], v[174:175], s[90:91]\n"
"v_pk_mul_f32 v[176:177], v[176:177], s[90:91]\n"
"v_pk_mul_f32 v[178:179], v[178:179], s[90:91]\n"
"s_waitcnt lgkmcnt(6)\n"
"v_mfma_f32_16x16x32_bf16 v[196:199], a[164:167], a[68:71], v[196:199]\n"
"ds_read_b128 a[80:83], v245\n"
"buffer_store_dwordx4 v[124:127], v247, s[56:59], s92 offen offset:1024 sc1\n"
"ds_read_b128 a[84:87], v246\n"
"v_cvt_pk_bf16_f32 v88, v172, v173\n"
"v_cvt_pk_bf16_f32 v89, v174, v175\n"
"v_cvt_pk_bf16_f32 v90, v176, v177\n"
"v_cvt_pk_bf16_f32 v91, v178, v179\n"
"v_lshlrev_b32 v172, 16, v92\n"
"v_and_b32 v173, s78, v92\n"
"v_lshlrev_b32 v174, 16, v93\n"
"v_and_b32 v175, s78, v93\n"
"v_lshlrev_b32 v176, 16, v94\n"
"v_and_b32 v177, s78, v94\n"
"v_lshlrev_b32 v178, 16, v95\n"
"v_and_b32 v179, s78, v95\n"
"v_pk_mul_f32 v[172:173], v[172:173], s[90:91]\n"
"v_pk_mul_f32 v[174:175], v[174:175], s[90:91]\n"
"v_pk_mul_f32 v[176:177], v[176:177], s[90:91]\n"
"v_pk_mul_f32 v[178:179], v[178:179], s[90:91]\n"
"v_mfma_f32_32x32x16_bf16 v[16:31], v[168:171], v[168:171], 0\n"
"ds_write_b128 v244, v[88:91] offset:0\n"
"buffer_store_dwordx4 v[128:131], v247, s[56:59], s92 offen offset:2048 sc1\n"
"v_cvt_pk_bf16_f32 v92, v172, v173\n"
"v_cvt_pk_bf16_f32 v93, v174, v175\n"
"v_cvt_pk_bf16_f32 v94, v176, v177\n"
"v_cvt_pk_bf16_f32 v95, v178, v179\n"
"v_mfma_f32_32x32x16_bf16 v[32:47], v[168:171], v[168:171], 0\n"
"s_mov_b32 s92, 21504\n"
"ds_write_b128 v244, v[92:95] offset:1024\n"
"v_mfma_f32_32x32x16_bf16 v[48:63], v[168:171], v[168:171], 0\n"
"v_max3_f32 v204, v188, v189, v190\n"
"v_max_f32 v204, v204, v191\n"
"v_mov_b32 v205, v204\n"
"ds_read_b128 a[88:91], v245\n"
"ds_read_b128 a[92:95], v246\n"
"s_waitcnt lgkmcnt(9)\n"
"v_mfma_f32_16x16x32_bf16 v[200:203], a[144:147], a[72:75], 0\n"
"buffer_store_dwordx4 v[132:135], v247, s[56:59], s92 offen offset:0 sc1\n"
"s_waitcnt lgkmcnt(8)\n"
"v_mfma_f32_16x16x32_bf16 v[200:203], a[148:151], a[76:79], v[200:203]\n"
"s_waitcnt lgkmcnt(5)\n"
"v_permlane32_swap_b32 v204, v205\n"
"v_max_f32 v204, v204, v205\n"
"v_mfma_f32_16x16x32_bf16 v[200:203], a[152:155], a[80:83], v[200:203]\n"
"s_waitcnt lgkmcnt(4)\n"
"v_mov_b32 v205, v204\n"
"v_mfma_f32_16x16x32_bf16 v[200:203], a[156:159], a[84:87], v[200:203]\n"
"buffer_store_dwordx4 v[136:139], v247, s[56:59], s92 offen offset:1024 sc1\n"
"s_waitcnt lgkmcnt(1)\n"
"v_mfma_f32_16x16x32_bf16 v[200:203], a[160:163], a[88:91], v[200:203]\n"
"s_waitcnt lgkmcnt(0)\n"
"v_permlane16_swap_b32 v204, v205\n"
"v_max_f32 v204, v204, v205\n"
"v_mfma_f32_16x16x32_bf16 v[200:203], a[164:167], a[92:95], v[200:203]\n"
"v_sub_f32 v188, -2.0, v204\n"
"v_sub_f32 v189, -2.0, v204\n"
"v_mfma_f32_32x32x16_bf16 v[64:79], v[168:171], v[168:171], 0\n"
"v_sub_f32 v190, -2.0, v204\n"
"v_sub_f32 v191, -2.0, v204\n"
"v_max3_f32 v204, v192, v193, v194\n"
"v_max_f32 v204, v204, v195\n"
"v_mov_b32 v205, v204\n"
"v_mfma_f32_32x32x16_bf16 v[80:95], v[168:171], v[168:171], 0\n"
"buffer_store_dwordx4 v[140:143], v247, s[56:59], s92 offen offset:2048 sc1\n"
"v_permlane32_swap_b32 v204, v205\n"
"v_max_f32 v204, v204, v205\n"
"v_mov_b32 v205, v204\n"
"v_mfma_f32_32x32x16_bf16 v[96:111], v[168:171], v[168:171], 0\n"
"v_mfma_f32_32x32x16_bf16 v[112:127], v[168:171], v[168:171], 0\n"
"v_permlane16_swap_b32 v204, v205\n"
"v_max_f32 v204, v204, v205\n"
"v_sub_f32 v192, -2.0, v204\n"
"v_sub_f32 v193, -2.0, v204\n"
"v_sub_f32 v194, -2.0, v204\n"
"v_sub_f32 v195, -2.0, v204\n"
"v_max3_f32 v204, v196, v197, v198\n"
"v_max_f32 v204, v204, v199\n"
"v_mov_b32 v205, v204\n"
"v_mfma_f32_32x32x16_bf16 v[128:143], v[168:171], v[168:171], 0\n"
"s_nop 0\n"
"v_permlane32_swap_b32 v204, v205\n"
"v_max_f32 v204, v204, v205\n"
"v_mov_b32 v205, v204\n"
"s_nop 1\n"
"v_permlane16_swap_b32 v204, v205\n"
"v_max_f32 v204, v204, v205\n"
"v_sub_f32 v196, -2.0, v204\n"
"v_sub_f32 v197, -2.0, v204\n"
"v_sub_f32 v198, -2.0, v204\n"
"v_sub_f32 v199, -2.0, v204\n"
"v_max3_f32 v204, v200, v201, v202\n"
"v_max_f32 v204, v204, v203\n"
"v_mov_b32 v205, v204\n"
"s_nop 1\n"
"v_permlane32_swap_b32 v204, v205\n"
"v_max_f32 v204, v204, v205\n"
"v_mov_b32 v205, v204\n"
"s_nop 1\n"
"v_permlane16_swap_b32 v204, v205\n"
"v_max_f32 v204, v204, v205\n"
"v_sub_f32 v200, -2.0, v204\n"
"v_sub_f32 v201, -2.0, v204\n"
"v_sub_f32 v202, -2.0, v204\n"
"v_sub_f32 v203, -2.0, v204\n"
"s_setpc_b64 s[62:63]\n"
".p2align 5\n"
"L_W1P1:\n"
"s_nop 7\n"
"s_branch L_W1SW\n"
"L_P1:\n"
"v_mfma_f32_16x16x32_bf16 v[160:163], a[96:99], a[48:51], v[196:199]\n"
"v_readlane_b32 s61, v235, s60\n"
"s_add_u32 s60, s60, 1\n"
"v_mfma_f32_16x16x32_bf16 v[160:163], a[100:103], a[52:55], v[160:163]\n"
"s_and_b32 s98, s61, 0x3ffff\n"
"s_add_u32 s62, s64, s98\n"
"s_addc_u32 s63, s65, 0\n"
"s_add_u32 m0, s97, 0\n"
"v_mfma_f32_16x16x32_bf16 v[160:163], a[104:107], a[56:59], v[160:163]\n"
"s_add_u32 s77, s66, 12288\n"
"ds_read_b128 a[144:147], v228 offset:12288\n"
"ds_read_b128 a[148:151], v229 offset:12288\n"
"v_add_f32 v152, 0x71000000, v236\n"
"v_mfma_f32_16x16x32_bf16 v[160:163], a[108:111], a[60:63], v[160:163]\n"
"s_lshr_b32 s67, s61, 18\n"
"ds_read_b128 a[152:155], v228 offset:13312\n"
"ds_read_b128 a[156:159], v229 offset:13312\n"
"v_min_f32 v152, v152, v192\n"
"v_mfma_f32_16x16x32_bf16 v[160:163], a[112:115], a[64:67], v[160:163]\n"
"ds_read_b128 a[160:163], v228 offset:14336\n"
"v_add_f32 v153, 0xf1000000, v236\n"
"v_min_f32 v153, v153, v193\n"
"v_mfma_f32_16x16x32_bf16 v[160:163], a[116:119], a[68:71], v[160:163]\n"
"v_add_f32 v154, 0xf1c00000, v236\n"
"v_min_f32 v154, v154, v194\n"
"v_add_f32 v155, 0xf2200000, v236\n"
"v_mfma_f32_16x16x32_bf16 v[176:179], a[120:123], a[48:51], v[196:199]\n"
"v_min_f32 v155, v155, v195\n"
"v_add_f32 v180, 0xf3780000, v236\n"
"v_min_f32 v180, v180, v192\n"
"v_mfma_f32_16x16x32_bf16 v[176:179], a[124:127], a[52:55], v[176:179]\n"
"v_add_f32 v181, 0xf3840000, v236\n"
"v_min_f32 v181, v181, v193\n"
"v_add_f32 v182, 0xf38c0000, v236\n"
"v_mfma_f32_16x16x32_bf16 v[176:179], a[128:131], a[56:59], v[176:179]\n"
"v_min_f32 v182, v182, v194\n"
"v_add_f32 v183, 0xf3940000, v236\n"
"v_min_f32 v183, v183, v195\n"
"v_mfma_f32_16x16x32_bf16 v[176:179], a[132:135], a[60:63], v[176:179]\n"
"v_add_f32 v156, 0xf3fc0000, v236\n"
"v_min_f32 v156, v156, v192\n"
"v_add_f32 v157, 0xf4020000, v236\n"
"v_mfma_f32_16x16x32_bf16 v[176:179], a[136:139], a[64:67], v[176:179]\n"
"v_min_f32 v157, v157, v193\n"
"v_add_f32 v158, 0xf4060000, v236\n"
"v_min_f32 v158, v158, v194\n"
"v_mfma_f32_16x16x32_bf16 v[176:179], a[140:143], a[68:71], v[176:179]\n"
"ds_read_b128 a[164:167], v229 offset:14336\n"
"v_add_f32 v159, 0xf40a0000, v236\n"
"v_min_f32 v159, v159, v195\n"
"v_mfma_f32_16x16x32_bf16 v[152:155], a[96:99], a[24:27], v[152:155]\n"
"buffer_load_dwordx4 v234, s[40:43], s66 offen offset:0 lds\n"
"s_add_u32 m0, s97, 896\n"
"v_mfma_f32_16x16x32_bf16 v[152:155], a[100:103], a[28:31], v[152:155]\n"
"buffer_load_dwordx4 v234, s[40:43], s66 offen offset:128 lds\n"
"s_add_u32 m0, s97, 1792\n"
"v_mfma_f32_16x16x32_bf16 v[152:155], a[104:107], a[32:35], v[152:155]\n"
"buffer_load_dwordx4 v234, s[40:43], s66 offen offset:256 lds\n"
"v_mfma_f32_16x16x32_bf16 v[152:155], a[108:111], a[36:39], v[152:155]\n"
"s_add_u32 m0, s97, 12288\n"
"v_exp_f32 v160, v160\n"
"v_mfma_f32_16x16x32_bf16 v[152:155], a[112:115], a[40:43], v[152:155]\n"
"v_exp_f32 v161, v161\n"
"v_mfma_f32_16x16x32_bf16 v[152:155], a[116:119], a[44:47], v[152:155]\n"
"v_exp_f32 v162, v162\n"
"v_cvt_pk_bf16_f32 v160, v160, v161\n"
"v_mfma_f32_16x16x32_bf16 v[180:183], a[120:123], a[24:27], v[180:183]\n"
"v_exp_f32 v163, v163\n"
"v_mfma_f32_16x16x32_bf16 v[180:183], a[124:127], a[28:31], v[180:183]\n"
"v_exp_f32 v176, v176\n"
"v_cvt_pk_bf16_f32 v161, v162, v163\n"
"v_mfma_f32_16x16x32_bf16 v[180:183], a[128:131], a[32:35], v[180:183]\n"
"v_exp_f32 v177, v177\n"
"v_mfma_f32_16x16x32_bf16 v[180:183], a[132:135], a[36:39], v[180:183]\n"
"v_exp_f32 v178, v178\n"
"v_cvt_pk_bf16_f32 v162, v176, v177\n"
"v_mfma_f32_16x16x32_bf16 v[180:183], a[136:139], a[40:43], v[180:183]\n"
"v_exp_f32 v179, v179\n"
"v_mfma_f32_16x16x32_bf16 v[180:183], a[140:143], a[44:47], v[180:183]\n"
"v_exp_f32 v152, v152\n"
"v_mfma_f32_16x16x32_bf16 v[168:171], a[96:99], a[72:75], v[200:203]\n"
"ds_read_b128 a[96:99], v228 offset:18432\n"
"v_exp_f32 v153, v153\n"
"v_mfma_f32_16x16x32_bf16 v[168:171], a[100:103], a[76:79], v[168:171]\n"
"ds_read_b128 a[100:103], v229 offset:18432\n"
"v_exp_f32 v154, v154\n"
"v_mfma_f32_16x16x32_bf16 v[168:171], a[104:107], a[80:83], v[168:171]\n"
"v_exp_f32 v155, v155\n"
"v_cvt_pk_bf16_f32 v163, v178, v179\n"
"v_mfma_f32_16x16x32_bf16 v[168:171], a[108:111], a[84:87], v[168:171]\n"
"ds_read_b128 a[104:107], v228 offset:19456\n"
"v_exp_f32 v180, v180\n"
"v_mfma_f32_16x16x32_bf16 v[168:171], a[112:115], a[88:91], v[168:171]\n"
"v_exp_f32 v181, v181\n"
"v_cvt_pk_bf16_f32 v152, v152, v153\n"
"v_mfma_f32_16x16x32_bf16 v[168:171], a[116:119], a[92:95], v[168:171]\n"
"ds_read_b128 a[108:111], v229 offset:19456\n"
"v_exp_f32 v182, v182\n"
"v_mfma_f32_16x16x32_bf16 v[184:187], a[120:123], a[72:75], v[200:203]\n"
"v_exp_f32 v183, v183\n"
"v_cvt_pk_bf16_f32 v153, v154, v155\n"
"v_mfma_f32_16x16x32_bf16 v[184:187], a[124:127], a[76:79], v[184:187]\n"
"ds_read_b128 a[112:115], v228 offset:20480\n"
"ds_read_b128 a[116:119], v229 offset:20480\n"
"v_cvt_pk_bf16_f32 v154, v180, v181\n"
"v_mfma_f32_16x16x32_bf16 v[184:187], a[128:131], a[80:83], v[184:187]\n"
"v_exp_f32 v168, v168\n"
"v_mfma_f32_16x16x32_bf16 v[184:187], a[132:135], a[84:87], v[184:187]\n"
"v_exp_f32 v169, v169\n"
"v_mfma_f32_16x16x32_bf16 v[184:187], a[136:139], a[88:91], v[184:187]\n"
"s_waitcnt lgkmcnt(6)\n"
"v_exp_f32 v170, v170\n"
"v_mfma_f32_16x16x32_bf16 v[184:187], a[140:143], a[92:95], v[184:187]\n"
"v_exp_f32 v171, v171\n"
"v_mfma_f32_16x16x32_bf16 v[164:167], a[144:147], a[48:51], v[196:199]\n"
"v_cvt_pk_bf16_f32 v155, v182, v183\n"
"v_add_f32 v180, 0xf43e0000, v236\n"
"v_mfma_f32_16x16x32_bf16 v[164:167], a[148:151], a[52:55], v[164:167]\n"
"v_min_f32 v180, v180, v192\n"
"v_add_f32 v181, 0xf4420000, v236\n"
"v_mfma_f32_16x16x32_bf16 v[164:167], a[152:155], a[56:59], v[164:167]\n"
"v_exp_f32 v184, v184\n"
"v_mfma_f32_16x16x32_bf16 v[164:167], a[156:159], a[60:63], v[164:167]\n"
"v_exp_f32 v185, v185\n"
"v_mfma_f32_16x16x32_bf16 v[164:167], a[160:163], a[64:67], v[164:167]\n"
"s_waitcnt vmcnt(5) lgkmcnt(0)\n"
"v_exp_f32 v186, v186\n"
"s_barrier\n"
"v_mfma_f32_16x16x32_bf16 v[164:167], a[164:167], a[68:71], v[164:167]\n"
"ds_read_b64_tr_b16 a[120:121], v230 offset:24576\n"
"v_exp_f32 v187, v187\n"
"v_mfma_f32_16x16x32_bf16 v[176:179], a[96:99], a[48:51], v[196:199]\n"
"ds_read_b64_tr_b16 a[122:123], v230 offset:30720\n"
"ds_read_b64_tr_b16 a[124:125], v230 offset:36864\n"
"v_min_f32 v181, v181, v193\n"
"v_mfma_f32_16x16x32_bf16 v[176:179], a[100:103], a[52:55], v[176:179]\n"
"ds_read_b64_tr_b16 a[126:127], v230 offset:43008\n"
"ds_read_b64_tr_b16 a[128:129], v231 offset:24576\n"
"v_add_f32 v182, 0xf4460000, v236\n"
"v_mfma_f32_16x16x32_bf16 v[176:179], a[104:107], a[56:59], v[176:179]\n"
"ds_read_b64_tr_b16 a[130:131], v231 offset:30720\n"
"ds_read_b64_tr_b16 a[132:133], v231 offset:36864\n"
"v_min_f32 v182, v182, v194\n"
"v_mfma_f32_16x16x32_bf16 v[176:179], a[108:111], a[60:63], v[176:179]\n"
"ds_read_b64_tr_b16 a[134:135], v231 offset:43008\n"
"ds_read_b64_tr_b16 a[136:137], v232 offset:24576\n"
"v_add_f32 v183, 0xf44a0000, v236\n"
"v_mfma_f32_16x16x32_bf16 v[176:179], a[112:115], a[64:67], v[176:179]\n"
"buffer_load_dwordx4 v234, s[40:43], s77 offen offset:0 lds\n"
"v_min_f32 v183, v183, v195\n"
"v_mfma_f32_16x16x32_bf16 v[176:179], a[116:119], a[68:71], v[176:179]\n"
"s_add_u32 m0, s97, 13184\n"
"ds_read_b64_tr_b16 a[138:139], v232 offset:30720\n"
"v_cvt_pk_bf16_f32 v168, v168, v169\n"
"v_cvt_pk_bf16_f32 v169, v170, v171\n"
"v_mfma_f32_16x16x32_bf16 v[156:159], a[144:147], a[24:27], v[156:159]\n"
"ds_read_b64_tr_b16 a[140:141], v232 offset:36864\n"
"ds_read_b64_tr_b16 a[142:143], v232 offset:43008\n"
"v_cvt_pk_bf16_f32 v170, v184, v185\n"
"v_mfma_f32_16x16x32_bf16 v[156:159], a[148:151], a[28:31], v[156:159]\n"
"buffer_load_dwordx4 v234, s[40:43], s77 offen offset:128 lds\n"
"v_mfma_f32_16x16x32_bf16 v[156:159], a[152:155], a[32:35], v[156:159]\n"
"s_add_u32 m0, s97, 14080\n"
"v_exp_f32 v164, v164\n"
"v_mfma_f32_16x16x32_bf16 v[156:159], a[156:159], a[36:39], v[156:159]\n"
"v_exp_f32 v165, v165\n"
"v_mfma_f32_16x16x32_bf16 v[156:159], a[160:163], a[40:43], v[156:159]\n"
"v_exp_f32 v166, v166\n"
"v_mfma_f32_16x16x32_bf16 v[156:159], a[164:167], a[44:47], v[156:159]\n"
"v_exp_f32 v167, v167\n"
"v_mfma_f32_16x16x32_bf16 v[180:183], a[96:99], a[24:27], v[180:183]\n"
"buffer_load_dwordx4 v234, s[40:43], s77 offen offset:256 lds\n"
"v_mfma_f32_16x16x32_bf16 v[180:183], a[100:103], a[28:31], v[180:183]\n"
"s_add_u32 m0, s97, 24576\n"
"v_exp_f32 v176, v176\n"
"v_mfma_f32_16x16x32_bf16 v[180:183], a[104:107], a[32:35], v[180:183]\n"
"v_exp_f32 v177, v177\n"
"v_mfma_f32_16x16x32_bf16 v[180:183], a[108:111], a[36:39], v[180:183]\n"
"v_exp_f32 v178, v178\n"
"v_mfma_f32_16x16x32_bf16 v[180:183], a[112:115], a[40:43], v[180:183]\n"
"v_exp_f32 v179, v179\n"
"v_mfma_f32_16x16x32_bf16 v[180:183], a[116:119], a[44:47], v[180:183]\n"
"v_exp_f32 v156, v156\n"
"v_mfma_f32_16x16x32_bf16 v[172:175], a[144:147], a[72:75], v[200:203]\n"
"v_exp_f32 v157, v157\n"
"v_mfma_f32_16x16x32_bf16 v[172:175], a[148:151], a[76:79], v[172:175]\n"
"v_exp_f32 v158, v158\n"
"v_mfma_f32_16x16x32_bf16 v[172:175], a[152:155], a[80:83], v[172:175]\n"
"v_exp_f32 v159, v159\n"
"v_mfma_f32_16x16x32_bf16 v[172:175], a[156:159], a[84:87], v[172:175]\n"
"ds_read_b64_tr_b16 a[144:145], v233 offset:24576\n"
"v_cvt_pk_bf16_f32 v171, v186, v187\n"
"v_mfma_f32_16x16x32_bf16 v[172:175], a[160:163], a[88:91], v[172:175]\n"
"ds_read_b64_tr_b16 a[146:147], v233 offset:30720\n"
"v_cvt_pk_bf16_f32 v164, v164, v165\n"
"v_mfma_f32_16x16x32_bf16 v[172:175], a[164:167], a[92:95], v[172:175]\n"
"ds_read_b64_tr_b16 a[148:149], v233 offset:36864\n"
"v_cvt_pk_bf16_f32 v165, v166, v167\n"
"v_mfma_f32_16x16x32_bf16 v[184:187], a[96:99], a[72:75], v[200:203]\n"
"ds_read_b64_tr_b16 a[150:151], v233 offset:43008\n"
"v_cvt_pk_bf16_f32 v166, v176, v177\n"
"v_mfma_f32_16x16x32_bf16 v[184:187], a[100:103], a[76:79], v[184:187]\n"
"v_exp_f32 v180, v180\n"
"v_mfma_f32_16x16x32_bf16 v[184:187], a[104:107], a[80:83], v[184:187]\n"
"v_exp_f32 v181, v181\n"
"v_mfma_f32_16x16x32_bf16 v[184:187], a[108:111], a[84:87], v[184:187]\n"
"v_exp_f32 v182, v182\n"
"v_mfma_f32_16x16x32_bf16 v[184:187], a[112:115], a[88:91], v[184:187]\n"
"s_waitcnt lgkmcnt(4)\n"
"v_exp_f32 v183, v183\n"
"v_mfma_f32_16x16x32_bf16 v[184:187], a[116:119], a[92:95], v[184:187]\n"
"ds_read_b64_tr_b16 a[152:153], v230 offset:25600\n"
"v_cvt_pk_bf16_f32 v167, v178, v179\n"
"v_mfma_f32_16x16x32_bf16 v[96:99], a[120:123], v[160:163], v[96:99]\n"
"ds_read_b64_tr_b16 a[154:155], v230 offset:31744\n"
"v_cvt_pk_bf16_f32 v156, v156, v157\n"
"v_mfma_f32_16x16x32_bf16 v[96:99], a[124:127], v[164:167], v[96:99]\n"
"ds_read_b64_tr_b16 a[156:157], v230 offset:37888\n"
"v_cvt_pk_bf16_f32 v157, v158, v159\n"
"v_mfma_f32_16x16x32_bf16 v[100:103], a[132:135], v[164:167], v[100:103]\n"
"ds_read_b64_tr_b16 a[158:159], v230 offset:44032\n"
"v_cvt_pk_bf16_f32 v158, v180, v181\n"
"v_mfma_f32_16x16x32_bf16 v[100:103], a[128:131], v[160:163], v[100:103]\n"
"ds_read_b64_tr_b16 a[160:161], v231 offset:25600\n"
"v_cvt_pk_bf16_f32 v159, v182, v183\n"
"v_mfma_f32_16x16x32_bf16 v[104:107], a[136:139], v[160:163], v[104:107]\n"
"v_exp_f32 v172, v172\n"
"v_mfma_f32_16x16x32_bf16 v[104:107], a[140:143], v[164:167], v[104:107]\n"
"v_exp_f32 v173, v173\n"
"v_smfmac_f32_16x16x64_bf16 a[220:223], a[224:227], v[160:167], v237\n"
"v_exp_f32 v174, v174\n"
"v_mfma_f32_16x16x32_bf16 v[56:59], a[140:143], v[156:159], v[56:59]\n"
"v_exp_f32 v175, v175\n"
"v_mfma_f32_16x16x32_bf16 v[56:59], a[136:139], v[152:155], v[56:59]\n"
"ds_read_b64_tr_b16 a[162:163], v231 offset:31744\n"
"v_cvt_pk_bf16_f32 v172, v172, v173\n"
"v_mfma_f32_16x16x32_bf16 v[52:55], a[128:131], v[152:155], v[52:55]\n"
"ds_read_b64_tr_b16 a[164:165], v231 offset:37888\n"
"v_cvt_pk_bf16_f32 v173, v174, v175\n"
"v_mfma_f32_16x16x32_bf16 v[52:55], a[132:135], v[156:159], v[52:55]\n"
"v_exp_f32 v184, v184\n"
"v_mfma_f32_16x16x32_bf16 v[48:51], a[124:127], v[156:159], v[48:51]\n"
"v_exp_f32 v185, v185\n"
"v_mfma_f32_16x16x32_bf16 v[48:51], a[120:123], v[152:155], v[48:51]\n"
"v_exp_f32 v186, v186\n"
"v_smfmac_f32_16x16x64_bf16 a[216:219], a[228:231], v[152:159], v237\n"
"v_exp_f32 v187, v187\n"
"v_mfma_f32_16x16x32_bf16 a[168:171], a[120:123], v[168:171], a[168:171]\n"
"ds_read_b64_tr_b16 a[166:167], v231 offset:44032\n"
"v_cvt_pk_bf16_f32 v174, v184, v185\n"
"v_cvt_pk_bf16_f32 v175, v186, v187\n"
"s_nop 1\n"
"v_mfma_f32_16x16x32_bf16 a[168:171], a[124:127], v[172:175], a[168:171]\n"
"ds_read_b64_tr_b16 a[96:97], v232 offset:25600\n"
"v_mfma_f32_16x16x32_bf16 a[172:175], a[132:135], v[172:175], a[172:175]\n"
"ds_read_b64_tr_b16 a[98:99], v232 offset:31744\n"
"v_mfma_f32_16x16x32_bf16 a[172:175], a[128:131], v[168:171], a[172:175]\n"
"ds_read_b64_tr_b16 a[100:101], v232 offset:37888\n"
"v_mfma_f32_16x16x32_bf16 a[176:179], a[136:139], v[168:171], a[176:179]\n"
"ds_read_b64_tr_b16 a[102:103], v232 offset:44032\n"
"v_mfma_f32_16x16x32_bf16 a[176:179], a[140:143], v[172:175], a[176:179]\n"
"ds_read_b64_tr_b16 a[104:105], v233 offset:25600\n"
"v_smfmac_f32_16x16x64_bf16 a[220:223], a[228:231], v[168:175], v237\n"
"s_waitcnt lgkmcnt(5)\n"
"ds_read_b64_tr_b16 a[106:107], v233 offset:31744\n"
"v_mfma_f32_16x16x32_bf16 v[108:111], a[144:147], v[160:163], v[108:111]\n"
"ds_read_b64_tr_b16 a[108:109], v233 offset:37888\n"
"v_mfma_f32_16x16x32_bf16 v[108:111], a[148:151], v[164:167], v[108:111]\n"
"ds_read_b64_tr_b16 a[110:111], v233 offset:44032\n"
"v_mfma_f32_16x16x32_bf16 v[112:115], a[156:159], v[164:167], v[112:115]\n"
"v_mfma_f32_16x16x32_bf16 v[112:115], a[152:155], v[160:163], v[112:115]\n"
"v_mfma_f32_16x16x32_bf16 v[116:119], a[160:163], v[160:163], v[116:119]\n"
"v_mfma_f32_16x16x32_bf16 v[116:119], a[164:167], v[164:167], v[116:119]\n"
"v_mfma_f32_16x16x32_bf16 v[68:71], a[164:167], v[156:159], v[68:71]\n"
"v_mfma_f32_16x16x32_bf16 v[68:71], a[160:163], v[152:155], v[68:71]\n"
"v_mfma_f32_16x16x32_bf16 v[64:67], a[152:155], v[152:155], v[64:67]\n"
"v_mfma_f32_16x16x32_bf16 v[64:67], a[156:159], v[156:159], v[64:67]\n"
"v_mfma_f32_16x16x32_bf16 v[60:63], a[148:151], v[156:159], v[60:63]\n"
"s_waitcnt vmcnt(3) lgkmcnt(0)\n"
"s_barrier\n"
"ds_read_b64_tr_b16 a[112:113], v230 offset:26624\n"
"v_mfma_f32_16x16x32_bf16 v[60:63], a[144:147], v[152:155], v[60:63]\n"
"ds_read_b64_tr_b16 a[114:115], v230 offset:32768\n"
"ds_read_b64_tr_b16 a[116:117], v230 offset:38912\n"
"v_mfma_f32_16x16x32_bf16 a[180:183], a[144:147], v[168:171], a[180:183]\n"
"ds_read_b64_tr_b16 a[118:119], v230 offset:45056\n"
"v_mfma_f32_16x16x32_bf16 a[180:183], a[148:151], v[172:175], a[180:183]\n"
"ds_read_b128 a[120:123], v228 offset:6144\n"
"v_mfma_f32_16x16x32_bf16 a[184:187], a[156:159], v[172:175], a[184:187]\n"
"ds_read_b64_tr_b16 a[144:145], v231 offset:26624\n"
"v_mfma_f32_16x16x32_bf16 a[184:187], a[152:155], v[168:171], a[184:187]\n"
"ds_read_b64_tr_b16 a[146:147], v231 offset:32768\n"
"v_mfma_f32_16x16x32_bf16 a[188:191], a[160:163], v[168:171], a[188:191]\n"
"ds_read_b64_tr_b16 a[148:149], v231 offset:38912\n"
"v_mfma_f32_16x16x32_bf16 a[188:191], a[164:167], v[172:175], a[188:191]\n"
"buffer_load_dwordx4 v234, s[44:47], s66 offen offset:0 lds\n"
"v_mfma_f32_16x16x32_bf16 v[120:123], a[96:99], v[160:163], v[120:123]\n"
"ds_read_b64_tr_b16 a[150:151], v231 offset:45056\n"
"s_add_u32 m0, s97, 36864\n"
"v_mfma_f32_16x16x32_bf16 v[120:123], a[100:103], v[164:167], v[120:123]\n"
"ds_read_b64_tr_b16 a[156:157], v232 offset:38912\n"
"v_mfma_f32_16x16x32_bf16 v[124:127], a[108:111], v[164:167], v[124:127]\n"
"ds_read_b64_tr_b16 a[158:159], v232 offset:45056\n"
"s_waitcnt lgkmcnt(7)\n"
"v_mfma_f32_16x16x32_bf16 v[124:127], a[104:107], v[160:163], v[124:127]\n"
"buffer_load_dwordx4 v234, s[44:47], s77 offen offset:0 lds\n"
"v_mfma_f32_16x16x32_bf16 v[128:131], a[112:115], v[160:163], v[128:131]\n"
"ds_read_b64_tr_b16 a[152:153], v232 offset:26624\n"
"s_add_u32 m0, s97, 25472\n"
"v_mfma_f32_16x16x32_bf16 v[128:131], a[116:119], v[164:167], v[128:131]\n"
"ds_read_b64_tr_b16 a[154:155], v232 offset:32768\n"
"v_mfma_f32_16x16x32_bf16 v[80:83], a[116:119], v[156:159], v[80:83]\n"
"buffer_load_dwordx4 v234, s[44:47], s66 offen offset:128 lds\n"
"v_mfma_f32_16x16x32_bf16 v[80:83], a[112:115], v[152:155], v[80:83]\n"
"ds_read_b64_tr_b16 a[160:161], v233 offset:26624\n"
"s_add_u32 m0, s97, 37760\n"
"v_mfma_f32_16x16x32_bf16 v[76:79], a[104:107], v[152:155], v[76:79]\n"
"ds_read_b64_tr_b16 a[162:163], v233 offset:32768\n"
"v_mfma_f32_16x16x32_bf16 v[76:79], a[108:111], v[156:159], v[76:79]\n"
"ds_read_b64_tr_b16 a[164:165], v233 offset:38912\n"
"v_mfma_f32_16x16x32_bf16 v[72:75], a[100:103], v[156:159], v[72:75]\n"
"buffer_load_dwordx4 v234, s[44:47], s77 offen offset:128 lds\n"
"v_mfma_f32_16x16x32_bf16 v[72:75], a[96:99], v[152:155], v[72:75]\n"
"ds_read_b64_tr_b16 a[166:167], v233 offset:45056\n"
"s_add_u32 m0, s97, 26368\n"
"v_mfma_f32_16x16x32_bf16 a[192:195], a[96:99], v[168:171], a[192:195]\n"
"ds_read_b128 a[124:127], v229 offset:6144\n"
"v_mfma_f32_16x16x32_bf16 a[192:195], a[100:103], v[172:175], a[192:195]\n"
"ds_read_b128 a[128:131], v228 offset:7168\n"
"v_mfma_f32_16x16x32_bf16 a[196:199], a[108:111], v[172:175], a[196:199]\n"
"ds_read_b128 a[132:135], v229 offset:7168\n"
"v_mfma_f32_16x16x32_bf16 a[196:199], a[104:107], v[168:171], a[196:199]\n"
"ds_read_b128 a[136:139], v228 offset:8192\n"
"v_mfma_f32_16x16x32_bf16 a[200:203], a[112:115], v[168:171], a[200:203]\n"
"ds_read_b128 a[140:143], v229 offset:8192\n"
"v_mfma_f32_16x16x32_bf16 a[200:203], a[116:119], v[172:175], a[200:203]\n"
"s_waitcnt lgkmcnt(5)\n"
"ds_read_b128 a[96:99], v228 offset:0\n"
"v_mfma_f32_16x16x32_bf16 v[132:135], a[144:147], v[160:163], v[132:135]\n"
"ds_read_b128 a[100:103], v229 offset:0\n"
"v_mfma_f32_16x16x32_bf16 v[132:135], a[148:151], v[164:167], v[132:135]\n"
"ds_read_b128 a[108:111], v229 offset:1024\n"
"v_mfma_f32_16x16x32_bf16 v[136:139], a[156:159], v[164:167], v[136:139]\n"
"ds_read_b128 a[104:107], v228 offset:1024\n"
"v_mfma_f32_16x16x32_bf16 v[136:139], a[152:155], v[160:163], v[136:139]\n"
"ds_read_b128 a[112:115], v228 offset:2048\n"
"v_mfma_f32_16x16x32_bf16 v[140:143], a[160:163], v[160:163], v[140:143]\n"
"ds_read_b128 a[116:119], v229 offset:2048\n"
"v_mfma_f32_16x16x32_bf16 v[140:143], a[164:167], v[164:167], v[140:143]\n"
"v_mfma_f32_16x16x32_bf16 v[92:95], a[164:167], v[156:159], v[92:95]\n"
"v_mfma_f32_16x16x32_bf16 v[92:95], a[160:163], v[152:155], v[92:95]\n"
"v_mfma_f32_16x16x32_bf16 v[88:91], a[152:155], v[152:155], v[88:91]\n"
"v_mfma_f32_16x16x32_bf16 v[88:91], a[156:159], v[156:159], v[88:91]\n"
"v_mfma_f32_16x16x32_bf16 v[84:87], a[148:151], v[156:159], v[84:87]\n"
"v_mfma_f32_16x16x32_bf16 v[84:87], a[144:147], v[152:155], v[84:87]\n"
"v_mfma_f32_16x16x32_bf16 a[204:207], a[144:147], v[168:171], a[204:207]\n"
"s_waitcnt vmcnt(4) lgkmcnt(0)\n"
"s_barrier\n"
"v_mfma_f32_16x16x32_bf16 a[204:207], a[148:151], v[172:175], a[204:207]\n"
"buffer_load_dwordx4 v234, s[44:47], s66 offen offset:256 lds\n"
"v_mfma_f32_16x16x32_bf16 a[208:211], a[156:159], v[172:175], a[208:211]\n"
"s_add_u32 m0, s97, 38656\n"
"s_mul_i32 s66, s67, 24576\n"
"v_mfma_f32_16x16x32_bf16 a[208:211], a[152:155], v[168:171], a[208:211]\n"
"buffer_load_dwordx4 v234, s[44:47], s77 offen offset:256 lds\n"
"v_mfma_f32_16x16x32_bf16 a[212:215], a[160:163], v[168:171], a[212:215]\n"
"v_mfma_f32_16x16x32_bf16 a[212:215], a[164:167], v[172:175], a[212:215]\n"
"s_setpc_b64 s[62:63]\n"
".p2align 12\n"
".p2align 5\n"
"L_W0EPZ:\n"
"s_nop 7\n"
"s_branch L_W0A1\n"
"L_W1EPZ:\n"
"s_nop 7\n"
"s_branch L_W1POLL\n"
"L_W2EPZ:\n"
"s_nop 7\n"
"s_branch L_W2POLL\n"
"L_EPZ:\n"
"v_mbcnt_lo_u32_b32 v146, -1, 0\n"
"v_mbcnt_hi_u32_b32 v146, -1, v146\n"
"v_bfe_u32 v144, v146, 3, 1\n"
"v_mul_u32_u24 v144, 3072, v144\n"
"v_and_b32 v147, 7, v146\n"
"v_lshrrev_b32 v148, 5, v146\n"
"v_lshl_add_u32 v144, v147, 7, v144\n"
"v_xor_b32 v145, v148, v147\n"
"v_lshl_add_u32 v144, v145, 4, v144\n"
"v_readlane_b32 s61, v235, s60\n"
"v_bfe_u32 v145, v146, 4, 1\n"
"s_add_u32 s60, s60, 1\n"
"s_and_b32 s98, s61, 0x3ffff\n"
"v_lshl_add_u32 v144, v145, 3, v144\n"
"s_add_u32 s62, s64, s98\n"
"v_add_u32 v240, s72, v144\n"
"s_addc_u32 s63, s65, 0\n"
"v_mov_b32 v144, 0x3f80\n"
"v_mov_b32 v145, 1.0\n"
"v_cndmask_b32 v144, v144, v145, s[82:83]\n"
"s_lshr_b32 s67, s61, 18\n"
"v_xor_b32 v241, 32, v240\n"
"v_xor_b32 v242, 64, v240\n"
"v_xor_b32 v243, 96, v240\n"
"v_mov_b32 v248, 0\n"
"v_mov_b32 v249, 0\n"
"v_mov_b32 v252, 0\n"
"v_mov_b32 v253, 0\n"
"v_lshlrev_b32 v255, 4, v146\n"
"v_cndmask_b32 v250, 0, v144, s[68:69]\n"
"v_cndmask_b32 v251, 0, v144, s[70:71]\n"
"v_mov_b32 v244, 0\n"
"v_mov_b32 v245, 0\n"
"v_mov_b32 v246, 0\n"
"v_mov_b32 v247, 0\n"
"s_mul_i32 s66, s67, 24576\n"
"s_setpc_b64 s[62:63]\n"
"L_D0:\n"
"v_mfma_f32_16x16x32_bf16 v[152:155], a[96:99], a[24:27], v[192:195]\n"
"v_readlane_b32 s61, v235, s60\n"
"s_add_u32 s60, s60, 1\n"
"v_mfma_f32_16x16x32_bf16 v[152:155], a[100:103], a[28:31], v[152:155]\n"
"s_and_b32 s98, s61, 0x3ffff\n"
"s_add_u32 s62, s64, s98\n"
"s_addc_u32 s63, s65, 0\n"
"v_add_f32 v144, 0x71000000, v236\n"
"v_mfma_f32_16x16x32_bf16 v[152:155], a[104:107], a[32:35], v[152:155]\n"
"s_add_u32 m0, s97, 0\n"
"v_min_f32 v144, v144, v188\n"
"v_add_f32 v145, 0xf1000000, v236\n"
"v_min_f32 v145, v145, v189\n"
"v_mfma_f32_16x16x32_bf16 v[152:155], a[108:111], a[36:39], v[152:155]\n"
"s_add_u32 s77, s66, 12288\n"
"v_add_f32 v146, 0xf1c00000, v236\n"
"v_min_f32 v146, v146, v190\n"
"v_add_f32 v147, 0xf2200000, v236\n"
"v_mfma_f32_16x16x32_bf16 v[152:155], a[112:115], a[40:43], v[152:155]\n"
"s_lshr_b32 s67, s61, 18\n"
"v_min_f32 v147, v147, v191\n"
"v_add_f32 v180, 0xf3780000, v236\n"
"v_min_f32 v180, v180, v188\n"
"v_mfma_f32_16x16x32_bf16 v[152:155], a[116:119], a[44:47], v[152:155]\n"
"v_add_f32 v181, 0xf3840000, v236\n"
"v_min_f32 v181, v181, v189\n"
"v_add_f32 v182, 0xf38c0000, v236\n"
"v_mfma_f32_16x16x32_bf16 v[176:179], a[120:123], a[24:27], v[192:195]\n"
"v_min_f32 v182, v182, v190\n"
"v_add_f32 v183, 0xf3940000, v236\n"
"v_min_f32 v183, v183, v191\n"
"v_mfma_f32_16x16x32_bf16 v[176:179], a[124:127], a[28:31], v[176:179]\n"
"buffer_load_dwordx4 v234, s[40:43], s66 offen offset:0 lds\n"
"v_add_f32 v148, 0xf3fc0000, v236\n"
"v_mfma_f32_16x16x32_bf16 v[176:179], a[128:131], a[32:35], v[176:179]\n"
"s_add_u32 m0, s97, 896\n"
"v_min_f32 v148, v148, v188\n"
"v_add_f32 v149, 0xf4020000, v236\n"
"v_min_f32 v149, v149, v189\n"
"v_mfma_f32_16x16x32_bf16 v[176:179], a[132:135], a[36:39], v[176:179]\n"
"buffer_load_dwordx4 v234, s[40:43], s66 offen offset:128 lds\n"
"v_add_f32 v150, 0xf4060000, v236\n"
"v_mfma_f32_16x16x32_bf16 v[176:179], a[136:139], a[40:43], v[176:179]\n"
"s_add_u32 m0, s97, 1792\n"
"ds_read_b128 a[144:147], v228 offset:12288\n"
"ds_read_b128 a[148:151], v229 offset:12288\n"
"v_min_f32 v150, v150, v190\n"
"v_mfma_f32_16x16x32_bf16 v[176:179], a[140:143], a[44:47], v[176:179]\n"
"ds_read_b128 a[152:155], v228 offset:13312\n"
"v_add_f32 v151, 0xf40a0000, v236\n"
"v_min_f32 v151, v151, v191\n"
"v_mfma_f32_16x16x32_bf16 v[144:147], a[96:99], a[0:3], v[144:147]\n"
"buffer_load_dwordx4 v234, s[40:43], s66 offen offset:256 lds\n"
"v_mfma_f32_16x16x32_bf16 v[144:147], a[100:103], a[4:7], v[144:147]\n"
"s_add_u32 m0, s97, 12288\n"
"v_exp_f32 v152, v152\n"
"v_mfma_f32_16x16x32_bf16 v[144:147], a[104:107], a[8:11], v[144:147]\n"
"v_exp_f32 v153, v153\n"
"v_mfma_f32_16x16x32_bf16 v[144:147], a[108:111], a[12:15], v[144:147]\n"
"v_exp_f32 v154, v154\n"
"v_mfma_f32_16x16x32_bf16 v[144:147], a[112:115], a[16:19], v[144:147]\n"
"v_exp_f32 v155, v155\n"
"v_mfma_f32_16x16x32_bf16 v[144:147], a[116:119], a[20:23], v[144:147]\n"
"ds_read_b128 a[156:159], v229 offset:13312\n"
"v_cvt_pk_bf16_f32 v152, v152, v153\n"
"v_mfma_f32_16x16x32_bf16 v[180:183], a[120:123], a[0:3], v[180:183]\n"
"ds_read_b128 a[160:163], v228 offset:14336\n"
"v_cvt_pk_bf16_f32 v153, v154, v155\n"
"v_mfma_f32_16x16x32_bf16 v[180:183], a[124:127], a[4:7], v[180:183]\n"
"v_exp_f32 v176, v176\n"
"v_mfma_f32_16x16x32_bf16 v[180:183], a[128:131], a[8:11], v[180:183]\n"
"v_exp_f32 v177, v177\n"
"v_mfma_f32_16x16x32_bf16 v[180:183], a[132:135], a[12:15], v[180:183]\n"
"v_exp_f32 v178, v178\n"
"v_mfma_f32_16x16x32_bf16 v[180:183], a[136:139], a[16:19], v[180:183]\n"
"v_exp_f32 v179, v179\n"
"v_mfma_f32_16x16x32_bf16 v[180:183], a[140:143], a[20:23], v[180:183]\n"
"v_cvt_pk_bf16_f32 v154, v176, v177\n"
"v_cvt_pk_bf16_f32 v155, v178, v179\n"
"v_mfma_f32_16x16x32_bf16 v[160:163], a[96:99], a[48:51], v[196:199]\n"
"v_exp_f32 v144, v144\n"
"v_mfma_f32_16x16x32_bf16 v[160:163], a[100:103], a[52:55], v[160:163]\n"
"v_exp_f32 v145, v145\n"
"v_mfma_f32_16x16x32_bf16 v[160:163], a[104:107], a[56:59], v[160:163]\n"
"v_exp_f32 v146, v146\n"
"v_mfma_f32_16x16x32_bf16 v[160:163], a[108:111], a[60:63], v[160:163]\n"
"v_exp_f32 v147, v147\n"
"v_mfma_f32_16x16x32_bf16 v[160:163], a[112:115], a[64:67], v[160:163]\n"
"v_cvt_pk_bf16_f32 v144, v144, v145\n"
"v_cvt_pk_bf16_f32 v145, v146, v147\n"
"v_mfma_f32_16x16x32_bf16 v[160:163], a[116:119], a[68:71], v[160:163]\n"
"v_exp_f32 v180, v180\n"
"v_mfma_f32_16x16x32_bf16 v[184:187], a[120:123], a[48:51], v[196:199]\n"
"v_exp_f32 v181, v181\n"
"v_mfma_f32_16x16x32_bf16 v[184:187], a[124:127], a[52:55], v[184:187]\n"
"v_exp_f32 v182, v182\n"
"v_mfma_f32_16x16x32_bf16 v[184:187], a[128:131], a[56:59], v[184:187]\n"
"v_exp_f32 v183, v183\n"
"v_mfma_f32_16x16x32_bf16 v[184:187], a[132:135], a[60:63], v[184:187]\n"
"ds_read_b128 a[164:167], v229 offset:14336\n"
"v_cvt_pk_bf16_f32 v146, v180, v181\n"
"v_mfma_f32_16x16x32_bf16 v[184:187], a[136:139], a[64:67], v[184:187]\n"
"v_exp_f32 v160, v160\n"
"v_mfma_f32_16x16x32_bf16 v[184:187], a[140:143], a[68:71], v[184:187]\n"
"v_exp_f32 v161, v161\n"
"v_mfma_f32_16x16x32_bf16 v[168:171], a[96:99], a[72:75], v[200:203]\n"
"v_exp_f32 v162, v162\n"
"v_mfma_f32_16x16x32_bf16 v[168:171], a[100:103], a[76:79], v[168:171]\n"
"v_exp_f32 v163, v163\n"
"v_mfma_f32_16x16x32_bf16 v[168:171], a[104:107], a[80:83], v[168:171]\n"
"ds_read_b128 a[96:99], v228 offset:18432\n"
"v_cvt_pk_bf16_f32 v147, v182, v183\n"
"v_mfma_f32_16x16x32_bf16 v[168:171], a[108:111], a[84:87], v[168:171]\n"
"ds_read_b128 a[100:103], v229 offset:18432\n"
"v_cvt_pk_bf16_f32 v160, v160, v161\n"
"v_mfma_f32_16x16x32_bf16 v[168:171], a[112:115], a[88:91], v[168:171]\n"
"v_exp_f32 v184, v184\n"
"v_mfma_f32_16x16x32_bf16 v[168:171], a[116:119], a[92:95], v[168:171]\n"
"v_exp_f32 v185, v185\n"
"v_mfma_f32_16x16x32_bf16 v[176:179], a[120:123], a[72:75], v[200:203]\n"
"v_exp_f32 v186, v186\n"
"v_mfma_f32_16x16x32_bf16 v[176:179], a[124:127], a[76:79], v[176:179]\n"
"v_exp_f32 v187, v187\n"
"v_mfma_f32_16x16x32_bf16 v[176:179], a[128:131], a[80:83], v[176:179]\n"
"ds_read_b128 a[104:107], v228 offset:19456\n"
"v_cvt_pk_bf16_f32 v161, v162, v163\n"
"v_mfma_f32_16x16x32_bf16 v[176:179], a[132:135], a[84:87], v[176:179]\n"
"ds_read_b128 a[108:111], v229 offset:19456\n"
"v_cvt_pk_bf16_f32 v162, v184, v185\n"
"v_mfma_f32_16x16x32_bf16 v[176:179], a[136:139], a[88:91], v[176:179]\n"
"s_waitcnt lgkmcnt(4)\n"
"ds_read_b128 a[112:115], v228 offset:20480\n"
"v_cvt_pk_bf16_f32 v163, v186, v187\n"
"v_mfma_f32_16x16x32_bf16 v[176:179], a[140:143], a[92:95], v[176:179]\n"
"ds_read_b128 a[116:119], v229 offset:20480\n"
"v_add_f32 v184, 0xf43e0000, v236\n"
"v_mfma_f32_16x16x32_bf16 v[156:159], a[144:147], a[24:27], v[192:195]\n"
"v_min_f32 v184, v184, v188\n"
"v_add_f32 v185, 0xf4420000, v236\n"
"v_mfma_f32_16x16x32_bf16 v[156:159], a[148:151], a[28:31], v[156:159]\n"
"v_min_f32 v185, v185, v189\n"
"v_add_f32 v186, 0xf4460000, v236\n"
"v_mfma_f32_16x16x32_bf16 v[156:159], a[152:155], a[32:35], v[156:159]\n"
"v_exp_f32 v168, v168\n"
"v_mfma_f32_16x16x32_bf16 v[156:159], a[156:159], a[36:39], v[156:159]\n"
"v_exp_f32 v169, v169\n"
"v_mfma_f32_16x16x32_bf16 v[156:159], a[160:163], a[40:43], v[156:159]\n"
"v_exp_f32 v170, v170\n"
"v_mfma_f32_16x16x32_bf16 v[156:159], a[164:167], a[44:47], v[156:159]\n"
"s_waitcnt vmcnt(5) lgkmcnt(0)\n"
"s_barrier\n"
"ds_read_b64_tr_b16 a[120:121], v230 offset:24576\n"
"v_mfma_f32_16x16x32_bf16 v[180:183], a[96:99], a[24:27], v[192:195]\n"
"v_exp_f32 v171, v171\n"
"v_mfma_f32_16x16x32_bf16 v[180:183], a[100:103], a[28:31], v[180:183]\n"
"v_exp_f32 v176, v176\n"
"v_mfma_f32_16x16x32_bf16 v[180:183], a[104:107], a[32:35], v[180:183]\n"
"v_exp_f32 v177, v177\n"
"v_mfma_f32_16x16x32_bf16 v[180:183], a[108:111], a[36:39], v[180:183]\n"
"v_exp_f32 v178, v178\n"
"v_mfma_f32_16x16x32_bf16 v[180:183], a[112:115], a[40:43], v[180:183]\n"
"v_exp_f32 v179, v179\n"
"v_mfma_f32_16x16x32_bf16 v[180:183], a[116:119], a[44:47], v[180:183]\n"
"buffer_load_dwordx4 v234, s[40:43], s77 offen offset:0 lds\n"
"v_mfma_f32_16x16x32_bf16 v[148:151], a[144:147], a[0:3], v[148:151]\n"
"s_add_u32 m0, s97, 13184\n"
"ds_read_b64_tr_b16 a[122:123], v230 offset:30720\n"
"v_min_f32 v186, v186, v190\n"
"v_mfma_f32_16x16x32_bf16 v[148:151], a[148:151], a[4:7], v[148:151]\n"
"ds_read_b64_tr_b16 a[124:125], v230 offset:36864\n"
"v_add_f32 v187, 0xf44a0000, v236\n"
"v_mfma_f32_16x16x32_bf16 v[148:151], a[152:155], a[8:11], v[148:151]\n"
"ds_read_b64_tr_b16 a[126:127], v230 offset:43008\n"
"v_min_f32 v187, v187, v191\n"
"v_mfma_f32_16x16x32_bf16 v[148:151], a[156:159], a[12:15], v[148:151]\n"
"ds_read_b64_tr_b16 a[128:129], v231 offset:24576\n"
"v_cvt_pk_bf16_f32 v168, v168, v169\n"
"v_mfma_f32_16x16x32_bf16 v[148:151], a[160:163], a[16:19], v[148:151]\n"
"buffer_load_dwordx4 v234, s[40:43], s77 offen offset:128 lds\n"
"v_mfma_f32_16x16x32_bf16 v[148:151], a[164:167], a[20:23], v[148:151]\n"
"s_add_u32 m0, s97, 14080\n"
"ds_read_b64_tr_b16 a[130:131], v231 offset:30720\n"
"v_cvt_pk_bf16_f32 v169, v170, v171\n"
"v_mfma_f32_16x16x32_bf16 v[184:187], a[96:99], a[0:3], v[184:187]\n"
"ds_read_b64_tr_b16 a[132:133], v231 offset:36864\n"
"v_cvt_pk_bf16_f32 v170, v176, v177\n"
"v_mfma_f32_16x16x32_bf16 v[184:187], a[100:103], a[4:7], v[184:187]\n"
"ds_read_b64_tr_b16 a[134:135], v231 offset:43008\n"
"v_cvt_pk_bf16_f32 v171, v178, v179\n"
"v_mfma_f32_16x16x32_bf16 v[184:187], a[104:107], a[8:11], v[184:187]\n"
"v_exp_f32 v156, v156\n"
"v_mfma_f32_16x16x32_bf16 v[184:187], a[108:111], a[12:15], v[184:187]\n"
"v_exp_f32 v157, v157\n"
"v_mfma_f32_16x16x32_bf16 v[184:187], a[112:115], a[16:19], v[184:187]\n"
"buffer_load_dwordx4 v234, s[40:43], s77 offen offset:256 lds\n"
"v_mfma_f32_16x16x32_bf16 v[184:187], a[116:119], a[20:23], v[184:187]\n"
"s_add_u32 m0, s97, 24576\n"
"v_exp_f32 v158, v158\n"
"v_mfma_f32_16x16x32_bf16 v[164:167], a[144:147], a[48:51], v[196:199]\n"
"v_exp_f32 v159, v159\n"
"v_mfma_f32_16x16x32_bf16 v[164:167], a[148:151], a[52:55], v[164:167]\n"
"ds_read_b64_tr_b16 a[136:137], v232 offset:24576\n"
"v_cvt_pk_bf16_f32 v156, v156, v157\n"
"v_mfma_f32_16x16x32_bf16 v[164:167], a[152:155], a[56:59], v[164:167]\n"
"ds_read_b64_tr_b16 a[138:139], v232 offset:30720\n"
"v_cvt_pk_bf16_f32 v157, v158, v159\n"
"v_mfma_f32_16x16x32_bf16 v[164:167], a[156:159], a[60:63], v[164:167]\n"
"v_exp_f32 v180, v180\n"
"v_mfma_f32_16x16x32_bf16 v[164:167], a[160:163], a[64:67], v[164:167]\n"
"v_exp_f32 v181, v181\n"
"v_mfma_f32_16x16x32_bf16 v[164:167], a[164:167], a[68:71], v[164:167]\n"
"v_exp_f32 v182, v182\n"
"v_mfma_f32_16x16x32_bf16 v[176:179], a[96:99], a[48:51], v[196:199]\n"
"v_exp_f32 v183, v183\n"
"v_mfma_f32_16x16x32_bf16 v[176:179], a[100:103], a[52:55], v[176:179]\n"
"ds_read_b64_tr_b16 a[140:141], v232 offset:36864\n"
"v_cvt_pk_bf16_f32 v158, v180, v181\n"
"v_mfma_f32_16x16x32_bf16 v[176:179], a[104:107], a[56:59], v[176:179]\n"
"ds_read_b64_tr_b16 a[142:143], v232 offset:43008\n"
"v_cvt_pk_bf16_f32 v159, v182, v183\n"
"v_mfma_f32_16x16x32_bf16 v[176:179], a[108:111], a[60:63], v[176:179]\n"
"v_exp_f32 v148, v148\n"
"v_mfma_f32_16x16x32_bf16 v[176:179], a[112:115], a[64:67], v[176:179]\n"
"v_exp_f32 v149, v149\n"
"v_mfma_f32_16x16x32_bf16 v[176:179], a[116:119], a[68:71], v[176:179]\n"
"v_exp_f32 v150, v150\n"
"v_mfma_f32_16x16x32_bf16 v[172:175], a[144:147], a[72:75], v[200:203]\n"
"v_exp_f32 v151, v151\n"
"v_mfma_f32_16x16x32_bf16 v[172:175], a[148:151], a[76:79], v[172:175]\n"
"v_exp_f32 v184, v184\n"
"v_mfma_f32_16x16x32_bf16 v[172:175], a[152:155], a[80:83], v[172:175]\n"
"v_exp_f32 v185, v185\n"
"v_mfma_f32_16x16x32_bf16 v[172:175], a[156:159], a[84:87], v[172:175]\n"
"v_exp_f32 v186, v186\n"
"v_mfma_f32_16x16x32_bf16 v[172:175], a[160:163], a[88:91], v[172:175]\n"
"v_exp_f32 v187, v187\n"
"v_mfma_f32_16x16x32_bf16 v[172:175], a[164:167], a[92:95], v[172:175]\n"
"v_exp_f32 v164, v164\n"
"v_mfma_f32_16x16x32_bf16 v[180:183], a[96:99], a[72:75], v[200:203]\n"
"v_exp_f32 v165, v165\n"
"v_mfma_f32_16x16x32_bf16 v[180:183], a[100:103], a[76:79], v[180:183]\n"
"v_exp_f32 v166, v166\n"
"v_mfma_f32_16x16x32_bf16 v[180:183], a[104:107], a[80:83], v[180:183]\n"
"v_exp_f32 v167, v167\n"
"v_mfma_f32_16x16x32_bf16 v[180:183], a[108:111], a[84:87], v[180:183]\n"
"ds_read_b64_tr_b16 a[144:145], v233 offset:24576\n"
"v_cvt_pk_bf16_f32 v148, v148, v149\n"
"v_mfma_f32_16x16x32_bf16 v[180:183], a[112:115], a[88:91], v[180:183]\n"
"s_waitcnt lgkmcnt(1)\n"
"ds_read_b64_tr_b16 a[146:147], v233 offset:30720\n"
"v_cvt_pk_bf16_f32 v149, v150, v151\n"
"v_mfma_f32_16x16x32_bf16 v[180:183], a[116:119], a[92:95], v[180:183]\n"
"ds_read_b64_tr_b16 a[148:149], v233 offset:36864\n"
"v_cvt_pk_bf16_f32 v150, v184, v185\n"
"v_mfma_f32_16x16x32_bf16 v[48:51], a[120:123], v[152:155], v[48:51]\n"
"ds_read_b64_tr_b16 a[150:151], v233 offset:43008\n"
"v_cvt_pk_bf16_f32 v151, v186, v187\n"
"v_mfma_f32_16x16x32_bf16 v[48:51], a[124:127], v[156:159], v[48:51]\n"
"v_exp_f32 v176, v176\n"
"v_mfma_f32_16x16x32_bf16 v[52:55], a[132:135], v[156:159], v[52:55]\n"
"v_exp_f32 v177, v177\n"
"v_mfma_f32_16x16x32_bf16 v[52:55], a[128:131], v[152:155], v[52:55]\n"
"v_exp_f32 v178, v178\n"
"v_mfma_f32_16x16x32_bf16 v[56:59], a[136:139], v[152:155], v[56:59]\n"
"v_exp_f32 v179, v179\n"
"v_mfma_f32_16x16x32_bf16 v[56:59], a[140:143], v[156:159], v[56:59]\n"
"ds_read_b64_tr_b16 a[152:153], v230 offset:25600\n"
"v_cvt_pk_bf16_f32 v164, v164, v165\n"
"v_smfmac_f32_16x16x64_bf16 a[216:219], a[228:231], v[152:159], v237\n"
"ds_read_b64_tr_b16 a[154:155], v230 offset:31744\n"
"v_cvt_pk_bf16_f32 v165, v166, v167\n"
"v_mfma_f32_16x16x32_bf16 v[8:11], a[140:143], v[148:151], v[8:11]\n"
"ds_read_b64_tr_b16 a[156:157], v230 offset:37888\n"
"v_cvt_pk_bf16_f32 v166, v176, v177\n"
"v_mfma_f32_16x16x32_bf16 v[8:11], a[136:139], v[144:147], v[8:11]\n"
"ds_read_b64_tr_b16 a[158:159], v230 offset:44032\n"
"v_cvt_pk_bf16_f32 v167, v178, v179\n"
"v_mfma_f32_16x16x32_bf16 v[4:7], a[128:131], v[144:147], v[4:7]\n"
"v_exp_f32 v172, v172\n"
"v_mfma_f32_16x16x32_bf16 v[4:7], a[132:135], v[148:151], v[4:7]\n"
"v_exp_f32 v173, v173\n"
"v_mfma_f32_16x16x32_bf16 v[0:3], a[124:127], v[148:151], v[0:3]\n"
"v_exp_f32 v174, v174\n"
"v_mfma_f32_16x16x32_bf16 v[0:3], a[120:123], v[144:147], v[0:3]\n"
"v_exp_f32 v175, v175\n"
"v_smfmac_f32_16x16x64_bf16 a[216:219], a[224:227], v[144:151], v237\n"
"ds_read_b64_tr_b16 a[160:161], v231 offset:25600\n"
"v_cvt_pk_bf16_f32 v172, v172, v173\n"
"v_mfma_f32_16x16x32_bf16 v[96:99], a[120:123], v[160:163], v[96:99]\n"
"ds_read_b64_tr_b16 a[162:163], v231 offset:31744\n"
"v_cvt_pk_bf16_f32 v173, v174, v175\n"
"v_mfma_f32_16x16x32_bf16 v[96:99], a[124:127], v[164:167], v[96:99]\n"
"v_exp_f32 v180, v180\n"
"v_mfma_f32_16x16x32_bf16 v[100:103], a[132:135], v[164:167], v[100:103]\n"
"v_exp_f32 v181, v181\n"
"v_mfma_f32_16x16x32_bf16 v[100:103], a[128:131], v[160:163], v[100:103]\n"
"v_exp_f32 v182, v182\n"
"v_mfma_f32_16x16x32_bf16 v[104:107], a[136:139], v[160:163], v[104:107]\n"
"v_exp_f32 v183, v183\n"
"v_mfma_f32_16x16x32_bf16 v[104:107], a[140:143], v[164:167], v[104:107]\n"
"ds_read_b64_tr_b16 a[164:165], v231 offset:37888\n"
"v_cvt_pk_bf16_f32 v174, v180, v181\n"
"v_smfmac_f32_16x16x64_bf16 a[220:223], a[224:227], v[160:167], v237\n"
"ds_read_b64_tr_b16 a[166:167], v231 offset:44032\n"
"v_cvt_pk_bf16_f32 v175, v182, v183\n"
"s_nop 1\n"
"v_mfma_f32_16x16x32_bf16 a[176:179], a[140:143], v[172:175], a[176:179]\n"
"ds_read_b64_tr_b16 a[96:97], v232 offset:25600\n"
"v_mfma_f32_16x16x32_bf16 a[176:179], a[136:139], v[168:171], a[176:179]\n"
"ds_read_b64_tr_b16 a[98:99], v232 offset:31744\n"
"v_mfma_f32_16x16x32_bf16 a[172:175], a[128:131], v[168:171], a[172:175]\n"
"ds_read_b64_tr_b16 a[100:101], v232 offset:37888\n"
"v_mfma_f32_16x16x32_bf16 a[172:175], a[132:135], v[172:175], a[172:175]\n"
"ds_read_b64_tr_b16 a[102:103], v232 offset:44032\n"
"v_mfma_f32_16x16x32_bf16 a[168:171], a[124:127], v[172:175], a[168:171]\n"
"ds_read_b64_tr_b16 a[104:105], v233 offset:25600\n"
"v_mfma_f32_16x16x32_bf16 a[168:171], a[120:123], v[168:171], a[168:171]\n"
"ds_read_b64_tr_b16 a[106:107], v233 offset:31744\n"
"v_smfmac_f32_16x16x64_bf16 a[220:223], a[228:231], v[168:175], v237\n"
"s_waitcnt lgkmcnt(6)\n"
"ds_read_b64_tr_b16 a[108:109], v233 offset:37888\n"
"v_mfma_f32_16x16x32_bf16 v[60:63], a[144:147], v[152:155], v[60:63]\n"
"ds_read_b64_tr_b16 a[110:111], v233 offset:44032\n"
"v_mfma_f32_16x16x32_bf16 v[60:63], a[148:151], v[156:159], v[60:63]\n"
"v_mfma_f32_16x16x32_bf16 v[64:67], a[156:159], v[156:159], v[64:67]\n"
"v_mfma_f32_16x16x32_bf16 v[64:67], a[152:155], v[152:155], v[64:67]\n"
"v_mfma_f32_16x16x32_bf16 v[68:71], a[160:163], v[152:155], v[68:71]\n"
"v_mfma_f32_16x16x32_bf16 v[68:71], a[164:167], v[156:159], v[68:71]\n"
"v_mfma_f32_16x16x32_bf16 v[20:23], a[164:167], v[148:151], v[20:23]\n"
"v_mfma_f32_16x16x32_bf16 v[20:23], a[160:163], v[144:147], v[20:23]\n"
"v_mfma_f32_16x16x32_bf16 v[16:19], a[152:155], v[144:147], v[16:19]\n"
"v_mfma_f32_16x16x32_bf16 v[16:19], a[156:159], v[148:151], v[16:19]\n"
"v_mfma_f32_16x16x32_bf16 v[12:15], a[148:151], v[148:151], v[12:15]\n"
"v_mfma_f32_16x16x32_bf16 v[12:15], a[144:147], v[144:147], v[12:15]\n"
"v_mfma_f32_16x16x32_bf16 v[108:111], a[144:147], v[160:163], v[108:111]\n"
"v_mfma_f32_16x16x32_bf16 v[108:111], a[148:151], v[164:167], v[108:111]\n"
"v_mfma_f32_16x16x32_bf16 v[112:115], a[156:159], v[164:167], v[112:115]\n"
"v_mfma_f32_16x16x32_bf16 v[112:115], a[152:155], v[160:163], v[112:115]\n"
"v_mfma_f32_16x16x32_bf16 v[116:119], a[160:163], v[160:163], v[116:119]\n"
"s_waitcnt vmcnt(3) lgkmcnt(0)\n"
"s_barrier\n"
"ds_read_b64_tr_b16 a[112:113], v230 offset:26624\n"
"v_mfma_f32_16x16x32_bf16 v[116:119], a[164:167], v[164:167], v[116:119]\n"
"ds_read_b64_tr_b16 a[114:115], v230 offset:32768\n"
"v_mfma_f32_16x16x32_bf16 a[188:191], a[164:167], v[172:175], a[188:191]\n"
"ds_read_b64_tr_b16 a[116:117], v230 offset:38912\n"
"v_mfma_f32_16x16x32_bf16 a[188:191], a[160:163], v[168:171], a[188:191]\n"
"ds_read_b64_tr_b16 a[118:119], v230 offset:45056\n"
"v_mfma_f32_16x16x32_bf16 a[184:187], a[152:155], v[168:171], a[184:187]\n"
"buffer_load_dwordx4 v234, s[44:47], s66 offen offset:0 lds\n"
"v_mfma_f32_16x16x32_bf16 a[184:187], a[156:159], v[172:175], a[184:187]\n"
"s_add_u32 m0, s97, 36864\n"
"ds_read_b64_tr_b16 a[164:165], v233 offset:38912\n"
"v_mfma_f32_16x16x32_bf16 a[180:183], a[148:151], v[172:175], a[180:183]\n"
"ds_read_b64_tr_b16 a[166:167], v233 offset:45056\n"
"v_mfma_f32_16x16x32_bf16 a[180:183], a[144:147], v[168:171], a[180:183]\n"
"ds_read_b64_tr_b16 a[160:161], v233 offset:26624\n"
"v_mfma_f32_16x16x32_bf16 v[72:75], a[96:99], v[152:155], v[72:75]\n"
"buffer_load_dwordx4 v234, s[44:47], s77 offen offset:0 lds\n"
"v_mfma_f32_16x16x32_bf16 v[72:75], a[100:103], v[156:159], v[72:75]\n"
"s_add_u32 m0, s97, 25472\n"
"ds_read_b64_tr_b16 a[162:163], v233 offset:32768\n"
"v_mfma_f32_16x16x32_bf16 v[76:79], a[108:111], v[156:159], v[76:79]\n"
"ds_read_b64_tr_b16 a[152:153], v232 offset:26624\n"
"v_mfma_f32_16x16x32_bf16 v[76:79], a[104:107], v[152:155], v[76:79]\n"
"s_waitcnt lgkmcnt(5)\n"
"ds_read_b64_tr_b16 a[154:155], v232 offset:32768\n"
"v_mfma_f32_16x16x32_bf16 v[80:83], a[112:115], v[152:155], v[80:83]\n"
"ds_read_b64_tr_b16 a[156:157], v232 offset:38912\n"
"v_mfma_f32_16x16x32_bf16 v[80:83], a[116:119], v[156:159], v[80:83]\n"
"buffer_load_dwordx4 v234, s[44:47], s66 offen offset:128 lds\n"
"v_mfma_f32_16x16x32_bf16 v[32:35], a[116:119], v[148:151], v[32:35]\n"
"s_add_u32 m0, s97, 37760\n"
"ds_read_b64_tr_b16 a[158:159], v232 offset:45056\n"
"v_mfma_f32_16x16x32_bf16 v[32:35], a[112:115], v[144:147], v[32:35]\n"
"ds_read_b64_tr_b16 a[148:149], v231 offset:38912\n"
"v_mfma_f32_16x16x32_bf16 v[28:31], a[104:107], v[144:147], v[28:31]\n"
"ds_read_b64_tr_b16 a[150:151], v231 offset:45056\n"
"v_mfma_f32_16x16x32_bf16 v[28:31], a[108:111], v[148:151], v[28:31]\n"
"ds_read_b64_tr_b16 a[144:145], v231 offset:26624\n"
"v_mfma_f32_16x16x32_bf16 v[24:27], a[100:103], v[148:151], v[24:27]\n"
"buffer_load_dwordx4 v234, s[44:47], s77 offen offset:128 lds\n"
"v_mfma_f32_16x16x32_bf16 v[24:27], a[96:99], v[144:147], v[24:27]\n"
"ds_read_b64_tr_b16 a[146:147], v231 offset:32768\n"
"s_add_u32 m0, s97, 26368\n"
"v_mfma_f32_16x16x32_bf16 v[120:123], a[96:99], v[160:163], v[120:123]\n"
"ds_read_b128 a[120:123], v228 offset:6144\n"
"v_mfma_f32_16x16x32_bf16 v[120:123], a[100:103], v[164:167], v[120:123]\n"
"ds_read_b128 a[124:127], v229 offset:6144\n"
"v_mfma_f32_16x16x32_bf16 v[124:127], a[108:111], v[164:167], v[124:127]\n"
"ds_read_b128 a[128:131], v228 offset:7168\n"
"v_mfma_f32_16x16x32_bf16 v[124:127], a[104:107], v[160:163], v[124:127]\n"
"ds_read_b128 a[132:135], v229 offset:7168\n"
"v_mfma_f32_16x16x32_bf16 v[128:131], a[112:115], v[160:163], v[128:131]\n"
"ds_read_b128 a[136:139], v228 offset:8192\n"
"v_mfma_f32_16x16x32_bf16 v[128:131], a[116:119], v[164:167], v[128:131]\n"
"ds_read_b128 a[140:143], v229 offset:8192\n"
"v_mfma_f32_16x16x32_bf16 a[200:203], a[116:119], v[172:175], a[200:203]\n"
"ds_read_b128 a[116:119], v229 offset:2048\n"
"v_mfma_f32_16x16x32_bf16 a[200:203], a[112:115], v[168:171], a[200:203]\n"
"ds_read_b128 a[112:115], v228 offset:2048\n"
"v_mfma_f32_16x16x32_bf16 a[196:199], a[104:107], v[168:171], a[196:199]\n"
"ds_read_b128 a[104:107], v228 offset:1024\n"
"v_mfma_f32_16x16x32_bf16 a[196:199], a[108:111], v[172:175], a[196:199]\n"
"ds_read_b128 a[108:111], v229 offset:1024\n"
"v_mfma_f32_16x16x32_bf16 a[192:195], a[100:103], v[172:175], a[192:195]\n"
"s_waitcnt lgkmcnt(10)\n"
"ds_read_b128 a[100:103], v229 offset:0\n"
"v_mfma_f32_16x16x32_bf16 a[192:195], a[96:99], v[168:171], a[192:195]\n"
"ds_read_b128 a[96:99], v228 offset:0\n"
"v_mfma_f32_16x16x32_bf16 v[84:87], a[144:147], v[152:155], v[84:87]\n"
"v_mfma_f32_16x16x32_bf16 v[84:87], a[148:151], v[156:159], v[84:87]\n"
"v_mfma_f32_16x16x32_bf16 v[88:91], a[156:159], v[156:159], v[88:91]\n"
"v_mfma_f32_16x16x32_bf16 v[88:91], a[152:155], v[152:155], v[88:91]\n"
"v_mfma_f32_16x16x32_bf16 v[92:95], a[160:163], v[152:155], v[92:95]\n"
"v_mfma_f32_16x16x32_bf16 v[92:95], a[164:167], v[156:159], v[92:95]\n"
"v_mfma_f32_16x16x32_bf16 v[44:47], a[164:167], v[148:151], v[44:47]\n"
"v_mfma_f32_16x16x32_bf16 v[44:47], a[160:163], v[144:147], v[44:47]\n"
"v_mfma_f32_16x16x32_bf16 v[40:43], a[152:155], v[144:147], v[40:43]\n"
"v_mfma_f32_16x16x32_bf16 v[40:43], a[156:159], v[148:151], v[40:43]\n"
"v_mfma_f32_16x16x32_bf16 v[36:39], a[148:151], v[148:151], v[36:39]\n"
"s_waitcnt vmcnt(4) lgkmcnt(0)\n"
"s_barrier\n"
"v_mfma_f32_16x16x32_bf16 v[36:39], a[144:147], v[144:147], v[36:39]\n"
"v_mfma_f32_16x16x32_bf16 v[132:135], a[144:147], v[160:163], v[132:135]\n"
"v_mfma_f32_16x16x32_bf16 v[132:135], a[148:151], v[164:167], v[132:135]\n"
"buffer_load_dwordx4 v234, s[44:47], s66 offen offset:256 lds\n"
"v_mfma_f32_16x16x32_bf16 v[136:139], a[156:159], v[164:167], v[136:139]\n"
"s_add_u32 m0, s97, 38656\n"
"s_mul_i32 s66, s67, 24576\n"
"v_mfma_f32_16x16x32_bf16 v[136:139], a[152:155], v[160:163], v[136:139]\n"
"v_mfma_f32_16x16x32_bf16 v[140:143], a[160:163], v[160:163], v[140:143]\n"
"v_mfma_f32_16x16x32_bf16 v[140:143], a[164:167], v[164:167], v[140:143]\n"
"v_mfma_f32_16x16x32_bf16 a[212:215], a[164:167], v[172:175], a[212:215]\n"
"buffer_load_dwordx4 v234, s[44:47], s77 offen offset:256 lds\n"
"v_mfma_f32_16x16x32_bf16 a[212:215], a[160:163], v[168:171], a[212:215]\n"
"v_mfma_f32_16x16x32_bf16 a[208:211], a[152:155], v[168:171], a[208:211]\n"
"v_mfma_f32_16x16x32_bf16 a[208:211], a[156:159], v[172:175], a[208:211]\n"
"v_mfma_f32_16x16x32_bf16 a[204:207], a[148:151], v[172:175], a[204:207]\n"
"v_mfma_f32_16x16x32_bf16 a[204:207], a[144:147], v[168:171], a[204:207]\n"
"s_setpc_b64 s[62:63]\n"
".p2align 12\n"
".p2align 5\n"
"L_W1POLL:\n"
"s_nop 7\n"
"s_branch L_TABDONE\n"
"L_W2POLL:\n"
"s_nop 7\n"
"s_branch L_TABDONE\n"
"L_POLL:\n"
"s_mov_b32 s93, 4000\n"
"s_waitcnt vmcnt(4)\n"
"L_POLLC:\n"
"v_readfirstlane_b32 s92, v254\n"
"s_nop 0\n"
"s_cmp_lg_u32 s92, s88\n"
"s_cbranch_scc1 L_M\n"
"s_sleep 2\n"
"buffer_load_dword v254, off, s[56:59], 0 sc1\n"
"s_waitcnt vmcnt(0)\n"
"s_sub_u32 s93, s93, 1\n"
"s_cmp_lg_u32 s93, 0\n"
"s_cbranch_scc1 L_POLLC\n"
"s_mov_b64 s[86:87], -1\n"
"s_branch L_M\n"
"L_M:\n"
"v_mfma_f32_16x16x32_bf16 v[144:147], a[96:99], a[0:3], v[188:191]\n"
"s_mov_b64 s[56:57], s[28:29]\n"
"v_readlane_b32 s61, v235, s60\n"
"s_add_u32 s60, s60, 1\n"
"v_mfma_f32_16x16x32_bf16 v[144:147], a[100:103], a[4:7], v[144:147]\n"
"s_and_b32 s98, s61, 0x3ffff\n"
"s_add_u32 s62, s64, s98\n"
"s_addc_u32 s63, s65, 0\n"
"v_mbcnt_lo_u32_b32 v246, -1, 0\n"
"v_mfma_f32_16x16x32_bf16 v[144:147], a[104:107], a[8:11], v[144:147]\n"
"s_mov_b32 s58, 25600\n"
"v_mbcnt_hi_u32_b32 v246, -1, v246\n"
"v_lshlrev_b32 v255, 4, v246\n"
"v_bfe_u32 v244, v246, 3, 1\n"
"v_mfma_f32_16x16x32_bf16 v[144:147], a[108:111], a[12:15], v[144:147]\n"
"s_mov_b32 s92, 24576\n"
"v_mul_u32_u24 v244, 3072, v244\n"
"v_and_b32 v247, 7, v246\n"
"v_lshl_add_u32 v244, v247, 7, v244\n"
"v_mfma_f32_16x16x32_bf16 v[144:147], a[112:115], a[16:19], v[144:147]\n"
"s_add_u32 m0, s97, 0\n"
"v_lshrrev_b32 v254, 5, v246\n"
"v_xor_b32 v245, v254, v247\n"
"v_lshl_add_u32 v244, v245, 4, v244\n"
"v_mfma_f32_16x16x32_bf16 v[144:147], a[116:119], a[20:23], v[144:147]\n"
"s_mov_b32 s93, 0\n"
"v_bfe_u32 v245, v246, 4, 1\n"
"v_lshl_add_u32 v244, v245, 3, v244\n"
"v_add_u32 v240, s72, v244\n"
"v_mfma_f32_16x16x32_bf16 v[176:179], a[120:123], a[0:3], v[188:191]\n"
"s_add_u32 s77, s66, 12288\n"
"v_mov_b32 v244, 0x3f80\n"
"v_mov_b32 v245, 1.0\n"
"v_cndmask_b32 v244, v244, v245, s[82:83]\n"
"v_mfma_f32_16x16x32_bf16 v[176:179], a[124:127], a[4:7], v[176:179]\n"
"s_lshr_b32 s67, s61, 18\n"
"ds_read_b128 a[144:147], v228 offset:12288\n"
"v_cndmask_b32 v250, 0, v244, s[68:69]\n"
"v_cndmask_b32 v251, 0, v244, s[70:71]\n"
"v_mfma_f32_16x16x32_bf16 v[176:179], a[128:131], a[8:11], v[176:179]\n"
"buffer_load_dwordx4 v[244:247], v255, s[56:59], s92 offen sc1\n"
"ds_read_b128 a[148:151], v229 offset:12288\n"
"v_mfma_f32_16x16x32_bf16 v[176:179], a[132:135], a[12:15], v[176:179]\n"
"buffer_load_dwordx4 v234, s[40:43], s66 offen offset:0 lds\n"
"ds_read_b128 a[152:155], v228 offset:13312\n"
"v_mfma_f32_16x16x32_bf16 v[176:179], a[136:139], a[16:19], v[176:179]\n"
"buffer_load_dwordx4 v[204:207], v255, s[56:59], s93 offen offset:0 sc1\n"
"ds_read_b128 a[156:159], v229 offset:13312\n"
"v_mfma_f32_16x16x32_bf16 v[176:179], a[140:143], a[20:23], v[176:179]\n"
"ds_read_b128 a[160:163], v228 offset:14336\n"
"s_add_u32 m0, s97, 896\n"
"v_mfma_f32_16x16x32_bf16 v[152:155], a[96:99], a[24:27], v[192:195]\n"
"ds_write_b128 v238, a[232:235] offset:12288\n"
"v_mfma_f32_16x16x32_bf16 v[152:155], a[100:103], a[28:31], v[152:155]\n"
"v_exp_f32 v144, v144\n"
"v_mfma_f32_16x16x32_bf16 v[152:155], a[104:107], a[32:35], v[152:155]\n"
"buffer_load_dwordx4 v[208:211], v255, s[56:59], s93 offen offset:1024 sc1\n"
"v_mfma_f32_16x16x32_bf16 v[152:155], a[108:111], a[36:39], v[152:155]\n"
"buffer_load_dwordx4 v234, s[40:43], s66 offen offset:128 lds\n"
"v_mfma_f32_16x16x32_bf16 v[152:155], a[112:115], a[40:43], v[152:155]\n"
"s_add_u32 m0, s97, 1792\n"
"v_exp_f32 v145, v145\n"
"v_mfma_f32_16x16x32_bf16 v[152:155], a[116:119], a[44:47], v[152:155]\n"
"v_exp_f32 v146, v146\n"
"v_mfma_f32_16x16x32_bf16 v[180:183], a[120:123], a[24:27], v[192:195]\n"
"v_exp_f32 v147, v147\n"
"v_mfma_f32_16x16x32_bf16 v[180:183], a[124:127], a[28:31], v[180:183]\n"
"ds_write_b128 v238, a[236:239] offset:13312\n"
"v_mfma_f32_16x16x32_bf16 v[180:183], a[128:131], a[32:35], v[180:183]\n"
"buffer_load_dwordx4 v[212:215], v255, s[56:59], s93 offen offset:2048 sc1\n"
"v_mfma_f32_16x16x32_bf16 v[180:183], a[132:135], a[36:39], v[180:183]\n"
"s_mov_b32 s93, 3072\n"
"ds_read_b128 a[164:167], v229 offset:14336\n"
"v_cvt_pk_bf16_f32 v144, v144, v145\n"
"v_mfma_f32_16x16x32_bf16 v[180:183], a[136:139], a[40:43], v[180:183]\n"
"ds_write_b128 v238, a[240:243] offset:14336\n"
"v_mfma_f32_16x16x32_bf16 v[180:183], a[140:143], a[44:47], v[180:183]\n"
"v_exp_f32 v176, v176\n"
"v_mfma_f32_16x16x32_bf16 v[160:163], a[96:99], a[48:51], v[196:199]\n"
"buffer_load_dwordx4 v[216:219], v255, s[56:59], s93 offen offset:0 sc1\n"
"v_mfma_f32_16x16x32_bf16 v[160:163], a[100:103], a[52:55], v[160:163]\n"
"buffer_load_dwordx4 v234, s[40:43], s66 offen offset:256 lds\n"
"v_mfma_f32_16x16x32_bf16 v[160:163], a[104:107], a[56:59], v[160:163]\n"
"s_add_u32 m0, s97, 12288\n"
"v_exp_f32 v177, v177\n"
"v_mfma_f32_16x16x32_bf16 v[160:163], a[108:111], a[60:63], v[160:163]\n"
"v_exp_f32 v178, v178\n"
"v_mfma_f32_16x16x32_bf16 v[160:163], a[112:115], a[64:67], v[160:163]\n"
"v_exp_f32 v179, v179\n"
"v_mfma_f32_16x16x32_bf16 v[160:163], a[116:119], a[68:71], v[160:163]\n"
"ds_write_b128 v238, a[244:247] offset:15360\n"
"v_mfma_f32_16x16x32_bf16 v[184:187], a[120:123], a[48:51], v[196:199]\n"
"buffer_load_dwordx4 v[220:223], v255, s[56:59], s93 offen offset:1024 sc1\n"
"v_mfma_f32_16x16x32_bf16 v[184:187], a[124:127], a[52:55], v[184:187]\n"
"v_exp_f32 v152, v152\n"
"v_mfma_f32_16x16x32_bf16 v[184:187], a[128:131], a[56:59], v[184:187]\n"
"v_exp_f32 v153, v153\n"
"v_mfma_f32_16x16x32_bf16 v[184:187], a[132:135], a[60:63], v[184:187]\n"
"v_exp_f32 v154, v154\n"
"v_mfma_f32_16x16x32_bf16 v[184:187], a[136:139], a[64:67], v[184:187]\n"
"v_exp_f32 v155, v155\n"
"v_mfma_f32_16x16x32_bf16 v[184:187], a[140:143], a[68:71], v[184:187]\n"
"buffer_load_dwordx4 v[224:227], v255, s[56:59], s93 offen offset:2048 sc1\n"
"v_mfma_f32_16x16x32_bf16 v[168:171], a[96:99], a[72:75], v[200:203]\n"
"s_mov_b32 s93, 6144\n"
"ds_write_b128 v238, a[248:251] offset:16384\n"
"v_mfma_f32_16x16x32_bf16 v[168:171], a[100:103], a[76:79], v[168:171]\n"
"v_exp_f32 v180, v180\n"
"v_mfma_f32_16x16x32_bf16 v[168:171], a[104:107], a[80:83], v[168:171]\n"
"v_exp_f32 v181, v181\n"
"v_mfma_f32_16x16x32_bf16 v[168:171], a[108:111], a[84:87], v[168:171]\n"
"v_exp_f32 v182, v182\n"
"v_mfma_f32_16x16x32_bf16 v[168:171], a[112:115], a[88:91], v[168:171]\n"
"v_exp_f32 v183, v183\n"
"v_mfma_f32_16x16x32_bf16 v[168:171], a[116:119], a[92:95], v[168:171]\n"
"v_cvt_pk_bf16_f32 v145, v146, v147\n"
"v_cvt_pk_bf16_f32 v146, v176, v177\n"
"v_cvt_pk_bf16_f32 v147, v178, v179\n"
"v_mfma_f32_16x16x32_bf16 v[176:179], a[120:123], a[72:75], v[200:203]\n"
"ds_read_b128 a[96:99], v228 offset:18432\n"
"ds_read_b128 a[100:103], v229 offset:18432\n"
"v_cvt_pk_bf16_f32 v152, v152, v153\n"
"v_mfma_f32_16x16x32_bf16 v[176:179], a[124:127], a[76:79], v[176:179]\n"
"ds_read_b128 a[104:107], v228 offset:19456\n"
"v_cvt_pk_bf16_f32 v153, v154, v155\n"
"v_mfma_f32_16x16x32_bf16 v[176:179], a[128:131], a[80:83], v[176:179]\n"
"buffer_load_dwordx4 a[232:235], v255, s[56:59], s93 offen offset:0 sc1\n"
"v_mfma_f32_16x16x32_bf16 v[176:179], a[132:135], a[84:87], v[176:179]\n"
"ds_read_b128 a[108:111], v229 offset:19456\n"
"v_cvt_pk_bf16_f32 v154, v180, v181\n"
"v_mfma_f32_16x16x32_bf16 v[176:179], a[136:139], a[88:91], v[176:179]\n"
"s_waitcnt lgkmcnt(7)\n"
"ds_read_b128 a[112:115], v228 offset:20480\n"
"v_cvt_pk_bf16_f32 v155, v182, v183\n"
"v_mfma_f32_16x16x32_bf16 v[176:179], a[140:143], a[92:95], v[176:179]\n"
"ds_read_b128 a[116:119], v229 offset:20480\n"
"v_mov_b32 v248, 0\n"
"v_mfma_f32_16x16x32_bf16 v[148:151], a[144:147], a[0:3], v[188:191]\n"
"buffer_load_dwordx4 a[236:239], v255, s[56:59], s93 offen offset:1024 sc1\n"
"v_mfma_f32_16x16x32_bf16 v[148:151], a[148:151], a[4:7], v[148:151]\n"
"ds_write_b128 v238, a[252:255] offset:17408\n"
"v_mfma_f32_16x16x32_bf16 v[148:151], a[152:155], a[8:11], v[148:151]\n"
"v_exp_f32 v160, v160\n"
"v_mfma_f32_16x16x32_bf16 v[148:151], a[156:159], a[12:15], v[148:151]\n"
"v_exp_f32 v161, v161\n"
"v_mfma_f32_16x16x32_bf16 v[148:151], a[160:163], a[16:19], v[148:151]\n"
"v_exp_f32 v162, v162\n"
"v_mfma_f32_16x16x32_bf16 v[148:151], a[164:167], a[20:23], v[148:151]\n"
"s_waitcnt vmcnt(14) lgkmcnt(1)\n"
"s_barrier\n"
"v_cvt_pk_bf16_f32 v160, v160, v161\n"
"v_mfma_f32_16x16x32_bf16 v[180:183], a[96:99], a[0:3], v[188:191]\n"
"v_exp_f32 v163, v163\n"
"v_mfma_f32_16x16x32_bf16 v[180:183], a[100:103], a[4:7], v[180:183]\n"
"buffer_load_dwordx4 a[240:243], v255, s[56:59], s93 offen offset:2048 sc1\n"
"v_mfma_f32_16x16x32_bf16 v[180:183], a[104:107], a[8:11], v[180:183]\n"
"v_cvt_pk_bf16_f32 v161, v162, v163\n"
"s_mov_b32 s93, 9216\n"
"v_mfma_f32_16x16x32_bf16 v[180:183], a[108:111], a[12:15], v[180:183]\n"
"v_exp_f32 v184, v184\n"
"v_mfma_f32_16x16x32_bf16 v[180:183], a[112:115], a[16:19], v[180:183]\n"
"v_exp_f32 v185, v185\n"
"v_mfma_f32_16x16x32_bf16 v[180:183], a[116:119], a[20:23], v[180:183]\n"
"v_exp_f32 v186, v186\n"
"v_mfma_f32_16x16x32_bf16 v[156:159], a[144:147], a[24:27], v[192:195]\n"
"buffer_load_dwordx4 v234, s[40:43], s77 offen offset:0 lds\n"
"v_mfma_f32_16x16x32_bf16 v[156:159], a[148:151], a[28:31], v[156:159]\n"
"buffer_load_dwordx4 a[244:247], v255, s[56:59], s93 offen offset:0 sc1\n"
"v_mfma_f32_16x16x32_bf16 v[156:159], a[152:155], a[32:35], v[156:159]\n"
"s_add_u32 m0, s97, 13184\n"
"v_exp_f32 v187, v187\n"
"v_mfma_f32_16x16x32_bf16 v[156:159], a[156:159], a[36:39], v[156:159]\n"
"v_cvt_pk_bf16_f32 v162, v184, v185\n"
"v_cvt_pk_bf16_f32 v163, v186, v187\n"
"v_mfma_f32_16x16x32_bf16 v[156:159], a[160:163], a[40:43], v[156:159]\n"
"v_exp_f32 v168, v168\n"
"v_mfma_f32_16x16x32_bf16 v[156:159], a[164:167], a[44:47], v[156:159]\n"
"v_exp_f32 v169, v169\n"
"v_mfma_f32_16x16x32_bf16 v[184:187], a[96:99], a[24:27], v[192:195]\n"
"v_exp_f32 v170, v170\n"
"v_mfma_f32_16x16x32_bf16 v[184:187], a[100:103], a[28:31], v[184:187]\n"
"buffer_load_dwordx4 a[248:251], v255, s[56:59], s93 offen offset:1024 sc1\n"
"v_mfma_f32_16x16x32_bf16 v[184:187], a[104:107], a[32:35], v[184:187]\n"
"v_exp_f32 v171, v171\n"
"v_mfma_f32_16x16x32_bf16 v[184:187], a[108:111], a[36:39], v[184:187]\n"
"v_cvt_pk_bf16_f32 v168, v168, v169\n"
"v_cvt_pk_bf16_f32 v169, v170, v171\n"
"v_mfma_f32_16x16x32_bf16 v[184:187], a[112:115], a[40:43], v[184:187]\n"
"buffer_load_dwordx4 v234, s[40:43], s77 offen offset:128 lds\n"
"v_mfma_f32_16x16x32_bf16 v[184:187], a[116:119], a[44:47], v[184:187]\n"
"s_add_u32 m0, s97, 14080\n"
"v_exp_f32 v176, v176\n"
"v_mfma_f32_16x16x32_bf16 v[164:167], a[144:147], a[48:51], v[196:199]\n"
"v_exp_f32 v177, v177\n"
"v_mfma_f32_16x16x32_bf16 v[164:167], a[148:151], a[52:55], v[164:167]\n"
"buffer_load_dwordx4 a[252:255], v255, s[56:59], s93 offen offset:2048 sc1\n"
"v_mfma_f32_16x16x32_bf16 v[164:167], a[152:155], a[56:59], v[164:167]\n"
"s_mov_b32 s93, 18432\n"
"v_exp_f32 v178, v178\n"
"v_mfma_f32_16x16x32_bf16 v[164:167], a[156:159], a[60:63], v[164:167]\n"
"v_exp_f32 v179, v179\n"
"v_mfma_f32_16x16x32_bf16 v[164:167], a[160:163], a[64:67], v[164:167]\n"
"ds_read_b64_tr_b16 a[120:121], v230 offset:24576\n"
"v_cvt_pk_bf16_f32 v170, v176, v177\n"
"v_mfma_f32_16x16x32_bf16 v[164:167], a[164:167], a[68:71], v[164:167]\n"
"ds_read_b64_tr_b16 a[122:123], v230 offset:30720\n"
"v_cvt_pk_bf16_f32 v171, v178, v179\n"
"v_mfma_f32_16x16x32_bf16 v[176:179], a[96:99], a[48:51], v[196:199]\n"
"v_exp_f32 v148, v148\n"
"v_mfma_f32_16x16x32_bf16 v[176:179], a[100:103], a[52:55], v[176:179]\n"
"v_exp_f32 v149, v149\n"
"v_mfma_f32_16x16x32_bf16 v[176:179], a[104:107], a[56:59], v[176:179]\n"
"buffer_load_dwordx4 v234, s[40:43], s77 offen offset:256 lds\n"
"v_mfma_f32_16x16x32_bf16 v[176:179], a[108:111], a[60:63], v[176:179]\n"
"s_add_u32 m0, s97, 24576\n"
"v_exp_f32 v150, v150\n"
"v_mfma_f32_16x16x32_bf16 v[176:179], a[112:115], a[64:67], v[176:179]\n"
"v_exp_f32 v151, v151\n"
"v_mfma_f32_16x16x32_bf16 v[176:179], a[116:119], a[68:71], v[176:179]\n"
"ds_read_b64_tr_b16 a[124:125], v230 offset:36864\n"
"v_cvt_pk_bf16_f32 v148, v148, v149\n"
"v_mfma_f32_16x16x32_bf16 v[172:175], a[144:147], a[72:75], v[200:203]\n"
"ds_read_b64_tr_b16 a[126:127], v230 offset:43008\n"
"v_cvt_pk_bf16_f32 v149, v150, v151\n"
"v_mfma_f32_16x16x32_bf16 v[172:175], a[148:151], a[76:79], v[172:175]\n"
"v_exp_f32 v180, v180\n"
"v_mfma_f32_16x16x32_bf16 v[172:175], a[152:155], a[80:83], v[172:175]\n"
"v_exp_f32 v181, v181\n"
"v_mfma_f32_16x16x32_bf16 v[172:175], a[156:159], a[84:87], v[172:175]\n"
"v_exp_f32 v182, v182\n"
"v_mfma_f32_16x16x32_bf16 v[172:175], a[160:163], a[88:91], v[172:175]\n"
"v_exp_f32 v183, v183\n"
"v_mfma_f32_16x16x32_bf16 v[172:175], a[164:167], a[92:95], v[172:175]\n"
"v_cvt_pk_bf16_f32 v150, v180, v181\n"
"v_cvt_pk_bf16_f32 v151, v182, v183\n"
"v_mfma_f32_16x16x32_bf16 v[180:183], a[96:99], a[72:75], v[200:203]\n"
"ds_read_b64_tr_b16 a[128:129], v231 offset:24576\n"
"v_mov_b32 v249, 0\n"
"v_mfma_f32_16x16x32_bf16 v[180:183], a[100:103], a[76:79], v[180:183]\n"
"ds_read_b64_tr_b16 a[130:131], v231 offset:30720\n"
"v_mov_b32 v252, 0\n"
"v_mfma_f32_16x16x32_bf16 v[180:183], a[104:107], a[80:83], v[180:183]\n"
"ds_read_b64_tr_b16 a[132:133], v231 offset:36864\n"
"v_mov_b32 v253, 0\n"
"v_mfma_f32_16x16x32_bf16 v[180:183], a[108:111], a[84:87], v[180:183]\n"
"ds_read_b64_tr_b16 a[134:135], v231 offset:43008\n"
"v_xor_b32 v241, 32, v240\n"
"v_mfma_f32_16x16x32_bf16 v[180:183], a[112:115], a[88:91], v[180:183]\n"
"ds_read_b64_tr_b16 a[136:137], v232 offset:24576\n"
"s_waitcnt vmcnt(9)\n"
"v_mfma_f32_16x16x32_bf16 v[180:183], a[116:119], a[92:95], v[180:183]\n"
"ds_read_b64_tr_b16 a[138:139], v232 offset:30720\n"
"v_xor_b32 v242, 64, v240\n"
"v_mfma_f32_16x16x32_bf16 v[0:3], v[250:253], v[204:207], v[0:3]\n"
"ds_read_b64_tr_b16 a[140:141], v232 offset:36864\n"
"v_xor_b32 v243, 96, v240\n"
"v_mfma_f32_16x16x32_bf16 v[4:7], v[248:251], v[204:207], v[4:7]\n"
"v_exp_f32 v156, v156\n"
"v_mfma_f32_16x16x32_bf16 v[8:11], v[250:253], v[208:211], v[8:11]\n"
"v_exp_f32 v157, v157\n"
"v_mfma_f32_16x16x32_bf16 v[12:15], v[248:251], v[208:211], v[12:15]\n"
"v_exp_f32 v158, v158\n"
"v_mfma_f32_16x16x32_bf16 v[16:19], v[250:253], v[212:215], v[16:19]\n"
"v_exp_f32 v159, v159\n"
"v_mfma_f32_16x16x32_bf16 v[20:23], v[248:251], v[212:215], v[20:23]\n"
"ds_read_b64_tr_b16 a[142:143], v232 offset:43008\n"
"v_cvt_pk_bf16_f32 v156, v156, v157\n"
"v_mfma_f32_16x16x32_bf16 v[24:27], v[250:253], v[216:219], v[24:27]\n"
"ds_read_b64_tr_b16 a[144:145], v233 offset:24576\n"
"v_cvt_pk_bf16_f32 v157, v158, v159\n"
"v_mfma_f32_16x16x32_bf16 v[28:31], v[248:251], v[216:219], v[28:31]\n"
"v_exp_f32 v184, v184\n"
"v_mfma_f32_16x16x32_bf16 v[32:35], v[250:253], v[220:223], v[32:35]\n"
"v_exp_f32 v185, v185\n"
"v_mfma_f32_16x16x32_bf16 v[36:39], v[248:251], v[220:223], v[36:39]\n"
"v_exp_f32 v186, v186\n"
"v_mfma_f32_16x16x32_bf16 v[40:43], v[250:253], v[224:227], v[40:43]\n"
"v_exp_f32 v187, v187\n"
"v_mfma_f32_16x16x32_bf16 v[44:47], v[248:251], v[224:227], v[44:47]\n"
"s_waitcnt lgkmcnt(1)\n"
"v_cvt_pk_bf16_f32 v158, v184, v185\n"
"v_cvt_pk_bf16_f32 v159, v186, v187\n"
"v_mfma_f32_16x16x32_bf16 v[0:3], a[120:123], v[144:147], v[0:3]\n"
"v_exp_f32 v164, v164\n"
"v_mfma_f32_16x16x32_bf16 v[0:3], a[124:127], v[148:151], v[0:3]\n"
"v_exp_f32 v165, v165\n"
"v_mfma_f32_16x16x32_bf16 v[4:7], a[132:135], v[148:151], v[4:7]\n"
"v_exp_f32 v166, v166\n"
"v_mfma_f32_16x16x32_bf16 v[4:7], a[128:131], v[144:147], v[4:7]\n"
"v_exp_f32 v167, v167\n"
"v_mfma_f32_16x16x32_bf16 v[8:11], a[136:139], v[144:147], v[8:11]\n"
"buffer_load_dwordx4 v[204:207], v255, s[56:59], s93 offen offset:0 sc1\n"
"v_mfma_f32_16x16x32_bf16 v[8:11], a[140:143], v[148:151], v[8:11]\n"
"ds_read_b64_tr_b16 a[146:147], v233 offset:30720\n"
"v_cvt_pk_bf16_f32 v164, v164, v165\n"
"v_smfmac_f32_16x16x64_bf16 a[216:219], a[224:227], v[144:151], v237\n"
"ds_read_b64_tr_b16 a[148:149], v233 offset:36864\n"
"v_cvt_pk_bf16_f32 v165, v166, v167\n"
"v_mfma_f32_16x16x32_bf16 v[56:59], a[140:143], v[156:159], v[56:59]\n"
"v_exp_f32 v176, v176\n"
"v_mfma_f32_16x16x32_bf16 v[56:59], a[136:139], v[152:155], v[56:59]\n"
"buffer_load_dwordx4 v[208:211], v255, s[56:59], s93 offen offset:1024 sc1\n"
"v_mfma_f32_16x16x32_bf16 v[52:55], a[128:131], v[152:155], v[52:55]\n"
"v_exp_f32 v177, v177\n"
"v_mfma_f32_16x16x32_bf16 v[52:55], a[132:135], v[156:159], v[52:55]\n"
"v_exp_f32 v178, v178\n"
"v_mfma_f32_16x16x32_bf16 v[48:51], a[124:127], v[156:159], v[48:51]\n"
"v_exp_f32 v179, v179\n"
"v_mfma_f32_16x16x32_bf16 v[48:51], a[120:123], v[152:155], v[48:51]\n"
"ds_read_b64_tr_b16 a[150:151], v233 offset:43008\n"
"v_cvt_pk_bf16_f32 v166, v176, v177\n"
"v_smfmac_f32_16x16x64_bf16 a[216:219], a[228:231], v[152:159], v237\n"
"buffer_load_dwordx4 v[212:215], v255, s[56:59], s93 offen offset:2048 sc1\n"
"v_mfma_f32_16x16x32_bf16 v[96:99], a[120:123], v[160:163], v[96:99]\n"
"s_mov_b32 s93, 21504\n"
"ds_read_b64_tr_b16 a[152:153], v230 offset:25600\n"
"v_cvt_pk_bf16_f32 v167, v178, v179\n"
"s_nop 1\n"
"v_mfma_f32_16x16x32_bf16 v[96:99], a[124:127], v[164:167], v[96:99]\n"
"ds_read_b64_tr_b16 a[154:155], v230 offset:31744\n"
"v_mfma_f32_16x16x32_bf16 v[100:103], a[132:135], v[164:167], v[100:103]\n"
"ds_read_b64_tr_b16 a[156:157], v230 offset:37888\n"
"v_mfma_f32_16x16x32_bf16 v[100:103], a[128:131], v[160:163], v[100:103]\n"
"ds_read_b64_tr_b16 a[158:159], v230 offset:44032\n"
"v_mfma_f32_16x16x32_bf16 v[104:107], a[136:139], v[160:163], v[104:107]\n"
"buffer_load_dwordx4 v[216:219], v255, s[56:59], s93 offen offset:0 sc1\n"
"v_mfma_f32_16x16x32_bf16 v[104:107], a[140:143], v[164:167], v[104:107]\n"
"s_waitcnt vmcnt(5)\n"
"v_exp_f32 v172, v172\n"
"v_smfmac_f32_16x16x64_bf16 a[220:223], a[224:227], v[160:167], v237\n"
"v_exp_f32 v173, v173\n"
"v_mfma_f32_16x16x32_bf16 v[48:51], v[250:253], a[232:235], v[48:51]\n"
"v_exp_f32 v174, v174\n"
"v_mfma_f32_16x16x32_bf16 v[52:55], v[248:251], a[232:235], v[52:55]\n"
"v_exp_f32 v175, v175\n"
"v_mfma_f32_16x16x32_bf16 v[56:59], v[250:253], a[236:239], v[56:59]\n"
"buffer_load_dwordx4 v[220:223], v255, s[56:59], s93 offen offset:1024 sc1\n"
"v_mfma_f32_16x16x32_bf16 v[60:63], v[248:251], a[236:239], v[60:63]\n"
"ds_read_b64_tr_b16 a[160:161], v231 offset:25600\n"
"v_cvt_pk_bf16_f32 v172, v172, v173\n"
"v_mfma_f32_16x16x32_bf16 v[64:67], v[250:253], a[240:243], v[64:67]\n"
"ds_read_b64_tr_b16 a[162:163], v231 offset:31744\n"
"v_cvt_pk_bf16_f32 v173, v174, v175\n"
"v_mfma_f32_16x16x32_bf16 v[68:71], v[248:251], a[240:243], v[68:71]\n"
"v_exp_f32 v180, v180\n"
"v_mfma_f32_16x16x32_bf16 v[72:75], v[250:253], a[244:247], v[72:75]\n"
"v_exp_f32 v181, v181\n"
"v_mfma_f32_16x16x32_bf16 v[76:79], v[248:251], a[244:247], v[76:79]\n"
"buffer_load_dwordx4 v[224:227], v255, s[56:59], s93 offen offset:2048 sc1\n"
"v_mfma_f32_16x16x32_bf16 v[80:83], v[250:253], a[248:251], v[80:83]\n"
"s_mov_b32 s93, 12288\n"
"v_exp_f32 v182, v182\n"
"v_mfma_f32_16x16x32_bf16 v[84:87], v[248:251], a[248:251], v[84:87]\n"
"v_exp_f32 v183, v183\n"
"v_mfma_f32_16x16x32_bf16 v[88:91], v[250:253], a[252:255], v[88:91]\n"
"ds_read_b64_tr_b16 a[164:165], v231 offset:37888\n"
"v_cvt_pk_bf16_f32 v174, v180, v181\n"
"v_mfma_f32_16x16x32_bf16 v[92:95], v[248:251], a[252:255], v[92:95]\n"
"ds_read_b64_tr_b16 a[166:167], v231 offset:44032\n"
"v_cvt_pk_bf16_f32 v175, v182, v183\n"
"s_nop 1\n"
"v_mfma_f32_16x16x32_bf16 a[176:179], a[140:143], v[172:175], a[176:179]\n"
"ds_read_b64_tr_b16 a[96:97], v232 offset:25600\n"
"v_mfma_f32_16x16x32_bf16 a[176:179], a[136:139], v[168:171], a[176:179]\n"
"buffer_load_dwordx4 a[232:235], v255, s[56:59], s93 offen offset:0 sc1\n"
"v_mfma_f32_16x16x32_bf16 a[172:175], a[128:131], v[168:171], a[172:175]\n"
"ds_read_b64_tr_b16 a[98:99], v232 offset:31744\n"
"v_mfma_f32_16x16x32_bf16 a[172:175], a[132:135], v[172:175], a[172:175]\n"
"ds_read_b64_tr_b16 a[100:101], v232 offset:37888\n"
"v_mfma_f32_16x16x32_bf16 a[168:171], a[124:127], v[172:175], a[168:171]\n"
"ds_read_b64_tr_b16 a[102:103], v232 offset:44032\n"
"v_mfma_f32_16x16x32_bf16 a[168:171], a[120:123], v[168:171], a[168:171]\n"
"buffer_load_dwordx4 a[236:239], v255, s[56:59], s93 offen offset:1024 sc1\n"
"v_smfmac_f32_16x16x64_bf16 a[220:223], a[228:231], v[168:175], v237\n"
"s_waitcnt lgkmcnt(4)\n"
"ds_read_b64_tr_b16 a[104:105], v233 offset:25600\n"
"v_mfma_f32_16x16x32_bf16 v[12:15], a[144:147], v[144:147], v[12:15]\n"
"ds_read_b64_tr_b16 a[106:107], v233 offset:31744\n"
"v_mfma_f32_16x16x32_bf16 v[12:15], a[148:151], v[148:151], v[12:15]\n"
"ds_read_b64_tr_b16 a[108:109], v233 offset:37888\n"
"v_mfma_f32_16x16x32_bf16 v[16:19], a[156:159], v[148:151], v[16:19]\n"
"buffer_load_dwordx4 a[240:243], v255, s[56:59], s93 offen offset:2048 sc1\n"
"v_mfma_f32_16x16x32_bf16 v[16:19], a[152:155], v[144:147], v[16:19]\n"
"ds_read_b64_tr_b16 a[110:111], v233 offset:44032\n"
"s_mov_b32 s93, 15360\n"
"v_mfma_f32_16x16x32_bf16 v[20:23], a[160:163], v[144:147], v[20:23]\n"
"buffer_load_dwordx4 a[244:247], v255, s[56:59], s93 offen offset:0 sc1\n"
"v_mfma_f32_16x16x32_bf16 v[20:23], a[164:167], v[148:151], v[20:23]\n"
"v_mfma_f32_16x16x32_bf16 v[68:71], a[164:167], v[156:159], v[68:71]\n"
"v_mfma_f32_16x16x32_bf16 v[68:71], a[160:163], v[152:155], v[68:71]\n"
"buffer_load_dwordx4 a[248:251], v255, s[56:59], s93 offen offset:1024 sc1\n"
"v_mfma_f32_16x16x32_bf16 v[64:67], a[152:155], v[152:155], v[64:67]\n"
"v_mfma_f32_16x16x32_bf16 v[64:67], a[156:159], v[156:159], v[64:67]\n"
"v_mfma_f32_16x16x32_bf16 v[60:63], a[148:151], v[156:159], v[60:63]\n"
"v_mfma_f32_16x16x32_bf16 v[60:63], a[144:147], v[152:155], v[60:63]\n"
"buffer_load_dwordx4 a[252:255], v255, s[56:59], s93 offen offset:2048 sc1\n"
"v_mfma_f32_16x16x32_bf16 v[108:111], a[144:147], v[160:163], v[108:111]\n"
"v_mfma_f32_16x16x32_bf16 v[108:111], a[148:151], v[164:167], v[108:111]\n"
"v_mfma_f32_16x16x32_bf16 v[112:115], a[156:159], v[164:167], v[112:115]\n"
"v_mfma_f32_16x16x32_bf16 v[112:115], a[152:155], v[160:163], v[112:115]\n"
"v_mfma_f32_16x16x32_bf16 v[116:119], a[160:163], v[160:163], v[116:119]\n"
"s_waitcnt vmcnt(23) lgkmcnt(0)\n"
"s_barrier\n"
"ds_read_b64_tr_b16 a[112:113], v230 offset:26624\n"
"v_mfma_f32_16x16x32_bf16 v[116:119], a[164:167], v[164:167], v[116:119]\n"
"buffer_load_dwordx4 v234, s[44:47], s66 offen offset:0 lds\n"
"v_mfma_f32_16x16x32_bf16 a[188:191], a[164:167], v[172:175], a[188:191]\n"
"ds_read_b64_tr_b16 a[114:115], v230 offset:32768\n"
"s_add_u32 m0, s97, 36864\n"
"v_mfma_f32_16x16x32_bf16 a[188:191], a[160:163], v[168:171], a[188:191]\n"
"ds_read_b64_tr_b16 a[116:117], v230 offset:38912\n"
"v_mfma_f32_16x16x32_bf16 a[184:187], a[152:155], v[168:171], a[184:187]\n"
"ds_read_b64_tr_b16 a[118:119], v230 offset:45056\n"
"v_mfma_f32_16x16x32_bf16 a[184:187], a[156:159], v[172:175], a[184:187]\n"
"buffer_load_dwordx4 v234, s[44:47], s77 offen offset:0 lds\n"
"v_mfma_f32_16x16x32_bf16 a[180:183], a[148:151], v[172:175], a[180:183]\n"
"ds_read_b64_tr_b16 a[164:165], v233 offset:38912\n"
"s_add_u32 m0, s97, 25472\n"
"v_mfma_f32_16x16x32_bf16 a[180:183], a[144:147], v[168:171], a[180:183]\n"
"ds_read_b64_tr_b16 a[166:167], v233 offset:45056\n"
"v_mfma_f32_16x16x32_bf16 v[24:27], a[96:99], v[144:147], v[24:27]\n"
"ds_read_b64_tr_b16 a[160:161], v233 offset:26624\n"
"v_mfma_f32_16x16x32_bf16 v[24:27], a[100:103], v[148:151], v[24:27]\n"
"ds_read_b64_tr_b16 a[162:163], v233 offset:32768\n"
"v_mfma_f32_16x16x32_bf16 v[28:31], a[108:111], v[148:151], v[28:31]\n"
"ds_read_b64_tr_b16 a[152:153], v232 offset:26624\n"
"v_mfma_f32_16x16x32_bf16 v[28:31], a[104:107], v[144:147], v[28:31]\n"
"s_waitcnt lgkmcnt(5)\n"
"ds_read_b64_tr_b16 a[154:155], v232 offset:32768\n"
"v_mfma_f32_16x16x32_bf16 v[32:35], a[112:115], v[144:147], v[32:35]\n"
"ds_read_b64_tr_b16 a[156:157], v232 offset:38912\n"
"v_mfma_f32_16x16x32_bf16 v[32:35], a[116:119], v[148:151], v[32:35]\n"
"ds_read_b64_tr_b16 a[158:159], v232 offset:45056\n"
"v_mfma_f32_16x16x32_bf16 v[80:83], a[116:119], v[156:159], v[80:83]\n"
"buffer_load_dwordx4 v234, s[44:47], s66 offen offset:128 lds\n"
"v_mfma_f32_16x16x32_bf16 v[80:83], a[112:115], v[152:155], v[80:83]\n"
"ds_read_b64_tr_b16 a[148:149], v231 offset:38912\n"
"s_add_u32 m0, s97, 37760\n"
"v_mfma_f32_16x16x32_bf16 v[76:79], a[104:107], v[152:155], v[76:79]\n"
"ds_read_b64_tr_b16 a[150:151], v231 offset:45056\n"
"v_mfma_f32_16x16x32_bf16 v[76:79], a[108:111], v[156:159], v[76:79]\n"
"ds_read_b64_tr_b16 a[144:145], v231 offset:26624\n"
"v_mfma_f32_16x16x32_bf16 v[72:75], a[100:103], v[156:159], v[72:75]\n"
"ds_read_b64_tr_b16 a[146:147], v231 offset:32768\n"
"v_mfma_f32_16x16x32_bf16 v[72:75], a[96:99], v[152:155], v[72:75]\n"
"ds_read_b128 a[120:123], v228 offset:6144\n"
"v_mfma_f32_16x16x32_bf16 v[120:123], a[96:99], v[160:163], v[120:123]\n"
"ds_read_b128 a[124:127], v229 offset:6144\n"
"v_mfma_f32_16x16x32_bf16 v[120:123], a[100:103], v[164:167], v[120:123]\n"
"ds_read_b128 a[128:131], v228 offset:7168\n"
"v_mfma_f32_16x16x32_bf16 v[124:127], a[108:111], v[164:167], v[124:127]\n"
"ds_read_b128 a[132:135], v229 offset:7168\n"
"v_mfma_f32_16x16x32_bf16 v[124:127], a[104:107], v[160:163], v[124:127]\n"
"ds_read_b128 a[136:139], v228 offset:8192\n"
"v_mfma_f32_16x16x32_bf16 v[128:131], a[112:115], v[160:163], v[128:131]\n"
"ds_read_b128 a[140:143], v229 offset:8192\n"
"v_mfma_f32_16x16x32_bf16 v[128:131], a[116:119], v[164:167], v[128:131]\n"
"v_mfma_f32_16x16x32_bf16 a[200:203], a[116:119], v[172:175], a[200:203]\n"
"ds_read_b128 a[116:119], v229 offset:2048\n"
"v_mfma_f32_16x16x32_bf16 a[200:203], a[112:115], v[168:171], a[200:203]\n"
"buffer_load_dwordx4 v234, s[44:47], s77 offen offset:128 lds\n"
"v_mfma_f32_16x16x32_bf16 a[196:199], a[104:107], v[168:171], a[196:199]\n"
"ds_read_b128 a[112:115], v228 offset:2048\n"
"s_add_u32 m0, s97, 26368\n"
"v_mfma_f32_16x16x32_bf16 a[196:199], a[108:111], v[172:175], a[196:199]\n"
"ds_read_b128 a[104:107], v228 offset:1024\n"
"v_mfma_f32_16x16x32_bf16 a[192:195], a[100:103], v[172:175], a[192:195]\n"
"s_waitcnt lgkmcnt(9)\n"
"ds_read_b128 a[108:111], v229 offset:1024\n"
"v_mfma_f32_16x16x32_bf16 a[192:195], a[96:99], v[168:171], a[192:195]\n"
"ds_read_b128 a[100:103], v229 offset:0\n"
"v_mfma_f32_16x16x32_bf16 v[36:39], a[144:147], v[144:147], v[36:39]\n"
"ds_read_b128 a[96:99], v228 offset:0\n"
"v_mfma_f32_16x16x32_bf16 v[36:39], a[148:151], v[148:151], v[36:39]\n"
"v_mfma_f32_16x16x32_bf16 v[40:43], a[156:159], v[148:151], v[40:43]\n"
"v_mfma_f32_16x16x32_bf16 v[40:43], a[152:155], v[144:147], v[40:43]\n"
"v_mfma_f32_16x16x32_bf16 v[44:47], a[160:163], v[144:147], v[44:47]\n"
"v_mfma_f32_16x16x32_bf16 v[44:47], a[164:167], v[148:151], v[44:47]\n"
"v_mfma_f32_16x16x32_bf16 v[92:95], a[164:167], v[156:159], v[92:95]\n"
"v_mfma_f32_16x16x32_bf16 v[92:95], a[160:163], v[152:155], v[92:95]\n"
"v_mfma_f32_16x16x32_bf16 v[88:91], a[152:155], v[152:155], v[88:91]\n"
"v_mfma_f32_16x16x32_bf16 v[88:91], a[156:159], v[156:159], v[88:91]\n"
"v_mfma_f32_16x16x32_bf16 v[84:87], a[148:151], v[156:159], v[84:87]\n"
"s_waitcnt vmcnt(16) lgkmcnt(0)\n"
"s_barrier\n"
"v_mfma_f32_16x16x32_bf16 v[84:87], a[144:147], v[152:155], v[84:87]\n"
"v_mfma_f32_16x16x32_bf16 v[132:135], a[144:147], v[160:163], v[132:135]\n"
"v_mfma_f32_16x16x32_bf16 v[132:135], a[148:151], v[164:167], v[132:135]\n"
"v_mfma_f32_16x16x32_bf16 v[136:139], a[156:159], v[164:167], v[136:139]\n"
"v_mfma_f32_16x16x32_bf16 v[136:139], a[152:155], v[160:163], v[136:139]\n"
"buffer_load_dwordx4 v234, s[44:47], s66 offen offset:256 lds\n"
"v_mfma_f32_16x16x32_bf16 v[140:143], a[160:163], v[160:163], v[140:143]\n"
"s_waitcnt vmcnt(5)\n"
"s_add_u32 m0, s97, 38656\n"
"s_mul_i32 s66, s67, 24576\n"
"v_mfma_f32_16x16x32_bf16 v[140:143], a[164:167], v[164:167], v[140:143]\n"
"v_mfma_f32_16x16x32_bf16 v[96:99], v[250:253], a[232:235], v[96:99]\n"
"v_mfma_f32_16x16x32_bf16 v[100:103], v[248:251], a[232:235], v[100:103]\n"
"ds_read_b128 a[232:235], v238 offset:12288\n"
"v_mfma_f32_16x16x32_bf16 v[104:107], v[250:253], a[236:239], v[104:107]\n"
"v_mfma_f32_16x16x32_bf16 v[108:111], v[248:251], a[236:239], v[108:111]\n"
"ds_read_b128 a[236:239], v238 offset:13312\n"
"v_mfma_f32_16x16x32_bf16 v[112:115], v[250:253], a[240:243], v[112:115]\n"
"v_mfma_f32_16x16x32_bf16 v[116:119], v[248:251], a[240:243], v[116:119]\n"
"ds_read_b128 a[240:243], v238 offset:14336\n"
"v_mfma_f32_16x16x32_bf16 v[120:123], v[250:253], a[244:247], v[120:123]\n"
"buffer_load_dwordx4 v234, s[44:47], s77 offen offset:256 lds\n"
"v_mfma_f32_16x16x32_bf16 v[124:127], v[248:251], a[244:247], v[124:127]\n"
"ds_read_b128 a[244:247], v238 offset:15360\n"
"v_mfma_f32_16x16x32_bf16 v[128:131], v[250:253], a[248:251], v[128:131]\n"
"v_mfma_f32_16x16x32_bf16 v[132:135], v[248:251], a[248:251], v[132:135]\n"
"ds_read_b128 a[248:251], v238 offset:16384\n"
"v_mfma_f32_16x16x32_bf16 v[136:139], v[250:253], a[252:255], v[136:139]\n"
"v_mfma_f32_16x16x32_bf16 v[140:143], v[248:251], a[252:255], v[140:143]\n"
"ds_read_b128 a[252:255], v238 offset:17408\n"
"v_mfma_f32_16x16x32_bf16 a[212:215], a[164:167], v[172:175], a[212:215]\n"
"v_mfma_f32_16x16x32_bf16 a[212:215], a[160:163], v[168:171], a[212:215]\n"
"v_mfma_f32_16x16x32_bf16 a[208:211], a[152:155], v[168:171], a[208:211]\n"
"v_mfma_f32_16x16x32_bf16 a[208:211], a[156:159], v[172:175], a[208:211]\n"
"v_mfma_f32_16x16x32_bf16 a[204:207], a[148:151], v[172:175], a[204:207]\n"
"s_waitcnt vmcnt(12)\n"
"v_mfma_f32_16x16x32_bf16 a[204:207], a[144:147], v[168:171], a[204:207]\n"
"v_mfma_f32_16x16x32_bf16 a[168:171], v[250:253], v[204:207], a[168:171]\n"
"v_mfma_f32_16x16x32_bf16 a[172:175], v[248:251], v[204:207], a[172:175]\n"
"v_mfma_f32_16x16x32_bf16 a[176:179], v[250:253], v[208:211], a[176:179]\n"
"v_mfma_f32_16x16x32_bf16 a[180:183], v[248:251], v[208:211], a[180:183]\n"
"v_mfma_f32_16x16x32_bf16 a[184:187], v[250:253], v[212:215], a[184:187]\n"
"v_mfma_f32_16x16x32_bf16 a[188:191], v[248:251], v[212:215], a[188:191]\n"
"v_mfma_f32_16x16x32_bf16 a[192:195], v[250:253], v[216:219], a[192:195]\n"
"v_mfma_f32_16x16x32_bf16 a[196:199], v[248:251], v[216:219], a[196:199]\n"
"v_mfma_f32_16x16x32_bf16 a[200:203], v[250:253], v[220:223], a[200:203]\n"
"v_mfma_f32_16x16x32_bf16 a[204:207], v[248:251], v[220:223], a[204:207]\n"
"v_mfma_f32_16x16x32_bf16 a[208:211], v[250:253], v[224:227], a[208:211]\n"
"s_waitcnt lgkmcnt(0)\n"
"v_mfma_f32_16x16x32_bf16 a[212:215], v[248:251], v[224:227], a[212:215]\n"
"s_setpc_b64 s[62:63]\n"
".p2align 12\n"
".p2align 5\n"
"L_W1P2:\n"
"s_nop 7\n"
"s_branch L_W1FLW\n"
"L_P2:\n"
"v_readlane_b32 s61, v235, s60\n"
"s_add_u32 s60, s60, 1\n"
"s_and_b32 s98, s61, 0x3ffff\n"
"s_add_u32 s62, s64, s98\n"
"s_addc_u32 s63, s65, 0\n"
"s_add_u32 m0, s97, 0\n"
"v_mfma_f32_16x16x32_bf16 v[168:171], a[96:99], a[72:75], v[200:203]\n"
"s_add_u32 s77, s66, 12288\n"
"buffer_load_dwordx4 v234, s[40:43], s66 offen offset:0 lds\n"
"v_mfma_f32_16x16x32_bf16 v[168:171], a[100:103], a[76:79], v[168:171]\n"
"s_add_u32 m0, s97, 896\n"
"ds_read_b128 a[144:147], v228 offset:12288\n"
"v_add_f32 v160, 0x71000000, v236\n"
"v_mfma_f32_16x16x32_bf16 v[168:171], a[104:107], a[80:83], v[168:171]\n"
"ds_read_b128 a[148:151], v229 offset:12288\n"
"v_add_f32 v161, 0xf1000000, v236\n"
"v_mfma_f32_16x16x32_bf16 v[168:171], a[108:111], a[84:87], v[168:171]\n"
"ds_read_b128 a[152:155], v228 offset:13312\n"
"v_add_f32 v162, 0xf1c00000, v236\n"
"v_mfma_f32_16x16x32_bf16 v[168:171], a[112:115], a[88:91], v[168:171]\n"
"ds_read_b128 a[156:159], v229 offset:13312\n"
"v_add_f32 v163, 0xf2200000, v236\n"
"v_mfma_f32_16x16x32_bf16 v[168:171], a[116:119], a[92:95], v[168:171]\n"
"buffer_load_dwordx4 v234, s[40:43], s66 offen offset:128 lds\n"
"v_mfma_f32_16x16x32_bf16 v[176:179], a[120:123], a[72:75], v[200:203]\n"
"s_add_u32 m0, s97, 1792\n"
"ds_read_b128 a[160:163], v228 offset:14336\n"
"v_min_f32 v160, v160, v196\n"
"v_mfma_f32_16x16x32_bf16 v[176:179], a[124:127], a[76:79], v[176:179]\n"
"ds_read_b128 a[164:167], v229 offset:14336\n"
"v_min_f32 v161, v161, v197\n"
"v_mfma_f32_16x16x32_bf16 v[176:179], a[128:131], a[80:83], v[176:179]\n"
"s_lshr_b32 s67, s61, 18\n"
"v_min_f32 v162, v162, v198\n"
"v_min_f32 v163, v163, v199\n"
"v_mfma_f32_16x16x32_bf16 v[176:179], a[132:135], a[84:87], v[176:179]\n"
"v_add_f32 v180, 0xf3780000, v236\n"
"v_add_f32 v181, 0xf3840000, v236\n"
"v_mfma_f32_16x16x32_bf16 v[176:179], a[136:139], a[88:91], v[176:179]\n"
"buffer_load_dwordx4 v234, s[40:43], s66 offen offset:256 lds\n"
"v_mfma_f32_16x16x32_bf16 v[176:179], a[140:143], a[92:95], v[176:179]\n"
"s_add_u32 m0, s97, 12288\n"
"v_add_f32 v182, 0xf38c0000, v236\n"
"v_add_f32 v183, 0xf3940000, v236\n"
"v_mfma_f32_16x16x32_bf16 v[160:163], a[96:99], a[48:51], v[160:163]\n"
"ds_read_b128 a[96:99], v228 offset:18432\n"
"v_min_f32 v180, v180, v196\n"
"v_mfma_f32_16x16x32_bf16 v[160:163], a[100:103], a[52:55], v[160:163]\n"
"ds_read_b128 a[100:103], v229 offset:18432\n"
"v_min_f32 v181, v181, v197\n"
"v_mfma_f32_16x16x32_bf16 v[160:163], a[104:107], a[56:59], v[160:163]\n"
"ds_read_b128 a[104:107], v228 offset:19456\n"
"v_min_f32 v182, v182, v198\n"
"v_mfma_f32_16x16x32_bf16 v[160:163], a[108:111], a[60:63], v[160:163]\n"
"ds_read_b128 a[108:111], v229 offset:19456\n"
"v_min_f32 v183, v183, v199\n"
"v_mfma_f32_16x16x32_bf16 v[160:163], a[112:115], a[64:67], v[160:163]\n"
"ds_read_b128 a[112:115], v228 offset:20480\n"
"v_add_f32 v164, 0xf3fc0000, v236\n"
"v_mfma_f32_16x16x32_bf16 v[160:163], a[116:119], a[68:71], v[160:163]\n"
"ds_read_b128 a[116:119], v229 offset:20480\n"
"v_add_f32 v165, 0xf4020000, v236\n"
"v_mfma_f32_16x16x32_bf16 v[180:183], a[120:123], a[48:51], v[180:183]\n"
"v_add_f32 v166, 0xf4060000, v236\n"
"v_add_f32 v167, 0xf40a0000, v236\n"
"v_mfma_f32_16x16x32_bf16 v[180:183], a[124:127], a[52:55], v[180:183]\n"
"v_min_f32 v164, v164, v196\n"
"v_min_f32 v165, v165, v197\n"
"v_mfma_f32_16x16x32_bf16 v[180:183], a[128:131], a[56:59], v[180:183]\n"
"v_min_f32 v166, v166, v198\n"
"v_min_f32 v167, v167, v199\n"
"v_mfma_f32_16x16x32_bf16 v[180:183], a[132:135], a[60:63], v[180:183]\n"
"v_exp_f32 v168, v168\n"
"v_mfma_f32_16x16x32_bf16 v[180:183], a[136:139], a[64:67], v[180:183]\n"
"s_waitcnt lgkmcnt(6)\n"
"v_exp_f32 v169, v169\n"
"v_mfma_f32_16x16x32_bf16 v[180:183], a[140:143], a[68:71], v[180:183]\n"
"v_exp_f32 v170, v170\n"
"v_mfma_f32_16x16x32_bf16 v[172:175], a[144:147], a[72:75], v[200:203]\n"
"v_exp_f32 v171, v171\n"
"v_mfma_f32_16x16x32_bf16 v[172:175], a[148:151], a[76:79], v[172:175]\n"
"v_cvt_pk_bf16_f32 v168, v168, v169\n"
"v_cvt_pk_bf16_f32 v169, v170, v171\n"
"v_mfma_f32_16x16x32_bf16 v[172:175], a[152:155], a[80:83], v[172:175]\n"
"v_exp_f32 v176, v176\n"
"v_mfma_f32_16x16x32_bf16 v[172:175], a[156:159], a[84:87], v[172:175]\n"
"v_exp_f32 v177, v177\n"
"v_mfma_f32_16x16x32_bf16 v[172:175], a[160:163], a[88:91], v[172:175]\n"
"s_waitcnt vmcnt(5) lgkmcnt(0)\n"
"s_barrier\n"
"v_cvt_pk_bf16_f32 v170, v176, v177\n"
"v_mfma_f32_16x16x32_bf16 v[172:175], a[164:167], a[92:95], v[172:175]\n"
"buffer_load_dwordx4 v234, s[40:43], s77 offen offset:0 lds\n"
"v_mfma_f32_16x16x32_bf16 v[184:187], a[96:99], a[72:75], v[200:203]\n"
"s_add_u32 m0, s97, 13184\n"
"ds_read_b64_tr_b16 a[120:121], v230 offset:24576\n"
"v_add_f32 v176, 0xf43e0000, v236\n"
"v_mfma_f32_16x16x32_bf16 v[184:187], a[100:103], a[76:79], v[184:187]\n"
"ds_read_b64_tr_b16 a[122:123], v230 offset:30720\n"
"v_add_f32 v177, 0xf4420000, v236\n"
"v_mfma_f32_16x16x32_bf16 v[184:187], a[104:107], a[80:83], v[184:187]\n"
"ds_read_b64_tr_b16 a[124:125], v230 offset:36864\n"
"v_min_f32 v176, v176, v196\n"
"v_mfma_f32_16x16x32_bf16 v[184:187], a[108:111], a[84:87], v[184:187]\n"
"ds_read_b64_tr_b16 a[126:127], v230 offset:43008\n"
"v_min_f32 v177, v177, v197\n"
"v_mfma_f32_16x16x32_bf16 v[184:187], a[112:115], a[88:91], v[184:187]\n"
"buffer_load_dwordx4 v234, s[40:43], s77 offen offset:128 lds\n"
"v_mfma_f32_16x16x32_bf16 v[184:187], a[116:119], a[92:95], v[184:187]\n"
"s_add_u32 m0, s97, 14080\n"
"ds_read_b64_tr_b16 a[128:129], v231 offset:24576\n"
"v_mfma_f32_16x16x32_bf16 v[164:167], a[144:147], a[48:51], v[164:167]\n"
"ds_read_b64_tr_b16 a[130:131], v231 offset:30720\n"
"v_mfma_f32_16x16x32_bf16 v[164:167], a[148:151], a[52:55], v[164:167]\n"
"ds_read_b64_tr_b16 a[132:133], v231 offset:36864\n"
"v_mfma_f32_16x16x32_bf16 v[164:167], a[152:155], a[56:59], v[164:167]\n"
"ds_read_b64_tr_b16 a[134:135], v231 offset:43008\n"
"buffer_load_dwordx4 v234, s[40:43], s77 offen offset:256 lds\n"
"v_mfma_f32_16x16x32_bf16 v[164:167], a[156:159], a[60:63], v[164:167]\n"
"s_add_u32 m0, s97, 24576\n"
"ds_read_b64_tr_b16 a[136:137], v232 offset:24576\n"
"v_mfma_f32_16x16x32_bf16 v[164:167], a[160:163], a[64:67], v[164:167]\n"
"ds_read_b64_tr_b16 a[138:139], v232 offset:30720\n"
"v_exp_f32 v178, v178\n"
"v_exp_f32 v179, v179\n"
"v_mfma_f32_16x16x32_bf16 v[164:167], a[164:167], a[68:71], v[164:167]\n"
"ds_read_b64_tr_b16 a[140:141], v232 offset:36864\n"
"ds_read_b64_tr_b16 a[142:143], v232 offset:43008\n"
"ds_read_b64_tr_b16 a[144:145], v233 offset:24576\n"
"ds_read_b64_tr_b16 a[146:147], v233 offset:30720\n"
"ds_read_b64_tr_b16 a[148:149], v233 offset:36864\n"
"ds_read_b64_tr_b16 a[150:151], v233 offset:43008\n"
"ds_read_b64_tr_b16 a[152:153], v230 offset:25600\n"
"ds_read_b64_tr_b16 a[154:155], v230 offset:31744\n"
"ds_read_b64_tr_b16 a[156:157], v230 offset:37888\n"
"ds_read_b64_tr_b16 a[158:159], v230 offset:44032\n"
"ds_read_b64_tr_b16 a[160:161], v231 offset:25600\n"
"ds_read_b64_tr_b16 a[162:163], v231 offset:31744\n"
"ds_read_b64_tr_b16 a[164:165], v231 offset:37888\n"
"ds_read_b64_tr_b16 a[166:167], v231 offset:44032\n"
"v_cvt_pk_bf16_f32 v171, v178, v179\n"
"v_add_f32 v178, 0xf4460000, v236\n"
"v_add_f32 v179, 0xf44a0000, v236\n"
"v_min_f32 v178, v178, v198\n"
"v_min_f32 v179, v179, v199\n"
"v_exp_f32 v172, v172\n"
"v_exp_f32 v173, v173\n"
"v_mfma_f32_16x16x32_bf16 v[176:179], a[96:99], a[48:51], v[176:179]\n"
"ds_read_b64_tr_b16 a[96:97], v232 offset:25600\n"
"v_cvt_pk_bf16_f32 v172, v172, v173\n"
"v_mfma_f32_16x16x32_bf16 v[176:179], a[100:103], a[52:55], v[176:179]\n"
"ds_read_b64_tr_b16 a[98:99], v232 offset:31744\n"
"v_mfma_f32_16x16x32_bf16 v[176:179], a[104:107], a[56:59], v[176:179]\n"
"ds_read_b64_tr_b16 a[100:101], v232 offset:37888\n"
"v_mfma_f32_16x16x32_bf16 v[176:179], a[108:111], a[60:63], v[176:179]\n"
"ds_read_b64_tr_b16 a[102:103], v232 offset:44032\n"
"ds_read_b64_tr_b16 a[104:105], v233 offset:25600\n"
"ds_read_b64_tr_b16 a[106:107], v233 offset:31744\n"
"ds_read_b64_tr_b16 a[108:109], v233 offset:37888\n"
"ds_read_b64_tr_b16 a[110:111], v233 offset:44032\n"
"v_mfma_f32_16x16x32_bf16 v[176:179], a[112:115], a[64:67], v[176:179]\n"
"s_waitcnt lgkmcnt(15)\n"
"v_exp_f32 v174, v174\n"
"v_mfma_f32_16x16x32_bf16 v[176:179], a[116:119], a[68:71], v[176:179]\n"
"v_exp_f32 v175, v175\n"
"v_mfma_f32_16x16x32_bf16 a[168:171], a[120:123], v[168:171], a[168:171]\n"
"v_exp_f32 v184, v184\n"
"v_exp_f32 v185, v185\n"
"v_exp_f32 v186, v186\n"
"v_exp_f32 v187, v187\n"
"v_cvt_pk_bf16_f32 v173, v174, v175\n"
"v_cvt_pk_bf16_f32 v174, v184, v185\n"
"v_cvt_pk_bf16_f32 v175, v186, v187\n"
"v_exp_f32 v164, v164\n"
"v_exp_f32 v165, v165\n"
"v_mfma_f32_16x16x32_bf16 a[168:171], a[124:127], v[172:175], a[168:171]\n"
"v_exp_f32 v166, v166\n"
"v_mfma_f32_16x16x32_bf16 a[172:175], a[132:135], v[172:175], a[172:175]\n"
"v_exp_f32 v167, v167\n"
"v_mfma_f32_16x16x32_bf16 a[172:175], a[128:131], v[168:171], a[172:175]\n"
"v_cvt_pk_bf16_f32 v164, v164, v165\n"
"v_cvt_pk_bf16_f32 v165, v166, v167\n"
"v_mfma_f32_16x16x32_bf16 a[176:179], a[136:139], v[168:171], a[176:179]\n"
"v_exp_f32 v176, v176\n"
"v_mfma_f32_16x16x32_bf16 a[176:179], a[140:143], v[172:175], a[176:179]\n"
"v_exp_f32 v177, v177\n"
"v_smfmac_f32_16x16x64_bf16 a[220:223], a[228:231], v[168:175], v237\n"
"v_exp_f32 v178, v178\n"
"v_exp_f32 v179, v179\n"
"v_exp_f32 v160, v160\n"
"v_exp_f32 v161, v161\n"
"v_cvt_pk_bf16_f32 v166, v176, v177\n"
"v_cvt_pk_bf16_f32 v167, v178, v179\n"
"v_exp_f32 v162, v162\n"
"v_exp_f32 v163, v163\n"
"v_mfma_f32_16x16x32_bf16 v[104:107], a[140:143], v[164:167], v[104:107]\n"
"v_cvt_pk_bf16_f32 v160, v160, v161\n"
"v_exp_f32 v180, v180\n"
"v_exp_f32 v181, v181\n"
"v_exp_f32 v182, v182\n"
"v_exp_f32 v183, v183\n"
"v_cvt_pk_bf16_f32 v161, v162, v163\n"
"v_cvt_pk_bf16_f32 v162, v180, v181\n"
"v_cvt_pk_bf16_f32 v163, v182, v183\n"
"s_nop 1\n"
"v_mfma_f32_16x16x32_bf16 v[104:107], a[136:139], v[160:163], v[104:107]\n"
"v_mfma_f32_16x16x32_bf16 v[100:103], a[128:131], v[160:163], v[100:103]\n"
"v_mfma_f32_16x16x32_bf16 v[100:103], a[132:135], v[164:167], v[100:103]\n"
"v_mfma_f32_16x16x32_bf16 v[96:99], a[124:127], v[164:167], v[96:99]\n"
"v_mfma_f32_16x16x32_bf16 v[96:99], a[120:123], v[160:163], v[96:99]\n"
"s_waitcnt lgkmcnt(8)\n"
"v_smfmac_f32_16x16x64_bf16 a[220:223], a[224:227], v[160:167], v237\n"
"v_mfma_f32_16x16x32_bf16 a[180:183], a[144:147], v[168:171], a[180:183]\n"
"v_mfma_f32_16x16x32_bf16 a[180:183], a[148:151], v[172:175], a[180:183]\n"
"v_mfma_f32_16x16x32_bf16 a[184:187], a[156:159], v[172:175], a[184:187]\n"
"v_mfma_f32_16x16x32_bf16 a[184:187], a[152:155], v[168:171], a[184:187]\n"
"v_mfma_f32_16x16x32_bf16 a[188:191], a[160:163], v[168:171], a[188:191]\n"
"s_waitcnt vmcnt(3) lgkmcnt(0)\n"
"s_barrier\n"
"buffer_load_dwordx4 v234, s[44:47], s66 offen offset:0 lds\n"
"v_mfma_f32_16x16x32_bf16 a[188:191], a[164:167], v[172:175], a[188:191]\n"
"s_add_u32 m0, s97, 36864\n"
"ds_read_b64_tr_b16 a[112:113], v230 offset:26624\n"
"v_mfma_f32_16x16x32_bf16 v[116:119], a[164:167], v[164:167], v[116:119]\n"
"ds_read_b64_tr_b16 a[114:115], v230 offset:32768\n"
"v_mfma_f32_16x16x32_bf16 v[116:119], a[160:163], v[160:163], v[116:119]\n"
"ds_read_b64_tr_b16 a[116:117], v230 offset:38912\n"
"ds_read_b64_tr_b16 a[118:119], v230 offset:45056\n"
"v_mfma_f32_16x16x32_bf16 v[112:115], a[152:155], v[160:163], v[112:115]\n"
"ds_read_b64_tr_b16 a[152:153], v232 offset:26624\n"
"buffer_load_dwordx4 v234, s[44:47], s77 offen offset:0 lds\n"
"v_mfma_f32_16x16x32_bf16 v[112:115], a[156:159], v[164:167], v[112:115]\n"
"s_add_u32 m0, s97, 25472\n"
"ds_read_b64_tr_b16 a[154:155], v232 offset:32768\n"
"v_mfma_f32_16x16x32_bf16 v[108:111], a[148:151], v[164:167], v[108:111]\n"
"ds_read_b64_tr_b16 a[148:149], v231 offset:38912\n"
"v_mfma_f32_16x16x32_bf16 v[108:111], a[144:147], v[160:163], v[108:111]\n"
"ds_read_b64_tr_b16 a[144:145], v231 offset:26624\n"
"v_mfma_f32_16x16x32_bf16 a[192:195], a[96:99], v[168:171], a[192:195]\n"
"ds_read_b64_tr_b16 a[146:147], v231 offset:32768\n"
"buffer_load_dwordx4 v234, s[44:47], s66 offen offset:128 lds\n"
"v_mfma_f32_16x16x32_bf16 a[192:195], a[100:103], v[172:175], a[192:195]\n"
"s_add_u32 m0, s97, 37760\n"
"ds_read_b64_tr_b16 a[150:151], v231 offset:45056\n"
"v_mfma_f32_16x16x32_bf16 a[196:199], a[108:111], v[172:175], a[196:199]\n"
"ds_read_b64_tr_b16 a[156:157], v232 offset:38912\n"
"s_waitcnt lgkmcnt(7)\n"
"v_mfma_f32_16x16x32_bf16 a[196:199], a[104:107], v[168:171], a[196:199]\n"
"ds_read_b64_tr_b16 a[158:159], v232 offset:45056\n"
"v_mfma_f32_16x16x32_bf16 a[200:203], a[112:115], v[168:171], a[200:203]\n"
"ds_read_b64_tr_b16 a[160:161], v233 offset:26624\n"
"buffer_load_dwordx4 v234, s[44:47], s77 offen offset:128 lds\n"
"v_mfma_f32_16x16x32_bf16 a[200:203], a[116:119], v[172:175], a[200:203]\n"
"s_add_u32 m0, s97, 26368\n"
"ds_read_b64_tr_b16 a[162:163], v233 offset:32768\n"
"v_mfma_f32_16x16x32_bf16 v[128:131], a[116:119], v[164:167], v[128:131]\n"
"ds_read_b64_tr_b16 a[164:165], v233 offset:38912\n"
"v_mfma_f32_16x16x32_bf16 v[128:131], a[112:115], v[160:163], v[128:131]\n"
"ds_read_b64_tr_b16 a[166:167], v233 offset:45056\n"
"v_mfma_f32_16x16x32_bf16 v[124:127], a[104:107], v[160:163], v[124:127]\n"
"ds_read_b128 a[120:123], v228 offset:6144\n"
"v_mfma_f32_16x16x32_bf16 v[124:127], a[108:111], v[164:167], v[124:127]\n"
"ds_read_b128 a[124:127], v229 offset:6144\n"
"v_mfma_f32_16x16x32_bf16 v[120:123], a[100:103], v[164:167], v[120:123]\n"
"ds_read_b128 a[128:131], v228 offset:7168\n"
"v_mfma_f32_16x16x32_bf16 v[120:123], a[96:99], v[160:163], v[120:123]\n"
"ds_read_b128 a[132:135], v229 offset:7168\n"
"ds_read_b128 a[136:139], v228 offset:8192\n"
"ds_read_b128 a[140:143], v229 offset:8192\n"
"ds_read_b128 a[96:99], v228 offset:0\n"
"ds_read_b128 a[100:103], v229 offset:0\n"
"ds_read_b128 a[104:107], v228 offset:1024\n"
"s_waitcnt lgkmcnt(9)\n"
"v_mfma_f32_16x16x32_bf16 a[204:207], a[144:147], v[168:171], a[204:207]\n"
"ds_read_b128 a[108:111], v229 offset:1024\n"
"v_mfma_f32_16x16x32_bf16 a[204:207], a[148:151], v[172:175], a[204:207]\n"
"ds_read_b128 a[112:115], v228 offset:2048\n"
"v_mfma_f32_16x16x32_bf16 a[208:211], a[156:159], v[172:175], a[208:211]\n"
"ds_read_b128 a[116:119], v229 offset:2048\n"
"v_mfma_f32_16x16x32_bf16 a[208:211], a[152:155], v[168:171], a[208:211]\n"
"v_mfma_f32_16x16x32_bf16 a[212:215], a[160:163], v[168:171], a[212:215]\n"
"v_mfma_f32_16x16x32_bf16 a[212:215], a[164:167], v[172:175], a[212:215]\n"
"v_mfma_f32_16x16x32_bf16 v[140:143], a[164:167], v[164:167], v[140:143]\n"
"v_mfma_f32_16x16x32_bf16 v[140:143], a[160:163], v[160:163], v[140:143]\n"
"v_mfma_f32_16x16x32_bf16 v[136:139], a[152:155], v[160:163], v[136:139]\n"
"v_mfma_f32_16x16x32_bf16 v[136:139], a[156:159], v[164:167], v[136:139]\n"
"v_mfma_f32_16x16x32_bf16 v[132:135], a[148:151], v[164:167], v[132:135]\n"
"v_mfma_f32_16x16x32_bf16 v[132:135], a[144:147], v[160:163], v[132:135]\n"
"s_waitcnt vmcnt(4) lgkmcnt(0)\n"
"s_barrier\n"
"buffer_load_dwordx4 v234, s[44:47], s66 offen offset:256 lds\n"
"s_add_u32 m0, s97, 38656\n"
"s_mul_i32 s66, s67, 24576\n"
"buffer_load_dwordx4 v234, s[44:47], s77 offen offset:256 lds\n"
"s_setpc_b64 s[62:63]\n"
"L_P3:\n"
"ds_read_b128 a[144:147], v228 offset:12288\n"
"v_readlane_b32 s61, v235, s60\n"
"s_add_u32 s60, s60, 1\n"
"s_and_b32 s98, s61, 0x3ffff\n"
"ds_read_b128 a[148:151], v229 offset:12288\n"
"s_add_u32 s62, s64, s98\n"
"ds_read_b128 a[152:155], v228 offset:13312\n"
"s_addc_u32 s63, s65, 0\n"
"s_add_u32 m0, s97, 0\n"
"ds_read_b128 a[156:159], v229 offset:13312\n"
"buffer_load_dwordx4 v234, s[40:43], s66 offen offset:0 lds\n"
"ds_read_b128 a[160:163], v228 offset:14336\n"
"s_add_u32 m0, s97, 896\n"
"v_add_f32 v168, 0x71000000, v236\n"
"v_add_f32 v169, 0xf1000000, v236\n"
"v_add_f32 v170, 0xf1c00000, v236\n"
"v_add_f32 v171, 0xf2200000, v236\n"
"v_min_f32 v168, v168, v200\n"
"v_min_f32 v169, v169, v201\n"
"v_min_f32 v170, v170, v202\n"
"v_min_f32 v171, v171, v203\n"
"v_add_f32 v176, 0xf3780000, v236\n"
"v_add_f32 v177, 0xf3840000, v236\n"
"v_mfma_f32_16x16x32_bf16 v[168:171], a[96:99], a[72:75], v[168:171]\n"
"s_add_u32 s77, s66, 12288\n"
"ds_read_b128 a[164:167], v229 offset:14336\n"
"v_add_f32 v178, 0xf38c0000, v236\n"
"v_mfma_f32_16x16x32_bf16 v[168:171], a[100:103], a[76:79], v[168:171]\n"
"buffer_load_dwordx4 v234, s[40:43], s66 offen offset:128 lds\n"
"v_mfma_f32_16x16x32_bf16 v[168:171], a[104:107], a[80:83], v[168:171]\n"
"s_add_u32 m0, s97, 1792\n"
"ds_read_b128 a[96:99], v228 offset:18432\n"
"v_add_f32 v179, 0xf3940000, v236\n"
"v_mfma_f32_16x16x32_bf16 v[168:171], a[108:111], a[84:87], v[168:171]\n"
"s_lshr_b32 s67, s61, 18\n"
"ds_read_b128 a[100:103], v229 offset:18432\n"
"v_min_f32 v176, v176, v200\n"
"v_mfma_f32_16x16x32_bf16 v[168:171], a[112:115], a[88:91], v[168:171]\n"
"ds_read_b128 a[104:107], v228 offset:19456\n"
"v_min_f32 v177, v177, v201\n"
"v_mfma_f32_16x16x32_bf16 v[168:171], a[116:119], a[92:95], v[168:171]\n"
"ds_read_b128 a[108:111], v229 offset:19456\n"
"ds_read_b128 a[112:115], v228 offset:20480\n"
"buffer_load_dwordx4 v234, s[40:43], s66 offen offset:256 lds\n"
"ds_read_b128 a[116:119], v229 offset:20480\n"
"v_min_f32 v178, v178, v202\n"
"v_min_f32 v179, v179, v203\n"
"v_add_f32 v172, 0xf3fc0000, v236\n"
"v_add_f32 v173, 0xf4020000, v236\n"
"v_mfma_f32_16x16x32_bf16 v[176:179], a[120:123], a[72:75], v[176:179]\n"
"s_add_u32 m0, s97, 12288\n"
"v_add_f32 v174, 0xf4060000, v236\n"
"v_add_f32 v175, 0xf40a0000, v236\n"
"v_mfma_f32_16x16x32_bf16 v[176:179], a[124:127], a[76:79], v[176:179]\n"
"v_min_f32 v172, v172, v200\n"
"v_min_f32 v173, v173, v201\n"
"v_mfma_f32_16x16x32_bf16 v[176:179], a[128:131], a[80:83], v[176:179]\n"
"v_min_f32 v174, v174, v202\n"
"v_min_f32 v175, v175, v203\n"
"v_mfma_f32_16x16x32_bf16 v[176:179], a[132:135], a[84:87], v[176:179]\n"
"v_add_f32 v180, 0xf43e0000, v236\n"
"v_add_f32 v181, 0xf4420000, v236\n"
"v_mfma_f32_16x16x32_bf16 v[176:179], a[136:139], a[88:91], v[176:179]\n"
"s_waitcnt lgkmcnt(6)\n"
"v_add_f32 v182, 0xf4460000, v236\n"
"v_add_f32 v183, 0xf44a0000, v236\n"
"v_mfma_f32_16x16x32_bf16 v[176:179], a[140:143], a[92:95], v[176:179]\n"
"v_min_f32 v180, v180, v200\n"
"v_min_f32 v181, v181, v201\n"
"v_mfma_f32_16x16x32_bf16 v[172:175], a[144:147], a[72:75], v[172:175]\n"
"v_min_f32 v182, v182, v202\n"
"v_min_f32 v183, v183, v203\n"
"v_mfma_f32_16x16x32_bf16 v[172:175], a[148:151], a[76:79], v[172:175]\n"
"v_exp_f32 v168, v168\n"
"v_mfma_f32_16x16x32_bf16 v[172:175], a[152:155], a[80:83], v[172:175]\n"
"v_exp_f32 v169, v169\n"
"v_mfma_f32_16x16x32_bf16 v[172:175], a[156:159], a[84:87], v[172:175]\n"
"v_exp_f32 v170, v170\n"
"v_mfma_f32_16x16x32_bf16 v[172:175], a[160:163], a[88:91], v[172:175]\n"
"s_waitcnt vmcnt(5) lgkmcnt(0)\n"
"s_barrier\n"
"buffer_load_dwordx4 v234, s[40:43], s77 offen offset:0 lds\n"
"v_mfma_f32_16x16x32_bf16 v[172:175], a[164:167], a[92:95], v[172:175]\n"
"s_add_u32 m0, s97, 13184\n"
"ds_read_b64_tr_b16 a[120:121], v230 offset:24576\n"
"v_cvt_pk_bf16_f32 v168, v168, v169\n"
"v_mfma_f32_16x16x32_bf16 v[180:183], a[96:99], a[72:75], v[180:183]\n"
"ds_read_b64_tr_b16 a[122:123], v230 offset:30720\n"
"v_mfma_f32_16x16x32_bf16 v[180:183], a[100:103], a[76:79], v[180:183]\n"
"ds_read_b64_tr_b16 a[124:125], v230 offset:36864\n"
"v_mfma_f32_16x16x32_bf16 v[180:183], a[104:107], a[80:83], v[180:183]\n"
"ds_read_b64_tr_b16 a[126:127], v230 offset:43008\n"
"buffer_load_dwordx4 v234, s[40:43], s77 offen offset:128 lds\n"
"v_mfma_f32_16x16x32_bf16 v[180:183], a[108:111], a[84:87], v[180:183]\n"
"s_add_u32 m0, s97, 14080\n"
"ds_read_b64_tr_b16 a[128:129], v231 offset:24576\n"
"ds_read_b64_tr_b16 a[130:131], v231 offset:30720\n"
"ds_read_b64_tr_b16 a[132:133], v231 offset:36864\n"
"ds_read_b64_tr_b16 a[134:135], v231 offset:43008\n"
"ds_read_b64_tr_b16 a[136:137], v232 offset:24576\n"
"ds_read_b64_tr_b16 a[138:139], v232 offset:30720\n"
"ds_read_b64_tr_b16 a[140:141], v232 offset:36864\n"
"ds_read_b64_tr_b16 a[142:143], v232 offset:43008\n"
"ds_read_b64_tr_b16 a[144:145], v233 offset:24576\n"
"buffer_load_dwordx4 v234, s[40:43], s77 offen offset:256 lds\n"
"ds_read_b64_tr_b16 a[146:147], v233 offset:30720\n"
"ds_read_b64_tr_b16 a[148:149], v233 offset:36864\n"
"ds_read_b64_tr_b16 a[150:151], v233 offset:43008\n"
"ds_read_b64_tr_b16 a[152:153], v230 offset:25600\n"
"ds_read_b64_tr_b16 a[154:155], v230 offset:31744\n"
"ds_read_b64_tr_b16 a[156:157], v230 offset:37888\n"
"ds_read_b64_tr_b16 a[158:159], v230 offset:44032\n"
"ds_read_b64_tr_b16 a[160:161], v231 offset:25600\n"
"ds_read_b64_tr_b16 a[162:163], v231 offset:31744\n"
"ds_read_b64_tr_b16 a[164:165], v231 offset:37888\n"
"ds_read_b64_tr_b16 a[166:167], v231 offset:44032\n"
"ds_read_b64_tr_b16 a[96:97], v232 offset:25600\n"
"ds_read_b64_tr_b16 a[98:99], v232 offset:31744\n"
"ds_read_b64_tr_b16 a[100:101], v232 offset:37888\n"
"ds_read_b64_tr_b16 a[102:103], v232 offset:44032\n"
"ds_read_b64_tr_b16 a[104:105], v233 offset:25600\n"
"ds_read_b64_tr_b16 a[106:107], v233 offset:31744\n"
"ds_read_b64_tr_b16 a[108:109], v233 offset:37888\n"
"ds_read_b64_tr_b16 a[110:111], v233 offset:44032\n"
"s_add_u32 m0, s97, 24576\n"
"v_mfma_f32_16x16x32_bf16 v[180:183], a[112:115], a[88:91], v[180:183]\n"
"s_waitcnt lgkmcnt(15)\n"
"v_exp_f32 v171, v171\n"
"v_mfma_f32_16x16x32_bf16 v[180:183], a[116:119], a[92:95], v[180:183]\n"
"v_exp_f32 v176, v176\n"
"v_exp_f32 v177, v177\n"
"v_exp_f32 v178, v178\n"
"v_exp_f32 v179, v179\n"
"v_cvt_pk_bf16_f32 v169, v170, v171\n"
"v_cvt_pk_bf16_f32 v170, v176, v177\n"
"v_cvt_pk_bf16_f32 v171, v178, v179\n"
"v_exp_f32 v172, v172\n"
"v_exp_f32 v173, v173\n"
"v_mfma_f32_16x16x32_bf16 a[168:171], a[120:123], v[168:171], a[168:171]\n"
"v_exp_f32 v174, v174\n"
"v_exp_f32 v175, v175\n"
"v_cvt_pk_bf16_f32 v172, v172, v173\n"
"v_exp_f32 v180, v180\n"
"v_exp_f32 v181, v181\n"
"v_exp_f32 v182, v182\n"
"v_exp_f32 v183, v183\n"
"v_cvt_pk_bf16_f32 v173, v174, v175\n"
"v_cvt_pk_bf16_f32 v174, v180, v181\n"
"v_cvt_pk_bf16_f32 v175, v182, v183\n"
"s_nop 1\n"
"v_mfma_f32_16x16x32_bf16 a[168:171], a[124:127], v[172:175], a[168:171]\n"
"v_mfma_f32_16x16x32_bf16 a[172:175], a[132:135], v[172:175], a[172:175]\n"
"v_mfma_f32_16x16x32_bf16 a[172:175], a[128:131], v[168:171], a[172:175]\n"
"v_mfma_f32_16x16x32_bf16 a[176:179], a[136:139], v[168:171], a[176:179]\n"
"v_mfma_f32_16x16x32_bf16 a[176:179], a[140:143], v[172:175], a[176:179]\n"
"s_waitcnt lgkmcnt(8)\n"
"v_smfmac_f32_16x16x64_bf16 a[220:223], a[228:231], v[168:175], v237\n"
"v_mfma_f32_16x16x32_bf16 a[180:183], a[144:147], v[168:171], a[180:183]\n"
"v_mfma_f32_16x16x32_bf16 a[180:183], a[148:151], v[172:175], a[180:183]\n"
"v_mfma_f32_16x16x32_bf16 a[184:187], a[156:159], v[172:175], a[184:187]\n"
"v_mfma_f32_16x16x32_bf16 a[184:187], a[152:155], v[168:171], a[184:187]\n"
"v_mfma_f32_16x16x32_bf16 a[188:191], a[160:163], v[168:171], a[188:191]\n"
"v_mfma_f32_16x16x32_bf16 a[188:191], a[164:167], v[172:175], a[188:191]\n"
"s_waitcnt vmcnt(3) lgkmcnt(0)\n"
"s_barrier\n"
"buffer_load_dwordx4 v234, s[44:47], s66 offen offset:0 lds\n"
"ds_read_b64_tr_b16 a[112:113], v230 offset:26624\n"
"ds_read_b64_tr_b16 a[114:115], v230 offset:32768\n"
"ds_read_b64_tr_b16 a[116:117], v230 offset:38912\n"
"s_add_u32 m0, s97, 36864\n"
"ds_read_b64_tr_b16 a[118:119], v230 offset:45056\n"
"v_mfma_f32_16x16x32_bf16 a[192:195], a[96:99], v[168:171], a[192:195]\n"
"ds_read_b64_tr_b16 a[144:145], v231 offset:26624\n"
"v_mfma_f32_16x16x32_bf16 a[192:195], a[100:103], v[172:175], a[192:195]\n"
"ds_read_b64_tr_b16 a[146:147], v231 offset:32768\n"
"v_mfma_f32_16x16x32_bf16 a[196:199], a[108:111], v[172:175], a[196:199]\n"
"ds_read_b64_tr_b16 a[148:149], v231 offset:38912\n"
"buffer_load_dwordx4 v234, s[44:47], s77 offen offset:0 lds\n"
"v_mfma_f32_16x16x32_bf16 a[196:199], a[104:107], v[168:171], a[196:199]\n"
"s_add_u32 m0, s97, 25472\n"
"ds_read_b64_tr_b16 a[150:151], v231 offset:45056\n"
"ds_read_b64_tr_b16 a[152:153], v232 offset:26624\n"
"ds_read_b64_tr_b16 a[154:155], v232 offset:32768\n"
"ds_read_b64_tr_b16 a[156:157], v232 offset:38912\n"
"ds_read_b64_tr_b16 a[158:159], v232 offset:45056\n"
"ds_read_b64_tr_b16 a[160:161], v233 offset:26624\n"
"ds_read_b64_tr_b16 a[162:163], v233 offset:32768\n"
"ds_read_b64_tr_b16 a[164:165], v233 offset:38912\n"
"ds_read_b64_tr_b16 a[166:167], v233 offset:45056\n"
"buffer_load_dwordx4 v234, s[44:47], s66 offen offset:128 lds\n"
"ds_read_b128 a[120:123], v228 offset:6144\n"
"ds_read_b128 a[124:127], v229 offset:6144\n"
"s_add_u32 m0, s97, 37760\n"
"s_waitcnt lgkmcnt(14)\n"
"v_mfma_f32_16x16x32_bf16 a[200:203], a[112:115], v[168:171], a[200:203]\n"
"ds_read_b128 a[128:131], v228 offset:7168\n"
"v_mfma_f32_16x16x32_bf16 a[200:203], a[116:119], v[172:175], a[200:203]\n"
"ds_read_b128 a[132:135], v229 offset:7168\n"
"ds_read_b128 a[136:139], v228 offset:8192\n"
"buffer_load_dwordx4 v234, s[44:47], s77 offen offset:128 lds\n"
"ds_read_b128 a[140:143], v229 offset:8192\n"
"ds_read_b128 a[96:99], v228 offset:0\n"
"s_add_u32 m0, s97, 26368\n"
"ds_read_b128 a[100:103], v229 offset:0\n"
"ds_read_b128 a[104:107], v228 offset:1024\n"
"s_waitcnt lgkmcnt(9)\n"
"v_mfma_f32_16x16x32_bf16 a[204:207], a[144:147], v[168:171], a[204:207]\n"
"ds_read_b128 a[108:111], v229 offset:1024\n"
"v_mfma_f32_16x16x32_bf16 a[204:207], a[148:151], v[172:175], a[204:207]\n"
"ds_read_b128 a[112:115], v228 offset:2048\n"
"v_mfma_f32_16x16x32_bf16 a[208:211], a[156:159], v[172:175], a[208:211]\n"
"ds_read_b128 a[116:119], v229 offset:2048\n"
"v_mfma_f32_16x16x32_bf16 a[208:211], a[152:155], v[168:171], a[208:211]\n"
"v_mfma_f32_16x16x32_bf16 a[212:215], a[160:163], v[168:171], a[212:215]\n"
"v_mfma_f32_16x16x32_bf16 a[212:215], a[164:167], v[172:175], a[212:215]\n"
"s_waitcnt vmcnt(4) lgkmcnt(0)\n"
"s_barrier\n"
"buffer_load_dwordx4 v234, s[44:47], s66 offen offset:256 lds\n"
"s_add_u32 m0, s97, 38656\n"
"s_mul_i32 s66, s67, 24576\n"
"buffer_load_dwordx4 v234, s[44:47], s77 offen offset:256 lds\n"
"s_setpc_b64 s[62:63]\n"
"L_UM:\n"
"v_add_f32 v236, 0x75000000, v236\n"
"s_branch L_P3\n"
"L_MM:\n"
"v_add_f32 v236, 0xf5000000, v236\n"
"s_branch L_P3\n"
".p2align 12\n"
".p2align 5\n"
"L_W0D1:\n"
"s_nop 7\n"
"s_branch L_TABDONE\n"
"L_W2D1:\n"
"s_nop 7\n"
"s_branch L_W2D3\n"
"L_D1:\n"
"v_readlane_b32 s61, v235, s60\n"
"s_add_u32 s60, s60, 1\n"
"s_and_b32 s98, s61, 0x3ffff\n"
"s_add_u32 s62, s64, s98\n"
"s_addc_u32 s63, s65, 0\n"
"s_add_u32 m0, s97, 0\n"
"v_mfma_f32_16x16x32_bf16 v[160:163], a[96:99], a[48:51], v[196:199]\n"
"s_add_u32 s77, s66, 12288\n"
"buffer_load_dwordx4 v234, s[40:43], s66 offen offset:0 lds\n"
"v_mfma_f32_16x16x32_bf16 v[160:163], a[100:103], a[52:55], v[160:163]\n"
"s_add_u32 m0, s97, 896\n"
"ds_read_b128 a[0:3], v238 offset:0\n"
"v_add_f32 v152, 0x71000000, v236\n"
"v_mfma_f32_16x16x32_bf16 v[160:163], a[104:107], a[56:59], v[160:163]\n"
"ds_read_b128 a[4:7], v238 offset:3072\n"
"v_add_f32 v153, 0xf1000000, v236\n"
"v_mfma_f32_16x16x32_bf16 v[160:163], a[108:111], a[60:63], v[160:163]\n"
"s_nop 0\n"
"ds_read_b128 a[8:11], v238 offset:1024\n"
"v_accvgpr_read_b32 v254, a216\n"
"v_mfma_f32_16x16x32_bf16 v[160:163], a[112:115], a[64:67], v[160:163]\n"
"s_add_u32 s36, s89, 0\n"
"ds_read_b128 a[12:15], v238 offset:4096\n"
"v_add_f32 v244, v254, v244\n"
"v_mfma_f32_16x16x32_bf16 v[160:163], a[116:119], a[68:71], v[160:163]\n"
"buffer_load_dwordx4 v234, s[40:43], s66 offen offset:128 lds\n"
"v_mfma_f32_16x16x32_bf16 v[176:179], a[120:123], a[48:51], v[196:199]\n"
"s_add_u32 m0, s97, 1792\n"
"ds_read_b128 a[144:147], v228 offset:12288\n"
"v_accvgpr_read_b32 v254, a217\n"
"v_mfma_f32_16x16x32_bf16 v[176:179], a[124:127], a[52:55], v[176:179]\n"
"ds_read_b128 a[148:151], v229 offset:12288\n"
"v_add_f32 v254, v254, v244\n"
"v_mfma_f32_16x16x32_bf16 v[176:179], a[128:131], a[56:59], v[176:179]\n"
"ds_read_b128 a[152:155], v228 offset:13312\n"
"v_cmp_nle_f32 vcc, 0x1f800000, v254\n"
"v_mfma_f32_16x16x32_bf16 v[176:179], a[132:135], a[60:63], v[176:179]\n"
"s_or_b64 s[86:87], s[86:87], vcc\n"
"ds_read_b128 a[156:159], v229 offset:13312\n"
"v_cmp_nge_f32 vcc, 0x67800000, v254\n"
"v_mfma_f32_16x16x32_bf16 v[176:179], a[136:139], a[64:67], v[176:179]\n"
"buffer_load_dwordx4 v234, s[40:43], s66 offen offset:256 lds\n"
"v_mfma_f32_16x16x32_bf16 v[176:179], a[140:143], a[68:71], v[176:179]\n"
"s_waitcnt lgkmcnt(6)\n"
"ds_read_b128 a[160:163], v228 offset:14336\n"
"ds_read_b128 a[164:167], v229 offset:14336\n"
"v_rcp_f32 v244, v254\n"
"v_add_f32 v154, 0xf1c00000, v236\n"
"v_mul_f32 v254, s81, v244\n"
"v_pk_mul_f32 v[0:1], v[0:1], v[254:255] op_sel_hi:[1,0]\n"
"v_pk_mul_f32 v[2:3], v[2:3], v[254:255] op_sel_hi:[1,0]\n"
"v_pk_mul_f32 v[4:5], v[4:5], v[254:255] op_sel_hi:[1,0]\n"
"v_pk_mul_f32 v[6:7], v[6:7], v[254:255] op_sel_hi:[1,0]\n"
"v_mfma_f32_16x16x32_bf16 v[0:3], v[250:253], a[0:3], v[0:3]\n"
"s_waitcnt lgkmcnt(6)\n"
"ds_read_b128 a[16:19], v238 offset:2048\n"
"v_add_f32 v155, 0xf2200000, v236\n"
"v_mfma_f32_16x16x32_bf16 v[4:7], v[248:251], a[0:3], v[4:7]\n"
"s_add_u32 m0, s97, 12288\n"
"ds_read_b128 a[20:23], v238 offset:5120\n"
"v_min_f32 v152, v152, v192\n"
"v_min_f32 v153, v153, v193\n"
"v_min_f32 v154, v154, v194\n"
"v_min_f32 v155, v155, v195\n"
"v_pk_mul_f32 v[8:9], v[8:9], v[254:255] op_sel_hi:[1,0]\n"
"v_pk_mul_f32 v[10:11], v[10:11], v[254:255] op_sel_hi:[1,0]\n"
"v_pk_mul_f32 v[12:13], v[12:13], v[254:255] op_sel_hi:[1,0]\n"
"v_pk_mul_f32 v[14:15], v[14:15], v[254:255] op_sel_hi:[1,0]\n"
"v_mfma_f32_16x16x32_bf16 v[8:11], v[250:253], a[4:7], v[8:11]\n"
"s_lshr_b32 s67, s61, 18\n"
"v_cvt_pk_bf16_f32 v0, v0, v1\n"
"v_cvt_pk_bf16_f32 v1, v2, v3\n"
"v_mfma_f32_16x16x32_bf16 v[12:15], v[248:251], a[4:7], v[12:15]\n"
"s_or_b64 s[86:87], s[86:87], vcc\n"
"v_cvt_pk_bf16_f32 v4, v4, v5\n"
"v_cvt_pk_bf16_f32 v5, v6, v7\n"
"v_mfma_f32_16x16x32_bf16 v[152:155], a[96:99], a[24:27], v[152:155]\n"
"ds_write_b64 v240, v[0:1] offset:0\n"
"v_mfma_f32_16x16x32_bf16 v[152:155], a[100:103], a[28:31], v[152:155]\n"
"v_cvt_pk_bf16_f32 v8, v8, v9\n"
"v_cvt_pk_bf16_f32 v9, v10, v11\n"
"v_mfma_f32_16x16x32_bf16 v[152:155], a[104:107], a[32:35], v[152:155]\n"
"ds_write_b64 v241, v[4:5] offset:0\n"
"v_mfma_f32_16x16x32_bf16 v[152:155], a[108:111], a[36:39], v[152:155]\n"
"v_cvt_pk_bf16_f32 v12, v12, v13\n"
"v_cvt_pk_bf16_f32 v13, v14, v15\n"
"v_mfma_f32_16x16x32_bf16 v[152:155], a[112:115], a[40:43], v[152:155]\n"
"s_waitcnt lgkmcnt(2)\n"
"v_add_f32 v180, 0xf3780000, v236\n"
"v_add_f32 v181, 0xf3840000, v236\n"
"v_mfma_f32_16x16x32_bf16 v[152:155], a[116:119], a[44:47], v[152:155]\n"
"ds_write_b64 v242, v[8:9] offset:0\n"
"v_add_f32 v182, 0xf38c0000, v236\n"
"v_add_f32 v183, 0xf3940000, v236\n"
"v_min_f32 v180, v180, v192\n"
"v_min_f32 v181, v181, v193\n"
"v_min_f32 v182, v182, v194\n"
"v_min_f32 v183, v183, v195\n"
"v_pk_mul_f32 v[16:17], v[16:17], v[254:255] op_sel_hi:[1,0]\n"
"v_pk_mul_f32 v[18:19], v[18:19], v[254:255] op_sel_hi:[1,0]\n"
"v_mfma_f32_16x16x32_bf16 v[180:183], a[120:123], a[24:27], v[180:183]\n"
"ds_write_b64 v243, v[12:13] offset:0\n"
"v_mfma_f32_16x16x32_bf16 v[180:183], a[124:127], a[28:31], v[180:183]\n"
"v_exp_f32 v160, v160\n"
"v_mfma_f32_16x16x32_bf16 v[180:183], a[128:131], a[32:35], v[180:183]\n"
"ds_read_b128 v[0:3], v238 offset:0\n"
"ds_read_b128 v[4:7], v238 offset:3072\n"
"v_mfma_f32_16x16x32_bf16 v[180:183], a[132:135], a[36:39], v[180:183]\n"
"v_exp_f32 v161, v161\n"
"v_mfma_f32_16x16x32_bf16 v[180:183], a[136:139], a[40:43], v[180:183]\n"
"v_exp_f32 v162, v162\n"
"v_mfma_f32_16x16x32_bf16 v[180:183], a[140:143], a[44:47], v[180:183]\n"
"v_exp_f32 v163, v163\n"
"v_mfma_f32_16x16x32_bf16 v[16:19], v[250:253], a[8:11], v[16:19]\n"
"v_cvt_pk_bf16_f32 v160, v160, v161\n"
"v_exp_f32 v176, v176\n"
"v_exp_f32 v177, v177\n"
"v_pk_mul_f32 v[20:21], v[20:21], v[254:255] op_sel_hi:[1,0]\n"
"v_pk_mul_f32 v[22:23], v[22:23], v[254:255] op_sel_hi:[1,0]\n"
"v_pk_mul_f32 v[24:25], v[24:25], v[254:255] op_sel_hi:[1,0]\n"
"v_pk_mul_f32 v[26:27], v[26:27], v[254:255] op_sel_hi:[1,0]\n"
"v_mfma_f32_16x16x32_bf16 v[20:23], v[248:251], a[8:11], v[20:23]\n"
"v_cvt_pk_bf16_f32 v16, v16, v17\n"
"v_cvt_pk_bf16_f32 v17, v18, v19\n"
"v_mfma_f32_16x16x32_bf16 v[24:27], v[250:253], a[12:15], v[24:27]\n"
"ds_write_b64 v240, v[16:17] offset:1024\n"
"v_exp_f32 v178, v178\n"
"s_waitcnt lgkmcnt(1)\n"
"buffer_store_dwordx4 v[0:3], v239, s[52:55], s36 offen offset:0 sc1\n"
"v_pk_mul_f32 v[28:29], v[28:29], v[254:255] op_sel_hi:[1,0]\n"
"v_pk_mul_f32 v[30:31], v[30:31], v[254:255] op_sel_hi:[1,0]\n"
"v_cvt_pk_bf16_f32 v20, v20, v21\n"
"v_cvt_pk_bf16_f32 v21, v22, v23\n"
"v_mfma_f32_16x16x32_bf16 v[28:31], v[248:251], a[12:15], v[28:31]\n"
"ds_write_b64 v241, v[20:21] offset:1024\n"
"v_mfma_f32_16x16x32_bf16 v[168:171], a[96:99], a[72:75], v[200:203]\n"
"v_cvt_pk_bf16_f32 v24, v24, v25\n"
"v_cvt_pk_bf16_f32 v25, v26, v27\n"
"v_mfma_f32_16x16x32_bf16 v[168:171], a[100:103], a[76:79], v[168:171]\n"
"buffer_store_dwordx4 v[4:7], v239, s[52:55], s36 offen offset:3072 sc1\n"
"v_mfma_f32_16x16x32_bf16 v[168:171], a[104:107], a[80:83], v[168:171]\n"
"ds_read_b128 a[96:99], v228 offset:18432\n"
"v_cvt_pk_bf16_f32 v28, v28, v29\n"
"v_mfma_f32_16x16x32_bf16 v[168:171], a[108:111], a[84:87], v[168:171]\n"
"ds_read_b128 a[100:103], v229 offset:18432\n"
"v_cvt_pk_bf16_f32 v29, v30, v31\n"
"v_mfma_f32_16x16x32_bf16 v[168:171], a[112:115], a[88:91], v[168:171]\n"
"ds_read_b128 a[104:107], v228 offset:19456\n"
"v_cvt_pk_bf16_f32 v161, v162, v163\n"
"v_mfma_f32_16x16x32_bf16 v[168:171], a[116:119], a[92:95], v[168:171]\n"
"ds_read_b128 a[108:111], v229 offset:19456\n"
"ds_read_b128 a[112:115], v228 offset:20480\n"
"v_mfma_f32_16x16x32_bf16 v[184:187], a[120:123], a[72:75], v[200:203]\n"
"ds_read_b128 a[116:119], v229 offset:20480\n"
"v_mfma_f32_16x16x32_bf16 v[184:187], a[124:127], a[76:79], v[184:187]\n"
"ds_write_b64 v242, v[24:25] offset:1024\n"
"v_mfma_f32_16x16x32_bf16 v[184:187], a[128:131], a[80:83], v[184:187]\n"
"v_exp_f32 v179, v179\n"
"v_mfma_f32_16x16x32_bf16 v[184:187], a[132:135], a[84:87], v[184:187]\n"
"ds_write_b64 v243, v[28:29] offset:1024\n"
"v_mfma_f32_16x16x32_bf16 v[184:187], a[136:139], a[88:91], v[184:187]\n"
"v_cvt_pk_bf16_f32 v162, v176, v177\n"
"v_cvt_pk_bf16_f32 v163, v178, v179\n"
"v_mfma_f32_16x16x32_bf16 v[184:187], a[140:143], a[92:95], v[184:187]\n"
"ds_read_b128 v[16:19], v238 offset:1024\n"
"ds_read_b128 v[20:23], v238 offset:4096\n"
"v_add_f32 v156, 0xf3fc0000, v236\n"
"v_add_f32 v157, 0xf4020000, v236\n"
"v_pk_mul_f32 v[32:33], v[32:33], v[254:255] op_sel_hi:[1,0]\n"
"v_pk_mul_f32 v[34:35], v[34:35], v[254:255] op_sel_hi:[1,0]\n"
"v_pk_mul_f32 v[36:37], v[36:37], v[254:255] op_sel_hi:[1,0]\n"
"v_pk_mul_f32 v[38:39], v[38:39], v[254:255] op_sel_hi:[1,0]\n"
"v_mfma_f32_16x16x32_bf16 v[32:35], v[250:253], a[16:19], v[32:35]\n"
"v_add_f32 v158, 0xf4060000, v236\n"
"v_add_f32 v159, 0xf40a0000, v236\n"
"v_mfma_f32_16x16x32_bf16 v[36:39], v[248:251], a[16:19], v[36:39]\n"
"v_min_f32 v156, v156, v192\n"
"v_min_f32 v157, v157, v193\n"
"v_min_f32 v158, v158, v194\n"
"v_min_f32 v159, v159, v195\n"
"v_exp_f32 v152, v152\n"
"v_pk_mul_f32 v[40:41], v[40:41], v[254:255] op_sel_hi:[1,0]\n"
"v_pk_mul_f32 v[42:43], v[42:43], v[254:255] op_sel_hi:[1,0]\n"
"v_pk_mul_f32 v[44:45], v[44:45], v[254:255] op_sel_hi:[1,0]\n"
"v_pk_mul_f32 v[46:47], v[46:47], v[254:255] op_sel_hi:[1,0]\n"
"v_mfma_f32_16x16x32_bf16 v[40:43], v[250:253], a[20:23], v[40:43]\n"
"v_cvt_pk_bf16_f32 v32, v32, v33\n"
"v_cvt_pk_bf16_f32 v33, v34, v35\n"
"v_mfma_f32_16x16x32_bf16 v[44:47], v[248:251], a[20:23], v[44:47]\n"
"ds_write_b64 v240, v[32:33] offset:2048\n"
"v_mfma_f32_16x16x32_bf16 v[164:167], a[144:147], a[48:51], v[196:199]\n"
"v_cvt_pk_bf16_f32 v36, v36, v37\n"
"s_waitcnt lgkmcnt(1)\n"
"buffer_store_dwordx4 v[16:19], v239, s[52:55], s36 offen offset:128 sc1\n"
"v_mfma_f32_16x16x32_bf16 v[164:167], a[148:151], a[52:55], v[164:167]\n"
"v_cvt_pk_bf16_f32 v37, v38, v39\n"
"v_cvt_pk_bf16_f32 v40, v40, v41\n"
"v_mfma_f32_16x16x32_bf16 v[164:167], a[152:155], a[56:59], v[164:167]\n"
"ds_write_b64 v241, v[36:37] offset:2048\n"
"v_mfma_f32_16x16x32_bf16 v[164:167], a[156:159], a[60:63], v[164:167]\n"
"v_cvt_pk_bf16_f32 v41, v42, v43\n"
"v_cvt_pk_bf16_f32 v44, v44, v45\n"
"v_mfma_f32_16x16x32_bf16 v[164:167], a[160:163], a[64:67], v[164:167]\n"
"s_waitcnt vmcnt(8)\n"
"s_barrier\n"
"buffer_store_dwordx4 v[20:23], v239, s[52:55], s36 offen offset:3200 sc1\n"
"v_mfma_f32_16x16x32_bf16 v[164:167], a[164:167], a[68:71], v[164:167]\n"
"ds_write_b64 v242, v[40:41] offset:2048\n"
"v_cvt_pk_bf16_f32 v45, v46, v47\n"
"v_mfma_f32_16x16x32_bf16 v[176:179], a[96:99], a[48:51], v[196:199]\n"
"ds_write_b64 v243, v[44:45] offset:2048\n"
"buffer_load_dwordx4 v234, s[40:43], s77 offen offset:0 lds\n"
"ds_read_b128 v[32:35], v238 offset:2048\n"
"s_add_u32 m0, s97, 13184\n"
"ds_read_b128 v[36:39], v238 offset:5120\n"
"v_mfma_f32_16x16x32_bf16 v[176:179], a[100:103], a[52:55], v[176:179]\n"
"ds_read_b64_tr_b16 a[120:121], v230 offset:24576\n"
"v_mfma_f32_16x16x32_bf16 v[176:179], a[104:107], a[56:59], v[176:179]\n"
"ds_read_b64_tr_b16 a[122:123], v230 offset:30720\n"
"v_mfma_f32_16x16x32_bf16 v[176:179], a[108:111], a[60:63], v[176:179]\n"
"ds_read_b64_tr_b16 a[124:125], v230 offset:36864\n"
"buffer_load_dwordx4 v234, s[40:43], s77 offen offset:128 lds\n"
"v_mfma_f32_16x16x32_bf16 v[176:179], a[112:115], a[64:67], v[176:179]\n"
"s_add_u32 m0, s97, 14080\n"
"ds_read_b64_tr_b16 a[126:127], v230 offset:43008\n"
"v_mfma_f32_16x16x32_bf16 v[176:179], a[116:119], a[68:71], v[176:179]\n"
"ds_read_b64_tr_b16 a[128:129], v231 offset:24576\n"
"v_mfma_f32_16x16x32_bf16 v[156:159], a[144:147], a[24:27], v[156:159]\n"
"ds_read_b64_tr_b16 a[130:131], v231 offset:30720\n"
"v_mfma_f32_16x16x32_bf16 v[156:159], a[148:151], a[28:31], v[156:159]\n"
"ds_read_b64_tr_b16 a[132:133], v231 offset:36864\n"
"buffer_load_dwordx4 v234, s[40:43], s77 offen offset:256 lds\n"
"v_mfma_f32_16x16x32_bf16 v[156:159], a[152:155], a[32:35], v[156:159]\n"
"s_waitcnt lgkmcnt(7)\n"
"ds_read_b64_tr_b16 a[134:135], v231 offset:43008\n"
"s_add_u32 m0, s97, 24576\n"
"v_mfma_f32_16x16x32_bf16 v[156:159], a[156:159], a[36:39], v[156:159]\n"
"ds_read_b64_tr_b16 a[136:137], v232 offset:24576\n"
"v_mfma_f32_16x16x32_bf16 v[156:159], a[160:163], a[40:43], v[156:159]\n"
"ds_read_b64_tr_b16 a[138:139], v232 offset:30720\n"
"v_exp_f32 v153, v153\n"
"v_exp_f32 v154, v154\n"
"v_exp_f32 v155, v155\n"
"v_cvt_pk_bf16_f32 v152, v152, v153\n"
"v_exp_f32 v180, v180\n"
"v_exp_f32 v181, v181\n"
"v_exp_f32 v182, v182\n"
"v_exp_f32 v183, v183\n"
"v_mfma_f32_16x16x32_bf16 v[156:159], a[164:167], a[44:47], v[156:159]\n"
"buffer_store_dwordx4 v[32:35], v239, s[52:55], s36 offen offset:256 sc1\n"
"ds_read_b64_tr_b16 a[140:141], v232 offset:36864\n"
"ds_read_b64_tr_b16 a[142:143], v232 offset:43008\n"
"v_cvt_pk_bf16_f32 v153, v154, v155\n"
"v_cvt_pk_bf16_f32 v154, v180, v181\n"
"v_cvt_pk_bf16_f32 v155, v182, v183\n"
"v_add_f32 v180, 0xf43e0000, v236\n"
"v_add_f32 v181, 0xf4420000, v236\n"
"v_add_f32 v182, 0xf4460000, v236\n"
"v_add_f32 v183, 0xf44a0000, v236\n"
"v_min_f32 v180, v180, v192\n"
"v_min_f32 v181, v181, v193\n"
"v_min_f32 v182, v182, v194\n"
"v_min_f32 v183, v183, v195\n"
"v_exp_f32 v168, v168\n"
"v_exp_f32 v169, v169\n"
"v_mfma_f32_16x16x32_bf16 v[180:183], a[96:99], a[24:27], v[180:183]\n"
"buffer_store_dwordx4 v[36:39], v239, s[52:55], s36 offen offset:3328 sc1\n"
"v_mfma_f32_16x16x32_bf16 v[180:183], a[100:103], a[28:31], v[180:183]\n"
"v_exp_f32 v170, v170\n"
"v_mfma_f32_16x16x32_bf16 v[180:183], a[104:107], a[32:35], v[180:183]\n"
"v_exp_f32 v171, v171\n"
"v_mfma_f32_16x16x32_bf16 v[180:183], a[108:111], a[36:39], v[180:183]\n"
"v_cvt_pk_bf16_f32 v168, v168, v169\n"
"v_cvt_pk_bf16_f32 v169, v170, v171\n"
"v_mfma_f32_16x16x32_bf16 v[180:183], a[112:115], a[40:43], v[180:183]\n"
"v_exp_f32 v184, v184\n"
"v_mfma_f32_16x16x32_bf16 v[180:183], a[116:119], a[44:47], v[180:183]\n"
"v_exp_f32 v185, v185\n"
"v_mfma_f32_16x16x32_bf16 v[172:175], a[144:147], a[72:75], v[200:203]\n"
"ds_read_b64_tr_b16 a[144:145], v233 offset:24576\n"
"v_cvt_pk_bf16_f32 v170, v184, v185\n"
"v_mfma_f32_16x16x32_bf16 v[172:175], a[148:151], a[76:79], v[172:175]\n"
"ds_read_b64_tr_b16 a[146:147], v233 offset:30720\n"
"v_mfma_f32_16x16x32_bf16 v[172:175], a[152:155], a[80:83], v[172:175]\n"
"ds_read_b64_tr_b16 a[148:149], v233 offset:36864\n"
"v_mfma_f32_16x16x32_bf16 v[172:175], a[156:159], a[84:87], v[172:175]\n"
"ds_read_b64_tr_b16 a[150:151], v233 offset:43008\n"
"v_mfma_f32_16x16x32_bf16 v[172:175], a[160:163], a[88:91], v[172:175]\n"
"ds_read_b64_tr_b16 a[152:153], v230 offset:25600\n"
"v_mfma_f32_16x16x32_bf16 v[172:175], a[164:167], a[92:95], v[172:175]\n"
"ds_read_b64_tr_b16 a[154:155], v230 offset:31744\n"
"ds_read_b64_tr_b16 a[156:157], v230 offset:37888\n"
"ds_read_b64_tr_b16 a[158:159], v230 offset:44032\n"
"ds_read_b64_tr_b16 a[160:161], v231 offset:25600\n"
"ds_read_b64_tr_b16 a[162:163], v231 offset:31744\n"
"ds_read_b64_tr_b16 a[164:165], v231 offset:37888\n"
"ds_read_b64_tr_b16 a[166:167], v231 offset:44032\n"
"v_exp_f32 v186, v186\n"
"v_exp_f32 v187, v187\n"
"v_exp_f32 v164, v164\n"
"v_cvt_pk_bf16_f32 v171, v186, v187\n"
"v_mfma_f32_16x16x32_bf16 v[184:187], a[96:99], a[72:75], v[200:203]\n"
"ds_read_b64_tr_b16 a[96:97], v232 offset:25600\n"
"v_mfma_f32_16x16x32_bf16 v[184:187], a[100:103], a[76:79], v[184:187]\n"
"ds_read_b64_tr_b16 a[98:99], v232 offset:31744\n"
"v_mfma_f32_16x16x32_bf16 v[184:187], a[104:107], a[80:83], v[184:187]\n"
"ds_read_b64_tr_b16 a[100:101], v232 offset:37888\n"
"v_mfma_f32_16x16x32_bf16 v[184:187], a[108:111], a[84:87], v[184:187]\n"
"ds_read_b64_tr_b16 a[102:103], v232 offset:44032\n"
"ds_read_b64_tr_b16 a[104:105], v233 offset:25600\n"
"ds_read_b64_tr_b16 a[106:107], v233 offset:31744\n"
"ds_read_b64_tr_b16 a[108:109], v233 offset:37888\n"
"ds_read_b64_tr_b16 a[110:111], v233 offset:44032\n"
"v_mfma_f32_16x16x32_bf16 v[184:187], a[112:115], a[88:91], v[184:187]\n"
"s_waitcnt lgkmcnt(15)\n"
"v_exp_f32 v165, v165\n"
"v_mfma_f32_16x16x32_bf16 v[184:187], a[116:119], a[92:95], v[184:187]\n"
"v_exp_f32 v166, v166\n"
"v_mfma_f32_16x16x32_bf16 v[96:99], a[120:123], v[160:163], v[96:99]\n"
"v_exp_f32 v167, v167\n"
"v_cvt_pk_bf16_f32 v164, v164, v165\n"
"v_exp_f32 v176, v176\n"
"v_exp_f32 v177, v177\n"
"v_exp_f32 v178, v178\n"
"v_exp_f32 v179, v179\n"
"v_cvt_pk_bf16_f32 v165, v166, v167\n"
"v_cvt_pk_bf16_f32 v166, v176, v177\n"
"v_cvt_pk_bf16_f32 v167, v178, v179\n"
"v_exp_f32 v156, v156\n"
"v_exp_f32 v157, v157\n"
"v_mfma_f32_16x16x32_bf16 v[96:99], a[124:127], v[164:167], v[96:99]\n"
"v_exp_f32 v158, v158\n"
"v_mfma_f32_16x16x32_bf16 v[100:103], a[132:135], v[164:167], v[100:103]\n"
"v_exp_f32 v159, v159\n"
"v_mfma_f32_16x16x32_bf16 v[100:103], a[128:131], v[160:163], v[100:103]\n"
"v_cvt_pk_bf16_f32 v156, v156, v157\n"
"v_cvt_pk_bf16_f32 v157, v158, v159\n"
"v_mfma_f32_16x16x32_bf16 v[104:107], a[136:139], v[160:163], v[104:107]\n"
"v_exp_f32 v180, v180\n"
"v_mfma_f32_16x16x32_bf16 v[104:107], a[140:143], v[164:167], v[104:107]\n"
"v_exp_f32 v181, v181\n"
"v_smfmac_f32_16x16x64_bf16 a[220:223], a[224:227], v[160:167], v237\n"
"v_exp_f32 v182, v182\n"
"v_exp_f32 v183, v183\n"
"v_cvt_pk_bf16_f32 v158, v180, v181\n"
"v_cvt_pk_bf16_f32 v159, v182, v183\n"
"v_exp_f32 v172, v172\n"
"v_exp_f32 v173, v173\n"
"v_mfma_f32_16x16x32_bf16 v[56:59], a[140:143], v[156:159], v[56:59]\n"
"v_exp_f32 v174, v174\n"
"v_mfma_f32_16x16x32_bf16 v[56:59], a[136:139], v[152:155], v[56:59]\n"
"v_exp_f32 v175, v175\n"
"v_mfma_f32_16x16x32_bf16 v[52:55], a[128:131], v[152:155], v[52:55]\n"
"v_cvt_pk_bf16_f32 v172, v172, v173\n"
"v_cvt_pk_bf16_f32 v173, v174, v175\n"
"v_mfma_f32_16x16x32_bf16 v[52:55], a[132:135], v[156:159], v[52:55]\n"
"v_exp_f32 v184, v184\n"
"v_mfma_f32_16x16x32_bf16 v[48:51], a[124:127], v[156:159], v[48:51]\n"
"v_exp_f32 v185, v185\n"
"v_mfma_f32_16x16x32_bf16 v[48:51], a[120:123], v[152:155], v[48:51]\n"
"v_exp_f32 v186, v186\n"
"v_smfmac_f32_16x16x64_bf16 a[216:219], a[228:231], v[152:159], v237\n"
"v_exp_f32 v187, v187\n"
"v_mfma_f32_16x16x32_bf16 a[168:171], a[120:123], v[168:171], a[168:171]\n"
"v_cvt_pk_bf16_f32 v174, v184, v185\n"
"v_cvt_pk_bf16_f32 v175, v186, v187\n"
"s_nop 1\n"
"v_mfma_f32_16x16x32_bf16 a[168:171], a[124:127], v[172:175], a[168:171]\n"
"v_mfma_f32_16x16x32_bf16 a[172:175], a[132:135], v[172:175], a[172:175]\n"
"v_mfma_f32_16x16x32_bf16 a[172:175], a[128:131], v[168:171], a[172:175]\n"
"v_mfma_f32_16x16x32_bf16 a[176:179], a[136:139], v[168:171], a[176:179]\n"
"v_mfma_f32_16x16x32_bf16 a[176:179], a[140:143], v[172:175], a[176:179]\n"
"s_waitcnt lgkmcnt(8)\n"
"v_smfmac_f32_16x16x64_bf16 a[220:223], a[228:231], v[168:175], v237\n"
"v_mfma_f32_16x16x32_bf16 v[108:111], a[144:147], v[160:163], v[108:111]\n"
"v_mfma_f32_16x16x32_bf16 v[108:111], a[148:151], v[164:167], v[108:111]\n"
"v_mfma_f32_16x16x32_bf16 v[112:115], a[156:159], v[164:167], v[112:115]\n"
"v_mfma_f32_16x16x32_bf16 v[112:115], a[152:155], v[160:163], v[112:115]\n"
"v_mfma_f32_16x16x32_bf16 v[116:119], a[160:163], v[160:163], v[116:119]\n"
"v_mfma_f32_16x16x32_bf16 v[116:119], a[164:167], v[164:167], v[116:119]\n"
"v_mfma_f32_16x16x32_bf16 v[68:71], a[164:167], v[156:159], v[68:71]\n"
"v_mfma_f32_16x16x32_bf16 v[68:71], a[160:163], v[152:155], v[68:71]\n"
"v_mfma_f32_16x16x32_bf16 v[64:67], a[152:155], v[152:155], v[64:67]\n"
"v_mfma_f32_16x16x32_bf16 v[64:67], a[156:159], v[156:159], v[64:67]\n"
"v_mfma_f32_16x16x32_bf16 v[60:63], a[148:151], v[156:159], v[60:63]\n"
"s_waitcnt vmcnt(9) lgkmcnt(0)\n"
"s_barrier\n"
"buffer_load_dwordx4 v234, s[44:47], s66 offen offset:0 lds\n"
"v_mfma_f32_16x16x32_bf16 v[60:63], a[144:147], v[152:155], v[60:63]\n"
"s_add_u32 m0, s97, 36864\n"
"ds_read_b64_tr_b16 a[112:113], v230 offset:26624\n"
"v_mfma_f32_16x16x32_bf16 a[180:183], a[144:147], v[168:171], a[180:183]\n"
"ds_read_b64_tr_b16 a[114:115], v230 offset:32768\n"
"v_mfma_f32_16x16x32_bf16 a[180:183], a[148:151], v[172:175], a[180:183]\n"
"ds_read_b64_tr_b16 a[116:117], v230 offset:38912\n"
"ds_read_b64_tr_b16 a[118:119], v230 offset:45056\n"
"v_mfma_f32_16x16x32_bf16 a[184:187], a[156:159], v[172:175], a[184:187]\n"
"ds_read_b64_tr_b16 a[144:145], v231 offset:26624\n"
"buffer_load_dwordx4 v234, s[44:47], s77 offen offset:0 lds\n"
"v_mfma_f32_16x16x32_bf16 a[184:187], a[152:155], v[168:171], a[184:187]\n"
"s_add_u32 m0, s97, 25472\n"
"ds_read_b64_tr_b16 a[146:147], v231 offset:32768\n"
"v_mfma_f32_16x16x32_bf16 a[188:191], a[160:163], v[168:171], a[188:191]\n"
"ds_read_b64_tr_b16 a[148:149], v231 offset:38912\n"
"v_mfma_f32_16x16x32_bf16 a[188:191], a[164:167], v[172:175], a[188:191]\n"
"ds_read_b64_tr_b16 a[150:151], v231 offset:45056\n"
"v_mfma_f32_16x16x32_bf16 v[120:123], a[96:99], v[160:163], v[120:123]\n"
"ds_read_b64_tr_b16 a[152:153], v232 offset:26624\n"
"buffer_load_dwordx4 v234, s[44:47], s66 offen offset:128 lds\n"
"v_mfma_f32_16x16x32_bf16 v[120:123], a[100:103], v[164:167], v[120:123]\n"
"s_add_u32 m0, s97, 37760\n"
"ds_read_b64_tr_b16 a[154:155], v232 offset:32768\n"
"v_mfma_f32_16x16x32_bf16 v[124:127], a[108:111], v[164:167], v[124:127]\n"
"ds_read_b64_tr_b16 a[156:157], v232 offset:38912\n"
"s_waitcnt lgkmcnt(7)\n"
"v_mfma_f32_16x16x32_bf16 v[124:127], a[104:107], v[160:163], v[124:127]\n"
"ds_read_b64_tr_b16 a[158:159], v232 offset:45056\n"
"v_mfma_f32_16x16x32_bf16 v[128:131], a[112:115], v[160:163], v[128:131]\n"
"ds_read_b64_tr_b16 a[160:161], v233 offset:26624\n"
"buffer_load_dwordx4 v234, s[44:47], s77 offen offset:128 lds\n"
"v_mfma_f32_16x16x32_bf16 v[128:131], a[116:119], v[164:167], v[128:131]\n"
"s_add_u32 m0, s97, 26368\n"
"ds_read_b64_tr_b16 a[162:163], v233 offset:32768\n"
"v_mfma_f32_16x16x32_bf16 v[80:83], a[116:119], v[156:159], v[80:83]\n"
"ds_read_b64_tr_b16 a[164:165], v233 offset:38912\n"
"v_mfma_f32_16x16x32_bf16 v[80:83], a[112:115], v[152:155], v[80:83]\n"
"ds_read_b64_tr_b16 a[166:167], v233 offset:45056\n"
"v_mfma_f32_16x16x32_bf16 v[76:79], a[104:107], v[152:155], v[76:79]\n"
"ds_read_b128 a[120:123], v228 offset:6144\n"
"v_mfma_f32_16x16x32_bf16 v[76:79], a[108:111], v[156:159], v[76:79]\n"
"ds_read_b128 a[124:127], v229 offset:6144\n"
"v_mfma_f32_16x16x32_bf16 v[72:75], a[100:103], v[156:159], v[72:75]\n"
"ds_read_b128 a[128:131], v228 offset:7168\n"
"v_mfma_f32_16x16x32_bf16 v[72:75], a[96:99], v[152:155], v[72:75]\n"
"ds_read_b128 a[132:135], v229 offset:7168\n"
"v_mfma_f32_16x16x32_bf16 a[192:195], a[96:99], v[168:171], a[192:195]\n"
"ds_read_b128 a[136:139], v228 offset:8192\n"
"v_mfma_f32_16x16x32_bf16 a[192:195], a[100:103], v[172:175], a[192:195]\n"
"ds_read_b128 a[140:143], v229 offset:8192\n"
"v_mfma_f32_16x16x32_bf16 a[196:199], a[108:111], v[172:175], a[196:199]\n"
"ds_read_b128 a[96:99], v228 offset:0\n"
"v_mfma_f32_16x16x32_bf16 a[196:199], a[104:107], v[168:171], a[196:199]\n"
"ds_read_b128 a[100:103], v229 offset:0\n"
"v_mfma_f32_16x16x32_bf16 a[200:203], a[112:115], v[168:171], a[200:203]\n"
"s_waitcnt lgkmcnt(8)\n"
"ds_read_b128 a[104:107], v228 offset:1024\n"
"v_mfma_f32_16x16x32_bf16 a[200:203], a[116:119], v[172:175], a[200:203]\n"
"ds_read_b128 a[108:111], v229 offset:1024\n"
"ds_read_b128 a[112:115], v228 offset:2048\n"
"v_mfma_f32_16x16x32_bf16 v[132:135], a[144:147], v[160:163], v[132:135]\n"
"ds_read_b128 a[116:119], v229 offset:2048\n"
"v_mfma_f32_16x16x32_bf16 v[132:135], a[148:151], v[164:167], v[132:135]\n"
"v_mfma_f32_16x16x32_bf16 v[136:139], a[156:159], v[164:167], v[136:139]\n"
"v_mfma_f32_16x16x32_bf16 v[136:139], a[152:155], v[160:163], v[136:139]\n"
"v_mfma_f32_16x16x32_bf16 v[140:143], a[160:163], v[160:163], v[140:143]\n"
"v_mfma_f32_16x16x32_bf16 v[140:143], a[164:167], v[164:167], v[140:143]\n"
"v_mfma_f32_16x16x32_bf16 v[92:95], a[164:167], v[156:159], v[92:95]\n"
"v_mfma_f32_16x16x32_bf16 v[92:95], a[160:163], v[152:155], v[92:95]\n"
"v_mfma_f32_16x16x32_bf16 v[88:91], a[152:155], v[152:155], v[88:91]\n"
"v_mfma_f32_16x16x32_bf16 v[88:91], a[156:159], v[156:159], v[88:91]\n"
"s_waitcnt vmcnt(6) lgkmcnt(0)\n"
"s_barrier\n"
"v_mfma_f32_16x16x32_bf16 v[84:87], a[148:151], v[156:159], v[84:87]\n"
"buffer_load_dwordx4 v234, s[44:47], s66 offen offset:256 lds\n"
"v_mfma_f32_16x16x32_bf16 v[84:87], a[144:147], v[152:155], v[84:87]\n"
"s_add_u32 m0, s97, 38656\n"
"s_mul_i32 s66, s67, 24576\n"
"v_mfma_f32_16x16x32_bf16 a[204:207], a[144:147], v[168:171], a[204:207]\n"
"v_mfma_f32_16x16x32_bf16 a[204:207], a[148:151], v[172:175], a[204:207]\n"
"v_mfma_f32_16x16x32_bf16 a[208:211], a[156:159], v[172:175], a[208:211]\n"
"v_mfma_f32_16x16x32_bf16 a[208:211], a[152:155], v[168:171], a[208:211]\n"
"buffer_load_dwordx4 v234, s[44:47], s77 offen offset:256 lds\n"
"v_mfma_f32_16x16x32_bf16 a[212:215], a[160:163], v[168:171], a[212:215]\n"
"v_mfma_f32_16x16x32_bf16 a[212:215], a[164:167], v[172:175], a[212:215]\n"
"s_setpc_b64 s[62:63]\n"
".p2align 12\n"
".p2align 5\n"
"L_W0D2:\n"
"s_nop 7\n"
"s_branch L_TABDONE\n"
"L_W2D2:\n"
"s_nop 7\n"
"s_branch L_TABDONE\n"
"L_D2:\n"
"v_readlane_b32 s61, v235, s60\n"
"s_add_u32 s60, s60, 1\n"
"s_and_b32 s98, s61, 0x3ffff\n"
"s_add_u32 s62, s64, s98\n"
"s_addc_u32 s63, s65, 0\n"
"s_add_u32 m0, s97, 0\n"
"v_mfma_f32_16x16x32_bf16 v[168:171], a[96:99], a[72:75], v[200:203]\n"
"s_add_u32 s77, s66, 12288\n"
"buffer_load_dwordx4 v234, s[40:43], s66 offen offset:0 lds\n"
"v_mfma_f32_16x16x32_bf16 v[168:171], a[100:103], a[76:79], v[168:171]\n"
"s_add_u32 m0, s97, 896\n"
"ds_read_b128 a[24:27], v238 offset:6144\n"
"v_add_f32 v160, 0x71000000, v236\n"
"v_mfma_f32_16x16x32_bf16 v[168:171], a[104:107], a[80:83], v[168:171]\n"
"ds_read_b128 a[28:31], v238 offset:9216\n"
"v_add_f32 v161, 0xf1000000, v236\n"
"v_mfma_f32_16x16x32_bf16 v[168:171], a[108:111], a[84:87], v[168:171]\n"
"s_nop 0\n"
"ds_read_b128 a[144:147], v228 offset:12288\n"
"v_accvgpr_read_b32 v254, a218\n"
"v_mfma_f32_16x16x32_bf16 v[168:171], a[112:115], a[88:91], v[168:171]\n"
"s_add_u32 s37, s89, 24576\n"
"ds_read_b128 a[148:151], v229 offset:12288\n"
"v_add_f32 v245, v254, v245\n"
"v_mfma_f32_16x16x32_bf16 v[168:171], a[116:119], a[92:95], v[168:171]\n"
"buffer_load_dwordx4 v234, s[40:43], s66 offen offset:128 lds\n"
"v_mfma_f32_16x16x32_bf16 v[176:179], a[120:123], a[72:75], v[200:203]\n"
"s_add_u32 m0, s97, 1792\n"
"ds_read_b128 a[152:155], v228 offset:13312\n"
"v_accvgpr_read_b32 v254, a219\n"
"v_mfma_f32_16x16x32_bf16 v[176:179], a[124:127], a[76:79], v[176:179]\n"
"ds_read_b128 a[156:159], v229 offset:13312\n"
"v_add_f32 v254, v254, v245\n"
"v_mfma_f32_16x16x32_bf16 v[176:179], a[128:131], a[80:83], v[176:179]\n"
"ds_read_b128 a[160:163], v228 offset:14336\n"
"v_cmp_nle_f32 vcc, 0x1f800000, v254\n"
"v_mfma_f32_16x16x32_bf16 v[176:179], a[132:135], a[84:87], v[176:179]\n"
"s_or_b64 s[86:87], s[86:87], vcc\n"
"ds_read_b128 a[164:167], v229 offset:14336\n"
"v_cmp_nge_f32 vcc, 0x67800000, v254\n"
"v_mfma_f32_16x16x32_bf16 v[176:179], a[136:139], a[88:91], v[176:179]\n"
"buffer_load_dwordx4 v234, s[40:43], s66 offen offset:256 lds\n"
"v_mfma_f32_16x16x32_bf16 v[176:179], a[140:143], a[92:95], v[176:179]\n"
"s_waitcnt lgkmcnt(6)\n"
"ds_read_b128 a[32:35], v238 offset:7168\n"
"ds_read_b128 a[36:39], v238 offset:10240\n"
"v_rcp_f32 v245, v254\n"
"v_add_f32 v162, 0xf1c00000, v236\n"
"v_mul_f32 v254, s81, v245\n"
"v_pk_mul_f32 v[48:49], v[48:49], v[254:255] op_sel_hi:[1,0]\n"
"v_pk_mul_f32 v[50:51], v[50:51], v[254:255] op_sel_hi:[1,0]\n"
"v_pk_mul_f32 v[52:53], v[52:53], v[254:255] op_sel_hi:[1,0]\n"
"v_pk_mul_f32 v[54:55], v[54:55], v[254:255] op_sel_hi:[1,0]\n"
"v_mfma_f32_16x16x32_bf16 v[48:51], v[250:253], a[24:27], v[48:51]\n"
"s_add_u32 m0, s97, 12288\n"
"ds_read_b128 a[40:43], v238 offset:8192\n"
"v_add_f32 v163, 0xf2200000, v236\n"
"v_mfma_f32_16x16x32_bf16 v[52:55], v[248:251], a[24:27], v[52:55]\n"
"ds_read_b128 a[44:47], v238 offset:11264\n"
"v_min_f32 v160, v160, v196\n"
"v_min_f32 v161, v161, v197\n"
"v_min_f32 v162, v162, v198\n"
"v_min_f32 v163, v163, v199\n"
"v_pk_mul_f32 v[56:57], v[56:57], v[254:255] op_sel_hi:[1,0]\n"
"v_pk_mul_f32 v[58:59], v[58:59], v[254:255] op_sel_hi:[1,0]\n"
"v_pk_mul_f32 v[60:61], v[60:61], v[254:255] op_sel_hi:[1,0]\n"
"v_pk_mul_f32 v[62:63], v[62:63], v[254:255] op_sel_hi:[1,0]\n"
"v_mfma_f32_16x16x32_bf16 v[56:59], v[250:253], a[28:31], v[56:59]\n"
"s_lshr_b32 s67, s61, 18\n"
"v_cvt_pk_bf16_f32 v48, v48, v49\n"
"v_cvt_pk_bf16_f32 v49, v50, v51\n"
"v_mfma_f32_16x16x32_bf16 v[60:63], v[248:251], a[28:31], v[60:63]\n"
"s_or_b64 s[86:87], s[86:87], vcc\n"
"v_cvt_pk_bf16_f32 v52, v52, v53\n"
"v_cvt_pk_bf16_f32 v53, v54, v55\n"
"v_mfma_f32_16x16x32_bf16 v[160:163], a[96:99], a[48:51], v[160:163]\n"
"ds_read_b128 a[96:99], v228 offset:18432\n"
"v_cvt_pk_bf16_f32 v56, v56, v57\n"
"v_mfma_f32_16x16x32_bf16 v[160:163], a[100:103], a[52:55], v[160:163]\n"
"s_waitcnt lgkmcnt(3)\n"
"ds_read_b128 a[100:103], v229 offset:18432\n"
"v_cvt_pk_bf16_f32 v57, v58, v59\n"
"v_mfma_f32_16x16x32_bf16 v[160:163], a[104:107], a[56:59], v[160:163]\n"
"ds_read_b128 a[104:107], v228 offset:19456\n"
"v_cvt_pk_bf16_f32 v60, v60, v61\n"
"v_mfma_f32_16x16x32_bf16 v[160:163], a[108:111], a[60:63], v[160:163]\n"
"ds_write_b64 v240, v[48:49] offset:6144\n"
"v_mfma_f32_16x16x32_bf16 v[160:163], a[112:115], a[64:67], v[160:163]\n"
"s_waitcnt lgkmcnt(4)\n"
"v_cvt_pk_bf16_f32 v61, v62, v63\n"
"v_add_f32 v180, 0xf3780000, v236\n"
"v_mfma_f32_16x16x32_bf16 v[160:163], a[116:119], a[68:71], v[160:163]\n"
"ds_write_b64 v241, v[52:53] offset:6144\n"
"v_add_f32 v181, 0xf3840000, v236\n"
"v_add_f32 v182, 0xf38c0000, v236\n"
"v_add_f32 v183, 0xf3940000, v236\n"
"v_min_f32 v180, v180, v196\n"
"v_min_f32 v181, v181, v197\n"
"v_min_f32 v182, v182, v198\n"
"v_min_f32 v183, v183, v199\n"
"v_pk_mul_f32 v[64:65], v[64:65], v[254:255] op_sel_hi:[1,0]\n"
"v_pk_mul_f32 v[66:67], v[66:67], v[254:255] op_sel_hi:[1,0]\n"
"v_mfma_f32_16x16x32_bf16 v[180:183], a[120:123], a[48:51], v[180:183]\n"
"ds_write_b64 v242, v[56:57] offset:6144\n"
"v_mfma_f32_16x16x32_bf16 v[180:183], a[124:127], a[52:55], v[180:183]\n"
"v_add_f32 v164, 0xf3fc0000, v236\n"
"v_add_f32 v165, 0xf4020000, v236\n"
"v_mfma_f32_16x16x32_bf16 v[180:183], a[128:131], a[56:59], v[180:183]\n"
"ds_write_b64 v243, v[60:61] offset:6144\n"
"v_mfma_f32_16x16x32_bf16 v[180:183], a[132:135], a[60:63], v[180:183]\n"
"v_add_f32 v166, 0xf4060000, v236\n"
"v_add_f32 v167, 0xf40a0000, v236\n"
"v_mfma_f32_16x16x32_bf16 v[180:183], a[136:139], a[64:67], v[180:183]\n"
"ds_read_b128 v[48:51], v238 offset:6144\n"
"ds_read_b128 v[52:55], v238 offset:9216\n"
"v_mfma_f32_16x16x32_bf16 v[180:183], a[140:143], a[68:71], v[180:183]\n"
"ds_read_b128 a[108:111], v229 offset:19456\n"
"v_mfma_f32_16x16x32_bf16 v[64:67], v[250:253], a[32:35], v[64:67]\n"
"ds_read_b128 a[112:115], v228 offset:20480\n"
"v_min_f32 v164, v164, v196\n"
"v_min_f32 v165, v165, v197\n"
"v_min_f32 v166, v166, v198\n"
"v_pk_mul_f32 v[68:69], v[68:69], v[254:255] op_sel_hi:[1,0]\n"
"v_pk_mul_f32 v[70:71], v[70:71], v[254:255] op_sel_hi:[1,0]\n"
"v_pk_mul_f32 v[72:73], v[72:73], v[254:255] op_sel_hi:[1,0]\n"
"v_pk_mul_f32 v[74:75], v[74:75], v[254:255] op_sel_hi:[1,0]\n"
"v_mfma_f32_16x16x32_bf16 v[68:71], v[248:251], a[32:35], v[68:71]\n"
"ds_read_b128 a[116:119], v229 offset:20480\n"
"v_cvt_pk_bf16_f32 v64, v64, v65\n"
"v_mfma_f32_16x16x32_bf16 v[72:75], v[250:253], a[36:39], v[72:75]\n"
"v_cvt_pk_bf16_f32 v65, v66, v67\n"
"v_min_f32 v167, v167, v199\n"
"v_exp_f32 v168, v168\n"
"v_exp_f32 v169, v169\n"
"v_pk_mul_f32 v[76:77], v[76:77], v[254:255] op_sel_hi:[1,0]\n"
"v_pk_mul_f32 v[78:79], v[78:79], v[254:255] op_sel_hi:[1,0]\n"
"v_cvt_pk_bf16_f32 v68, v68, v69\n"
"v_cvt_pk_bf16_f32 v69, v70, v71\n"
"v_mfma_f32_16x16x32_bf16 v[76:79], v[248:251], a[36:39], v[76:79]\n"
"ds_write_b64 v240, v[64:65] offset:7168\n"
"s_waitcnt lgkmcnt(4)\n"
"buffer_store_dwordx4 v[48:51], v239, s[52:55], s37 offen offset:0 sc1\n"
"v_mfma_f32_16x16x32_bf16 v[172:175], a[144:147], a[72:75], v[200:203]\n"
"ds_write_b64 v241, v[68:69] offset:7168\n"
"v_cvt_pk_bf16_f32 v72, v72, v73\n"
"v_cvt_pk_bf16_f32 v73, v74, v75\n"
"v_mfma_f32_16x16x32_bf16 v[172:175], a[148:151], a[76:79], v[172:175]\n"
"ds_write_b64 v242, v[72:73] offset:7168\n"
"buffer_store_dwordx4 v[52:55], v239, s[52:55], s37 offen offset:3072 sc1\n"
"v_cvt_pk_bf16_f32 v76, v76, v77\n"
"v_cvt_pk_bf16_f32 v77, v78, v79\n"
"v_mfma_f32_16x16x32_bf16 v[172:175], a[152:155], a[80:83], v[172:175]\n"
"ds_write_b64 v243, v[76:77] offset:7168\n"
"v_mfma_f32_16x16x32_bf16 v[172:175], a[156:159], a[84:87], v[172:175]\n"
"v_exp_f32 v170, v170\n"
"v_mfma_f32_16x16x32_bf16 v[172:175], a[160:163], a[88:91], v[172:175]\n"
"s_waitcnt vmcnt(7) lgkmcnt(4)\n"
"s_barrier\n"
"ds_read_b128 v[64:67], v238 offset:7168\n"
"buffer_load_dwordx4 v234, s[40:43], s77 offen offset:0 lds\n"
"ds_read_b128 v[68:71], v238 offset:10240\n"
"s_add_u32 m0, s97, 13184\n"
"v_mfma_f32_16x16x32_bf16 v[172:175], a[164:167], a[92:95], v[172:175]\n"
"ds_read_b64_tr_b16 a[120:121], v230 offset:24576\n"
"v_cvt_pk_bf16_f32 v168, v168, v169\n"
"v_mfma_f32_16x16x32_bf16 v[184:187], a[96:99], a[72:75], v[200:203]\n"
"ds_read_b64_tr_b16 a[122:123], v230 offset:30720\n"
"v_mfma_f32_16x16x32_bf16 v[184:187], a[100:103], a[76:79], v[184:187]\n"
"ds_read_b64_tr_b16 a[124:125], v230 offset:36864\n"
"v_mfma_f32_16x16x32_bf16 v[184:187], a[104:107], a[80:83], v[184:187]\n"
"ds_read_b64_tr_b16 a[126:127], v230 offset:43008\n"
"buffer_load_dwordx4 v234, s[40:43], s77 offen offset:128 lds\n"
"v_mfma_f32_16x16x32_bf16 v[184:187], a[108:111], a[84:87], v[184:187]\n"
"s_add_u32 m0, s97, 14080\n"
"ds_read_b64_tr_b16 a[128:129], v231 offset:24576\n"
"v_mfma_f32_16x16x32_bf16 v[184:187], a[112:115], a[88:91], v[184:187]\n"
"ds_read_b64_tr_b16 a[130:131], v231 offset:30720\n"
"v_mfma_f32_16x16x32_bf16 v[184:187], a[116:119], a[92:95], v[184:187]\n"
"ds_read_b64_tr_b16 a[132:133], v231 offset:36864\n"
"ds_read_b64_tr_b16 a[134:135], v231 offset:43008\n"
"ds_read_b64_tr_b16 a[136:137], v232 offset:24576\n"
"ds_read_b64_tr_b16 a[138:139], v232 offset:30720\n"
"ds_read_b64_tr_b16 a[140:141], v232 offset:36864\n"
"buffer_load_dwordx4 v234, s[40:43], s77 offen offset:256 lds\n"
"ds_read_b64_tr_b16 a[142:143], v232 offset:43008\n"
"s_waitcnt lgkmcnt(12)\n"
"v_pk_mul_f32 v[80:81], v[80:81], v[254:255] op_sel_hi:[1,0]\n"
"v_pk_mul_f32 v[82:83], v[82:83], v[254:255] op_sel_hi:[1,0]\n"
"v_pk_mul_f32 v[84:85], v[84:85], v[254:255] op_sel_hi:[1,0]\n"
"v_pk_mul_f32 v[86:87], v[86:87], v[254:255] op_sel_hi:[1,0]\n"
"v_mfma_f32_16x16x32_bf16 v[80:83], v[250:253], a[40:43], v[80:83]\n"
"s_add_u32 m0, s97, 24576\n"
"v_exp_f32 v171, v171\n"
"v_mfma_f32_16x16x32_bf16 v[84:87], v[248:251], a[40:43], v[84:87]\n"
"v_exp_f32 v176, v176\n"
"v_exp_f32 v177, v177\n"
"v_exp_f32 v178, v178\n"
"v_pk_mul_f32 v[88:89], v[88:89], v[254:255] op_sel_hi:[1,0]\n"
"v_pk_mul_f32 v[90:91], v[90:91], v[254:255] op_sel_hi:[1,0]\n"
"v_pk_mul_f32 v[92:93], v[92:93], v[254:255] op_sel_hi:[1,0]\n"
"v_pk_mul_f32 v[94:95], v[94:95], v[254:255] op_sel_hi:[1,0]\n"
"v_mfma_f32_16x16x32_bf16 v[88:91], v[250:253], a[44:47], v[88:91]\n"
"buffer_store_dwordx4 v[64:67], v239, s[52:55], s37 offen offset:128 sc1\n"
"v_mfma_f32_16x16x32_bf16 v[92:95], v[248:251], a[44:47], v[92:95]\n"
"v_exp_f32 v179, v179\n"
"v_mfma_f32_16x16x32_bf16 v[164:167], a[144:147], a[48:51], v[164:167]\n"
"ds_read_b64_tr_b16 a[144:145], v233 offset:24576\n"
"v_cvt_pk_bf16_f32 v169, v170, v171\n"
"v_mfma_f32_16x16x32_bf16 v[164:167], a[148:151], a[52:55], v[164:167]\n"
"ds_read_b64_tr_b16 a[146:147], v233 offset:30720\n"
"v_cvt_pk_bf16_f32 v170, v176, v177\n"
"v_mfma_f32_16x16x32_bf16 v[164:167], a[152:155], a[56:59], v[164:167]\n"
"ds_read_b64_tr_b16 a[148:149], v233 offset:36864\n"
"buffer_store_dwordx4 v[68:71], v239, s[52:55], s37 offen offset:3200 sc1\n"
"v_mfma_f32_16x16x32_bf16 v[164:167], a[156:159], a[60:63], v[164:167]\n"
"ds_read_b64_tr_b16 a[150:151], v233 offset:43008\n"
"v_cvt_pk_bf16_f32 v171, v178, v179\n"
"v_mfma_f32_16x16x32_bf16 v[164:167], a[160:163], a[64:67], v[164:167]\n"
"ds_read_b64_tr_b16 a[152:153], v230 offset:25600\n"
"v_add_f32 v176, 0xf43e0000, v236\n"
"v_mfma_f32_16x16x32_bf16 v[164:167], a[164:167], a[68:71], v[164:167]\n"
"ds_read_b64_tr_b16 a[154:155], v230 offset:31744\n"
"ds_read_b64_tr_b16 a[156:157], v230 offset:37888\n"
"ds_read_b64_tr_b16 a[158:159], v230 offset:44032\n"
"ds_read_b64_tr_b16 a[160:161], v231 offset:25600\n"
"ds_read_b64_tr_b16 a[162:163], v231 offset:31744\n"
"ds_read_b64_tr_b16 a[164:165], v231 offset:37888\n"
"ds_read_b64_tr_b16 a[166:167], v231 offset:44032\n"
"v_add_f32 v177, 0xf4420000, v236\n"
"v_add_f32 v178, 0xf4460000, v236\n"
"v_add_f32 v179, 0xf44a0000, v236\n"
"v_min_f32 v176, v176, v196\n"
"v_min_f32 v177, v177, v197\n"
"v_min_f32 v178, v178, v198\n"
"v_min_f32 v179, v179, v199\n"
"v_cvt_pk_bf16_f32 v80, v80, v81\n"
"v_cvt_pk_bf16_f32 v81, v82, v83\n"
"v_mfma_f32_16x16x32_bf16 v[176:179], a[96:99], a[48:51], v[176:179]\n"
"ds_read_b64_tr_b16 a[96:97], v232 offset:25600\n"
"v_cvt_pk_bf16_f32 v84, v84, v85\n"
"v_mfma_f32_16x16x32_bf16 v[176:179], a[100:103], a[52:55], v[176:179]\n"
"ds_read_b64_tr_b16 a[98:99], v232 offset:31744\n"
"v_cvt_pk_bf16_f32 v85, v86, v87\n"
"v_mfma_f32_16x16x32_bf16 v[176:179], a[104:107], a[56:59], v[176:179]\n"
"ds_read_b64_tr_b16 a[100:101], v232 offset:37888\n"
"v_cvt_pk_bf16_f32 v88, v88, v89\n"
"v_mfma_f32_16x16x32_bf16 v[176:179], a[108:111], a[60:63], v[176:179]\n"
"ds_read_b64_tr_b16 a[102:103], v232 offset:44032\n"
"ds_read_b64_tr_b16 a[104:105], v233 offset:25600\n"
"ds_read_b64_tr_b16 a[106:107], v233 offset:31744\n"
"ds_read_b64_tr_b16 a[108:109], v233 offset:37888\n"
"ds_read_b64_tr_b16 a[110:111], v233 offset:44032\n"
"ds_write_b64 v240, v[80:81] offset:8192\n"
"v_cvt_pk_bf16_f32 v89, v90, v91\n"
"v_cvt_pk_bf16_f32 v92, v92, v93\n"
"v_cvt_pk_bf16_f32 v93, v94, v95\n"
"v_mfma_f32_16x16x32_bf16 v[176:179], a[112:115], a[64:67], v[176:179]\n"
"s_waitcnt lgkmcnt(15)\n"
"ds_write_b64 v241, v[84:85] offset:8192\n"
"v_mfma_f32_16x16x32_bf16 v[176:179], a[116:119], a[68:71], v[176:179]\n"
"v_exp_f32 v172, v172\n"
"v_exp_f32 v173, v173\n"
"v_mfma_f32_16x16x32_bf16 a[168:171], a[120:123], v[168:171], a[168:171]\n"
"ds_write_b64 v242, v[88:89] offset:8192\n"
"v_exp_f32 v174, v174\n"
"v_exp_f32 v175, v175\n"
"v_cvt_pk_bf16_f32 v172, v172, v173\n"
"v_exp_f32 v184, v184\n"
"v_exp_f32 v185, v185\n"
"v_exp_f32 v186, v186\n"
"v_exp_f32 v187, v187\n"
"v_cvt_pk_bf16_f32 v173, v174, v175\n"
"v_cvt_pk_bf16_f32 v174, v184, v185\n"
"v_cvt_pk_bf16_f32 v175, v186, v187\n"
"v_exp_f32 v164, v164\n"
"v_exp_f32 v165, v165\n"
"v_mfma_f32_16x16x32_bf16 a[168:171], a[124:127], v[172:175], a[168:171]\n"
"ds_write_b64 v243, v[92:93] offset:8192\n"
"v_mfma_f32_16x16x32_bf16 a[172:175], a[132:135], v[172:175], a[172:175]\n"
"v_exp_f32 v166, v166\n"
"v_mfma_f32_16x16x32_bf16 a[172:175], a[128:131], v[168:171], a[172:175]\n"
"ds_read_b128 v[80:83], v238 offset:8192\n"
"ds_read_b128 v[84:87], v238 offset:11264\n"
"v_mfma_f32_16x16x32_bf16 a[176:179], a[136:139], v[168:171], a[176:179]\n"
"v_exp_f32 v167, v167\n"
"v_mfma_f32_16x16x32_bf16 a[176:179], a[140:143], v[172:175], a[176:179]\n"
"v_cvt_pk_bf16_f32 v164, v164, v165\n"
"v_cvt_pk_bf16_f32 v165, v166, v167\n"
"v_smfmac_f32_16x16x64_bf16 a[220:223], a[228:231], v[168:175], v237\n"
"v_exp_f32 v176, v176\n"
"v_exp_f32 v177, v177\n"
"v_exp_f32 v178, v178\n"
"v_exp_f32 v179, v179\n"
"v_exp_f32 v160, v160\n"
"v_exp_f32 v161, v161\n"
"v_cvt_pk_bf16_f32 v166, v176, v177\n"
"v_cvt_pk_bf16_f32 v167, v178, v179\n"
"v_exp_f32 v162, v162\n"
"v_exp_f32 v163, v163\n"
"v_mfma_f32_16x16x32_bf16 v[104:107], a[140:143], v[164:167], v[104:107]\n"
"v_cvt_pk_bf16_f32 v160, v160, v161\n"
"v_exp_f32 v180, v180\n"
"v_exp_f32 v181, v181\n"
"s_waitcnt lgkmcnt(0)\n"
"buffer_store_dwordx4 v[80:83], v239, s[52:55], s37 offen offset:256 sc1\n"
"v_exp_f32 v182, v182\n"
"v_exp_f32 v183, v183\n"
"v_cvt_pk_bf16_f32 v161, v162, v163\n"
"v_cvt_pk_bf16_f32 v162, v180, v181\n"
"v_cvt_pk_bf16_f32 v163, v182, v183\n"
"buffer_store_dwordx4 v[84:87], v239, s[52:55], s37 offen offset:3328 sc1\n"
"s_nop 0\n"
"v_mfma_f32_16x16x32_bf16 v[104:107], a[136:139], v[160:163], v[104:107]\n"
"v_mfma_f32_16x16x32_bf16 v[100:103], a[128:131], v[160:163], v[100:103]\n"
"v_mfma_f32_16x16x32_bf16 v[100:103], a[132:135], v[164:167], v[100:103]\n"
"v_mfma_f32_16x16x32_bf16 v[96:99], a[124:127], v[164:167], v[96:99]\n"
"v_mfma_f32_16x16x32_bf16 v[96:99], a[120:123], v[160:163], v[96:99]\n"
"v_smfmac_f32_16x16x64_bf16 a[220:223], a[224:227], v[160:167], v237\n"
"v_mfma_f32_16x16x32_bf16 a[180:183], a[144:147], v[168:171], a[180:183]\n"
"v_mfma_f32_16x16x32_bf16 a[180:183], a[148:151], v[172:175], a[180:183]\n"
"v_mfma_f32_16x16x32_bf16 a[184:187], a[156:159], v[172:175], a[184:187]\n"
"v_mfma_f32_16x16x32_bf16 a[184:187], a[152:155], v[168:171], a[184:187]\n"
"v_mfma_f32_16x16x32_bf16 a[188:191], a[160:163], v[168:171], a[188:191]\n"
"s_waitcnt vmcnt(9)\n"
"s_barrier\n"
"buffer_load_dwordx4 v234, s[44:47], s66 offen offset:0 lds\n"
"v_mfma_f32_16x16x32_bf16 a[188:191], a[164:167], v[172:175], a[188:191]\n"
"s_add_u32 m0, s97, 36864\n"
"ds_read_b64_tr_b16 a[112:113], v230 offset:26624\n"
"v_mfma_f32_16x16x32_bf16 v[116:119], a[164:167], v[164:167], v[116:119]\n"
"ds_read_b64_tr_b16 a[114:115], v230 offset:32768\n"
"v_mfma_f32_16x16x32_bf16 v[116:119], a[160:163], v[160:163], v[116:119]\n"
"ds_read_b64_tr_b16 a[116:117], v230 offset:38912\n"
"ds_read_b64_tr_b16 a[118:119], v230 offset:45056\n"
"v_mfma_f32_16x16x32_bf16 v[112:115], a[152:155], v[160:163], v[112:115]\n"
"ds_read_b64_tr_b16 a[152:153], v232 offset:26624\n"
"buffer_load_dwordx4 v234, s[44:47], s77 offen offset:0 lds\n"
"v_mfma_f32_16x16x32_bf16 v[112:115], a[156:159], v[164:167], v[112:115]\n"
"s_add_u32 m0, s97, 25472\n"
"ds_read_b64_tr_b16 a[154:155], v232 offset:32768\n"
"v_mfma_f32_16x16x32_bf16 v[108:111], a[148:151], v[164:167], v[108:111]\n"
"ds_read_b64_tr_b16 a[148:149], v231 offset:38912\n"
"v_mfma_f32_16x16x32_bf16 v[108:111], a[144:147], v[160:163], v[108:111]\n"
"ds_read_b64_tr_b16 a[144:145], v231 offset:26624\n"
"v_mfma_f32_16x16x32_bf16 a[192:195], a[96:99], v[168:171], a[192:195]\n"
"ds_read_b64_tr_b16 a[146:147], v231 offset:32768\n"
"buffer_load_dwordx4 v234, s[44:47], s66 offen offset:128 lds\n"
"v_mfma_f32_16x16x32_bf16 a[192:195], a[100:103], v[172:175], a[192:195]\n"
"s_add_u32 m0, s97, 37760\n"
"ds_read_b64_tr_b16 a[150:151], v231 offset:45056\n"
"v_mfma_f32_16x16x32_bf16 a[196:199], a[108:111], v[172:175], a[196:199]\n"
"ds_read_b64_tr_b16 a[156:157], v232 offset:38912\n"
"s_waitcnt lgkmcnt(7)\n"
"v_mfma_f32_16x16x32_bf16 a[196:199], a[104:107], v[168:171], a[196:199]\n"
"ds_read_b64_tr_b16 a[158:159], v232 offset:45056\n"
"v_mfma_f32_16x16x32_bf16 a[200:203], a[112:115], v[168:171], a[200:203]\n"
"ds_read_b64_tr_b16 a[160:161], v233 offset:26624\n"
"buffer_load_dwordx4 v234, s[44:47], s77 offen offset:128 lds\n"
"v_mfma_f32_16x16x32_bf16 a[200:203], a[116:119], v[172:175], a[200:203]\n"
"s_add_u32 m0, s97, 26368\n"
"ds_read_b64_tr_b16 a[162:163], v233 offset:32768\n"
"v_mfma_f32_16x16x32_bf16 v[128:131], a[116:119], v[164:167], v[128:131]\n"
"ds_read_b64_tr_b16 a[164:165], v233 offset:38912\n"
"v_mfma_f32_16x16x32_bf16 v[128:131], a[112:115], v[160:163], v[128:131]\n"
"ds_read_b64_tr_b16 a[166:167], v233 offset:45056\n"
"v_mfma_f32_16x16x32_bf16 v[124:127], a[104:107], v[160:163], v[124:127]\n"
"ds_read_b128 a[120:123], v228 offset:6144\n"
"v_mfma_f32_16x16x32_bf16 v[124:127], a[108:111], v[164:167], v[124:127]\n"
"ds_read_b128 a[124:127], v229 offset:6144\n"
"v_mfma_f32_16x16x32_bf16 v[120:123], a[100:103], v[164:167], v[120:123]\n"
"ds_read_b128 a[128:131], v228 offset:7168\n"
"v_mfma_f32_16x16x32_bf16 v[120:123], a[96:99], v[160:163], v[120:123]\n"
"ds_read_b128 a[132:135], v229 offset:7168\n"
"ds_read_b128 a[136:139], v228 offset:8192\n"
"ds_read_b128 a[140:143], v229 offset:8192\n"
"ds_read_b128 a[96:99], v228 offset:0\n"
"ds_read_b128 a[100:103], v229 offset:0\n"
"ds_read_b128 a[104:107], v228 offset:1024\n"
"s_waitcnt lgkmcnt(9)\n"
"v_mfma_f32_16x16x32_bf16 a[204:207], a[144:147], v[168:171], a[204:207]\n"
"ds_read_b128 a[108:111], v229 offset:1024\n"
"v_mfma_f32_16x16x32_bf16 a[204:207], a[148:151], v[172:175], a[204:207]\n"
"ds_read_b128 a[112:115], v228 offset:2048\n"
"v_mfma_f32_16x16x32_bf16 a[208:211], a[156:159], v[172:175], a[208:211]\n"
"ds_read_b128 a[116:119], v229 offset:2048\n"
"v_mfma_f32_16x16x32_bf16 a[208:211], a[152:155], v[168:171], a[208:211]\n"
"v_mfma_f32_16x16x32_bf16 a[212:215], a[160:163], v[168:171], a[212:215]\n"
"v_mfma_f32_16x16x32_bf16 a[212:215], a[164:167], v[172:175], a[212:215]\n"
"v_mfma_f32_16x16x32_bf16 v[140:143], a[164:167], v[164:167], v[140:143]\n"
"v_mfma_f32_16x16x32_bf16 v[140:143], a[160:163], v[160:163], v[140:143]\n"
"v_mfma_f32_16x16x32_bf16 v[136:139], a[152:155], v[160:163], v[136:139]\n"
"v_mfma_f32_16x16x32_bf16 v[136:139], a[156:159], v[164:167], v[136:139]\n"
"v_mfma_f32_16x16x32_bf16 v[132:135], a[148:151], v[164:167], v[132:135]\n"
"v_mfma_f32_16x16x32_bf16 v[132:135], a[144:147], v[160:163], v[132:135]\n"
"s_waitcnt vmcnt(8) lgkmcnt(0)\n"
"s_barrier\n"
"buffer_load_dwordx4 v234, s[44:47], s66 offen offset:256 lds\n"
"s_add_u32 m0, s97, 38656\n"
"s_mul_i32 s66, s67, 24576\n"
"buffer_load_dwordx4 v234, s[44:47], s77 offen offset:256 lds\n"
"s_setpc_b64 s[62:63]\n"
"L_TAB6:\n"
"s_mov_b32 s92, L_G-L_BASE+3407872\n"
"v_writelane_b32 v235, s92, 0\n"
"s_mov_b32 s92, L_G-L_BASE+3145728\n"
"v_writelane_b32 v235, s92, 1\n"
"s_mov_b32 s92, L_GA-L_BASE+2883584\n"
"v_writelane_b32 v235, s92, 2\n"
"s_mov_b32 s92, L_TS0-L_BASE+2621440\n"
"v_writelane_b32 v235, s92, 3\n"
"s_mov_b32 s92, L_TS1-L_BASE+2359296\n"
"v_writelane_b32 v235, s92, 4\n"
"s_mov_b32 s92, L_TS2-L_BASE+2097152\n"
"v_writelane_b32 v235, s92, 5\n"
"s_mov_b32 s92, L_TS3-L_BASE+1835008\n"
"v_writelane_b32 v235, s92, 6\n"
"s_mov_b32 s92, L_VS-L_BASE+1572864\n"
"v_writelane_b32 v235, s92, 7\n"
"s_mov_b32 s92, L_GA-L_BASE+1310720\n"
"v_writelane_b32 v235, s92, 8\n"
"s_mov_b32 s92, L_N2-L_BASE+1048576\n"
"v_writelane_b32 v235, s92, 9\n"
"s_mov_b32 s92, L_GA-L_BASE+0\n"
"v_writelane_b32 v235, s92, 10\n"
"s_mov_b32 s92, L_N2-L_BASE+262144\n"
"v_writelane_b32 v235, s92, 11\n"
"s_mov_b32 s92, L_ST-L_BASE+524288\n"
"v_writelane_b32 v235, s92, 12\n"
"s_mov_b32 s92, L_N2-L_BASE+786432\n"
"v_writelane_b32 v235, s92, 13\n"
"s_mov_b32 s92, L_POLL-L_BASE+6291456\n"
"v_writelane_b32 v235, s92, 14\n"
"s_mov_b32 s92, L_D0-L_BASE+6553600\n"
"v_writelane_b32 v235, s92, 15\n"
"s_mov_b32 s92, L_D1-L_BASE+6815744\n"
"v_writelane_b32 v235, s92, 16\n"
"s_mov_b32 s92, L_D2-L_BASE+7077888\n"
"v_writelane_b32 v235, s92, 17\n"
"s_mov_b32 s92, L_D3-L_BASE+10485760\n"
"v_writelane_b32 v235, s92, 18\n"
"s_mov_b32 s92, L_E3-L_BASE+0\n"
"v_writelane_b32 v235, s92, 19\n"
"s_mov_b32 s66, 344064\n"
"s_branch L_WARM2\n"
".p2align 12\n"
".p2align 5\n"
"L_W0D3:\n"
"s_nop 7\n"
"s_branch L_TABDONE\n"
"L_W2D3:\n"
"s_nop 7\n"
"s_branch L_TABDONE\n"
"L_D3:\n"
"ds_read_b128 a[144:147], v228 offset:12288\n"
"v_add_f32 v168, 0x71000000, v236\n"
"v_add_f32 v169, 0xf1000000, v236\n"
"v_add_f32 v170, 0xf1c00000, v236\n"
"v_add_f32 v171, 0xf2200000, v236\n"
"v_min_f32 v168, v168, v200\n"
"v_min_f32 v169, v169, v201\n"
"v_min_f32 v170, v170, v202\n"
"v_min_f32 v171, v171, v203\n"
"v_add_f32 v176, 0xf3780000, v236\n"
"v_add_f32 v177, 0xf3840000, v236\n"
"v_mfma_f32_16x16x32_bf16 v[168:171], a[96:99], a[72:75], v[168:171]\n"
"ds_read_b128 a[148:151], v229 offset:12288\n"
"v_add_f32 v178, 0xf38c0000, v236\n"
"v_mfma_f32_16x16x32_bf16 v[168:171], a[100:103], a[76:79], v[168:171]\n"
"ds_read_b128 a[152:155], v228 offset:13312\n"
"v_add_f32 v179, 0xf3940000, v236\n"
"v_mfma_f32_16x16x32_bf16 v[168:171], a[104:107], a[80:83], v[168:171]\n"
"ds_read_b128 a[156:159], v229 offset:13312\n"
"v_min_f32 v176, v176, v200\n"
"v_mfma_f32_16x16x32_bf16 v[168:171], a[108:111], a[84:87], v[168:171]\n"
"s_nop 0\n"
"ds_read_b128 a[160:163], v228 offset:14336\n"
"v_min_f32 v177, v177, v201\n"
"v_mfma_f32_16x16x32_bf16 v[168:171], a[112:115], a[88:91], v[168:171]\n"
"ds_read_b128 a[164:167], v229 offset:14336\n"
"v_min_f32 v178, v178, v202\n"
"v_mfma_f32_16x16x32_bf16 v[168:171], a[116:119], a[92:95], v[168:171]\n"
"ds_read_b128 a[96:99], v228 offset:18432\n"
"v_min_f32 v179, v179, v203\n"
"v_accvgpr_read_b32 v254, a220\n"
"v_readlane_b32 s61, v235, s60\n"
"v_mfma_f32_16x16x32_bf16 v[176:179], a[120:123], a[72:75], v[176:179]\n"
"s_add_u32 s60, s60, 1\n"
"ds_read_b128 a[100:103], v229 offset:18432\n"
"v_add_f32 v246, v254, v246\n"
"v_mfma_f32_16x16x32_bf16 v[176:179], a[124:127], a[76:79], v[176:179]\n"
"s_and_b32 s98, s61, 0x3ffff\n"
"ds_read_b128 a[104:107], v228 offset:19456\n"
"v_accvgpr_read_b32 v254, a221\n"
"v_mfma_f32_16x16x32_bf16 v[176:179], a[128:131], a[80:83], v[176:179]\n"
"s_add_u32 s62, s64, s98\n"
"ds_read_b128 a[108:111], v229 offset:19456\n"
"v_add_f32 v254, v254, v246\n"
"v_mfma_f32_16x16x32_bf16 v[176:179], a[132:135], a[84:87], v[176:179]\n"
"s_addc_u32 s63, s65, 0\n"
"ds_read_b128 a[112:115], v228 offset:20480\n"
"v_cmp_nle_f32 vcc, 0x1f800000, v254\n"
"v_mfma_f32_16x16x32_bf16 v[176:179], a[136:139], a[88:91], v[176:179]\n"
"s_or_b64 s[86:87], s[86:87], vcc\n"
"ds_read_b128 a[116:119], v229 offset:20480\n"
"v_cmp_nge_f32 vcc, 0x67800000, v254\n"
"v_mfma_f32_16x16x32_bf16 v[176:179], a[140:143], a[92:95], v[176:179]\n"
"s_add_u32 s38, s89, 49152\n"
"v_rcp_f32 v246, v254\n"
"v_add_f32 v172, 0xf3fc0000, v236\n"
"v_mul_f32 v254, s81, v246\n"
"v_add_f32 v173, 0xf4020000, v236\n"
"v_pk_mul_f32 v[96:97], v[96:97], v[254:255] op_sel_hi:[1,0]\n"
"v_pk_mul_f32 v[98:99], v[98:99], v[254:255] op_sel_hi:[1,0]\n"
"v_pk_mul_f32 v[100:101], v[100:101], v[254:255] op_sel_hi:[1,0]\n"
"v_pk_mul_f32 v[102:103], v[102:103], v[254:255] op_sel_hi:[1,0]\n"
"v_mfma_f32_16x16x32_bf16 v[96:99], v[250:253], a[232:235], v[96:99]\n"
"s_lshr_b32 s67, s61, 18\n"
"v_add_f32 v174, 0xf4060000, v236\n"
"v_add_f32 v175, 0xf40a0000, v236\n"
"v_mfma_f32_16x16x32_bf16 v[100:103], v[248:251], a[232:235], v[100:103]\n"
"s_or_b64 s[86:87], s[86:87], vcc\n"
"v_min_f32 v172, v172, v200\n"
"v_min_f32 v173, v173, v201\n"
"v_min_f32 v174, v174, v202\n"
"v_min_f32 v175, v175, v203\n"
"v_cvt_pk_bf16_f32 v96, v96, v97\n"
"v_pk_mul_f32 v[104:105], v[104:105], v[254:255] op_sel_hi:[1,0]\n"
"v_pk_mul_f32 v[106:107], v[106:107], v[254:255] op_sel_hi:[1,0]\n"
"v_pk_mul_f32 v[108:109], v[108:109], v[254:255] op_sel_hi:[1,0]\n"
"v_pk_mul_f32 v[110:111], v[110:111], v[254:255] op_sel_hi:[1,0]\n"
"v_mfma_f32_16x16x32_bf16 v[104:107], v[250:253], a[236:239], v[104:107]\n"
"s_mul_i32 s66, s67, 24576\n"
"v_cvt_pk_bf16_f32 v97, v98, v99\n"
"v_cvt_pk_bf16_f32 v100, v100, v101\n"
"v_mfma_f32_16x16x32_bf16 v[108:111], v[248:251], a[236:239], v[108:111]\n"
"ds_write_b64 v240, v[96:97] offset:12288\n"
"v_cvt_pk_bf16_f32 v101, v102, v103\n"
"v_add_f32 v180, 0xf43e0000, v236\n"
"v_pk_mul_f32 v[112:113], v[112:113], v[254:255] op_sel_hi:[1,0]\n"
"v_pk_mul_f32 v[114:115], v[114:115], v[254:255] op_sel_hi:[1,0]\n"
"v_pk_mul_f32 v[116:117], v[116:117], v[254:255] op_sel_hi:[1,0]\n"
"v_pk_mul_f32 v[118:119], v[118:119], v[254:255] op_sel_hi:[1,0]\n"
"v_mfma_f32_16x16x32_bf16 v[112:115], v[250:253], a[240:243], v[112:115]\n"
"ds_write_b64 v241, v[100:101] offset:12288\n"
"v_mfma_f32_16x16x32_bf16 v[116:119], v[248:251], a[240:243], v[116:119]\n"
"v_cvt_pk_bf16_f32 v104, v104, v105\n"
"v_cvt_pk_bf16_f32 v105, v106, v107\n"
"v_cvt_pk_bf16_f32 v108, v108, v109\n"
"v_cvt_pk_bf16_f32 v109, v110, v111\n"
"v_add_f32 v181, 0xf4420000, v236\n"
"v_pk_mul_f32 v[120:121], v[120:121], v[254:255] op_sel_hi:[1,0]\n"
"v_pk_mul_f32 v[122:123], v[122:123], v[254:255] op_sel_hi:[1,0]\n"
"v_pk_mul_f32 v[124:125], v[124:125], v[254:255] op_sel_hi:[1,0]\n"
"v_pk_mul_f32 v[126:127], v[126:127], v[254:255] op_sel_hi:[1,0]\n"
"v_mfma_f32_16x16x32_bf16 v[120:123], v[250:253], a[244:247], v[120:123]\n"
"s_waitcnt lgkmcnt(8)\n"
"ds_write_b64 v242, v[104:105] offset:12288\n"
"v_mfma_f32_16x16x32_bf16 v[124:127], v[248:251], a[244:247], v[124:127]\n"
"v_cvt_pk_bf16_f32 v112, v112, v113\n"
"v_cvt_pk_bf16_f32 v113, v114, v115\n"
"v_cvt_pk_bf16_f32 v116, v116, v117\n"
"v_cvt_pk_bf16_f32 v117, v118, v119\n"
"v_mfma_f32_16x16x32_bf16 v[172:175], a[144:147], a[72:75], v[172:175]\n"
"ds_write_b64 v243, v[108:109] offset:12288\n"
"v_cvt_pk_bf16_f32 v120, v120, v121\n"
"v_cvt_pk_bf16_f32 v121, v122, v123\n"
"v_cvt_pk_bf16_f32 v124, v124, v125\n"
"v_cvt_pk_bf16_f32 v125, v126, v127\n"
"v_mfma_f32_16x16x32_bf16 v[172:175], a[148:151], a[76:79], v[172:175]\n"
"ds_read_b128 v[96:99], v238 offset:12288\n"
"ds_read_b128 v[100:103], v238 offset:15360\n"
"v_mfma_f32_16x16x32_bf16 v[172:175], a[152:155], a[80:83], v[172:175]\n"
"ds_write_b64 v240, v[112:113] offset:13312\n"
"v_mfma_f32_16x16x32_bf16 v[172:175], a[156:159], a[84:87], v[172:175]\n"
"v_add_f32 v182, 0xf4460000, v236\n"
"v_add_f32 v183, 0xf44a0000, v236\n"
"v_mfma_f32_16x16x32_bf16 v[172:175], a[160:163], a[88:91], v[172:175]\n"
"s_waitcnt vmcnt(2) lgkmcnt(7)\n"
"ds_write_b64 v241, v[116:117] offset:13312\n"
"v_mfma_f32_16x16x32_bf16 v[172:175], a[164:167], a[92:95], v[172:175]\n"
"s_barrier\n"
"v_min_f32 v180, v180, v200\n"
"v_min_f32 v181, v181, v201\n"
"v_min_f32 v182, v182, v202\n"
"v_min_f32 v183, v183, v203\n"
"v_exp_f32 v168, v168\n"
"v_pk_mul_f32 v[128:129], v[128:129], v[254:255] op_sel_hi:[1,0]\n"
"v_mfma_f32_16x16x32_bf16 v[180:183], a[96:99], a[72:75], v[180:183]\n"
"ds_write_b64 v242, v[120:121] offset:13312\n"
"v_mfma_f32_16x16x32_bf16 v[180:183], a[100:103], a[76:79], v[180:183]\n"
"v_exp_f32 v169, v169\n"
"v_mfma_f32_16x16x32_bf16 v[180:183], a[104:107], a[80:83], v[180:183]\n"
"s_waitcnt lgkmcnt(3)\n"
"ds_write_b64 v243, v[124:125] offset:13312\n"
"buffer_store_dwordx4 v[96:99], v239, s[52:55], s38 offen offset:0 sc1\n"
"ds_read_b128 v[112:115], v238 offset:13312\n"
"ds_read_b128 v[116:119], v238 offset:16384\n"
"v_mfma_f32_16x16x32_bf16 v[180:183], a[108:111], a[84:87], v[180:183]\n"
"ds_read_b64_tr_b16 a[120:121], v230 offset:24576\n"
"v_mfma_f32_16x16x32_bf16 v[180:183], a[112:115], a[88:91], v[180:183]\n"
"ds_read_b64_tr_b16 a[122:123], v230 offset:30720\n"
"v_cvt_pk_bf16_f32 v168, v168, v169\n"
"v_mfma_f32_16x16x32_bf16 v[180:183], a[116:119], a[92:95], v[180:183]\n"
"ds_read_b64_tr_b16 a[124:125], v230 offset:36864\n"
"buffer_store_dwordx4 v[100:103], v239, s[52:55], s38 offen offset:3072 sc1\n"
"ds_read_b64_tr_b16 a[126:127], v230 offset:43008\n"
"ds_read_b64_tr_b16 a[128:129], v231 offset:24576\n"
"ds_read_b64_tr_b16 a[130:131], v231 offset:30720\n"
"ds_read_b64_tr_b16 a[132:133], v231 offset:36864\n"
"ds_read_b64_tr_b16 a[134:135], v231 offset:43008\n"
"ds_read_b64_tr_b16 a[136:137], v232 offset:24576\n"
"ds_read_b64_tr_b16 a[138:139], v232 offset:30720\n"
"ds_read_b64_tr_b16 a[140:141], v232 offset:36864\n"
"ds_read_b64_tr_b16 a[142:143], v232 offset:43008\n"
"ds_read_b64_tr_b16 a[144:145], v233 offset:24576\n"
"s_waitcnt lgkmcnt(13)\n"
"buffer_store_dwordx4 v[112:115], v239, s[52:55], s38 offen offset:128 sc1\n"
"ds_read_b64_tr_b16 a[146:147], v233 offset:30720\n"
"ds_read_b64_tr_b16 a[148:149], v233 offset:36864\n"
"ds_read_b64_tr_b16 a[150:151], v233 offset:43008\n"
"ds_read_b64_tr_b16 a[152:153], v230 offset:25600\n"
"ds_read_b64_tr_b16 a[154:155], v230 offset:31744\n"
"ds_read_b64_tr_b16 a[156:157], v230 offset:37888\n"
"ds_read_b64_tr_b16 a[158:159], v230 offset:44032\n"
"ds_read_b64_tr_b16 a[160:161], v231 offset:25600\n"
"buffer_store_dwordx4 v[116:119], v239, s[52:55], s38 offen offset:3200 sc1\n"
"ds_read_b64_tr_b16 a[162:163], v231 offset:31744\n"
"ds_read_b64_tr_b16 a[164:165], v231 offset:37888\n"
"ds_read_b64_tr_b16 a[166:167], v231 offset:44032\n"
"v_pk_mul_f32 v[130:131], v[130:131], v[254:255] op_sel_hi:[1,0]\n"
"v_pk_mul_f32 v[132:133], v[132:133], v[254:255] op_sel_hi:[1,0]\n"
"v_pk_mul_f32 v[134:135], v[134:135], v[254:255] op_sel_hi:[1,0]\n"
"v_mfma_f32_16x16x32_bf16 v[128:131], v[250:253], a[248:251], v[128:131]\n"
"ds_read_b64_tr_b16 a[96:97], v232 offset:25600\n"
"v_mfma_f32_16x16x32_bf16 v[132:135], v[248:251], a[248:251], v[132:135]\n"
"ds_read_b64_tr_b16 a[98:99], v232 offset:31744\n"
"ds_read_b64_tr_b16 a[100:101], v232 offset:37888\n"
"ds_read_b64_tr_b16 a[102:103], v232 offset:44032\n"
"ds_read_b64_tr_b16 a[104:105], v233 offset:25600\n"
"ds_read_b64_tr_b16 a[106:107], v233 offset:31744\n"
"ds_read_b64_tr_b16 a[108:109], v233 offset:37888\n"
"ds_read_b64_tr_b16 a[110:111], v233 offset:44032\n"
"v_pk_mul_f32 v[136:137], v[136:137], v[254:255] op_sel_hi:[1,0]\n"
"v_pk_mul_f32 v[138:139], v[138:139], v[254:255] op_sel_hi:[1,0]\n"
"v_pk_mul_f32 v[140:141], v[140:141], v[254:255] op_sel_hi:[1,0]\n"
"v_pk_mul_f32 v[142:143], v[142:143], v[254:255] op_sel_hi:[1,0]\n"
"v_mfma_f32_16x16x32_bf16 v[136:139], v[250:253], a[252:255], v[136:139]\n"
"s_waitcnt lgkmcnt(15)\n"
"ds_read_b128 v[0:3], v238 offset:18432\n"
"v_cvt_pk_bf16_f32 v128, v128, v129\n"
"v_mfma_f32_16x16x32_bf16 v[140:143], v[248:251], a[252:255], v[140:143]\n"
"ds_read_b128 v[4:7], v238 offset:21504\n"
"v_cvt_pk_bf16_f32 v129, v130, v131\n"
"v_cvt_pk_bf16_f32 v132, v132, v133\n"
"v_cvt_pk_bf16_f32 v133, v134, v135\n"
"v_cvt_pk_bf16_f32 v136, v136, v137\n"
"v_cvt_pk_bf16_f32 v137, v138, v139\n"
"v_exp_f32 v170, v170\n"
"v_exp_f32 v171, v171\n"
"v_cvt_pk_bf16_f32 v140, v140, v141\n"
"v_cvt_pk_bf16_f32 v141, v142, v143\n"
"v_exp_f32 v176, v176\n"
"v_exp_f32 v177, v177\n"
"v_exp_f32 v178, v178\n"
"v_exp_f32 v179, v179\n"
"v_cvt_pk_bf16_f32 v169, v170, v171\n"
"v_cvt_pk_bf16_f32 v170, v176, v177\n"
"v_cvt_pk_bf16_f32 v171, v178, v179\n"
"v_exp_f32 v172, v172\n"
"v_exp_f32 v173, v173\n"
"v_mfma_f32_16x16x32_bf16 a[168:171], a[120:123], v[168:171], a[168:171]\n"
"ds_write_b64 v240, v[128:129] offset:14336\n"
"v_exp_f32 v174, v174\n"
"v_exp_f32 v175, v175\n"
"v_cvt_pk_bf16_f32 v172, v172, v173\n"
"v_exp_f32 v180, v180\n"
"v_exp_f32 v181, v181\n"
"v_exp_f32 v182, v182\n"
"v_exp_f32 v183, v183\n"
"v_cvt_pk_bf16_f32 v173, v174, v175\n"
"v_cvt_pk_bf16_f32 v174, v180, v181\n"
"v_cvt_pk_bf16_f32 v175, v182, v183\n"
"ds_write_b64 v241, v[132:133] offset:14336\n"
"ds_write_b64 v242, v[136:137] offset:14336\n"
"v_mfma_f32_16x16x32_bf16 a[168:171], a[124:127], v[172:175], a[168:171]\n"
"ds_write_b64 v243, v[140:141] offset:14336\n"
"v_mfma_f32_16x16x32_bf16 a[172:175], a[132:135], v[172:175], a[172:175]\n"
"ds_read_b128 v[128:131], v238 offset:14336\n"
"v_mfma_f32_16x16x32_bf16 a[172:175], a[128:131], v[168:171], a[172:175]\n"
"ds_read_b128 v[132:135], v238 offset:17408\n"
"v_mfma_f32_16x16x32_bf16 a[176:179], a[136:139], v[168:171], a[176:179]\n"
"ds_read_b128 v[8:11], v238 offset:19456\n"
"v_mfma_f32_16x16x32_bf16 a[176:179], a[140:143], v[172:175], a[176:179]\n"
"s_waitcnt lgkmcnt(15)\n"
"ds_read_b128 v[12:15], v238 offset:22528\n"
"v_smfmac_f32_16x16x64_bf16 a[220:223], a[228:231], v[168:175], v237\n"
"s_waitcnt vmcnt(4) lgkmcnt(10)\n"
"s_barrier\n"
"ds_read_b128 v[16:19], v238 offset:20480\n"
"v_mfma_f32_16x16x32_bf16 a[180:183], a[144:147], v[168:171], a[180:183]\n"
"ds_read_b64_tr_b16 a[112:113], v230 offset:26624\n"
"v_mfma_f32_16x16x32_bf16 a[180:183], a[148:151], v[172:175], a[180:183]\n"
"ds_read_b64_tr_b16 a[114:115], v230 offset:32768\n"
"v_mfma_f32_16x16x32_bf16 a[184:187], a[156:159], v[172:175], a[184:187]\n"
"ds_read_b64_tr_b16 a[116:117], v230 offset:38912\n"
"v_mfma_f32_16x16x32_bf16 a[184:187], a[152:155], v[168:171], a[184:187]\n"
"ds_read_b64_tr_b16 a[118:119], v230 offset:45056\n"
"v_mfma_f32_16x16x32_bf16 a[188:191], a[160:163], v[168:171], a[188:191]\n"
"ds_read_b64_tr_b16 a[144:145], v231 offset:26624\n"
"v_mfma_f32_16x16x32_bf16 a[188:191], a[164:167], v[172:175], a[188:191]\n"
"ds_read_b64_tr_b16 a[146:147], v231 offset:32768\n"
"ds_read_b64_tr_b16 a[148:149], v231 offset:38912\n"
"ds_read_b64_tr_b16 a[150:151], v231 offset:45056\n"
"ds_read_b64_tr_b16 a[152:153], v232 offset:26624\n"
"ds_read_b64_tr_b16 a[154:155], v232 offset:32768\n"
"ds_read_b64_tr_b16 a[156:157], v232 offset:38912\n"
"ds_read_b64_tr_b16 a[158:159], v232 offset:45056\n"
"ds_read_b64_tr_b16 a[160:161], v233 offset:26624\n"
"ds_read_b64_tr_b16 a[162:163], v233 offset:32768\n"
"ds_read_b64_tr_b16 a[164:165], v233 offset:38912\n"
"ds_read_b64_tr_b16 a[166:167], v233 offset:45056\n"
"s_waitcnt lgkmcnt(15)\n"
"buffer_store_dwordx4 v[128:131], v239, s[52:55], s38 offen offset:256 sc1\n"
"v_mfma_f32_16x16x32_bf16 a[192:195], a[96:99], v[168:171], a[192:195]\n"
"s_waitcnt lgkmcnt(12)\n"
"ds_read_b128 v[20:23], v238 offset:23552\n"
"v_mfma_f32_16x16x32_bf16 a[192:195], a[100:103], v[172:175], a[192:195]\n"
"v_mfma_f32_16x16x32_bf16 a[196:199], a[108:111], v[172:175], a[196:199]\n"
"v_mfma_f32_16x16x32_bf16 a[196:199], a[104:107], v[168:171], a[196:199]\n"
"buffer_store_dwordx4 v[132:135], v239, s[52:55], s38 offen offset:3328 sc1\n"
"v_mfma_f32_16x16x32_bf16 a[200:203], a[112:115], v[168:171], a[200:203]\n"
"v_mfma_f32_16x16x32_bf16 a[200:203], a[116:119], v[172:175], a[200:203]\n"
"s_waitcnt lgkmcnt(1)\n"
"v_mfma_f32_16x16x32_bf16 a[204:207], a[144:147], v[168:171], a[204:207]\n"
"v_mfma_f32_16x16x32_bf16 a[204:207], a[148:151], v[172:175], a[204:207]\n"
"v_mfma_f32_16x16x32_bf16 a[208:211], a[156:159], v[172:175], a[208:211]\n"
"s_waitcnt lgkmcnt(0)\n"
"v_mfma_f32_16x16x32_bf16 a[208:211], a[152:155], v[168:171], a[208:211]\n"
"v_mfma_f32_16x16x32_bf16 a[212:215], a[160:163], v[168:171], a[212:215]\n"
"v_mfma_f32_16x16x32_bf16 a[212:215], a[164:167], v[172:175], a[212:215]\n"
"s_setpc_b64 s[62:63]\n"
"L_E3:\n"
"s_nop 6\n"
"v_accvgpr_read_b32 v168, a222\n"
"v_add_f32 v247, v168, v247\n"
"v_accvgpr_read_b32 v168, a223\n"
"v_add_f32 v168, v168, v247\n"
"v_cmp_nle_f32 vcc, 0x1f800000, v168\n"
"v_rcp_f32 v169, v168\n"
"s_or_b64 s[86:87], s[86:87], vcc\n"
"v_cmp_nge_f32 vcc, 0x67800000, v168\n"
"v_mul_f32 v168, s81, v169\n"
"v_accvgpr_read_b32 v24, a168\n"
"v_accvgpr_read_b32 v25, a169\n"
"v_accvgpr_read_b32 v26, a170\n"
"v_accvgpr_read_b32 v27, a171\n"
"v_pk_mul_f32 v[24:25], v[24:25], v[168:169] op_sel_hi:[1,0]\n"
"v_pk_mul_f32 v[26:27], v[26:27], v[168:169] op_sel_hi:[1,0]\n"
"v_accvgpr_read_b32 v28, a172\n"
"v_accvgpr_read_b32 v29, a173\n"
"v_mfma_f32_16x16x32_bf16 v[24:27], v[250:253], v[0:3], v[24:27]\n"
"s_add_u32 s39, s89, 73728\n"
"v_accvgpr_read_b32 v30, a174\n"
"v_accvgpr_read_b32 v31, a175\n"
"v_accvgpr_read_b32 v32, a176\n"
"v_accvgpr_read_b32 v33, a177\n"
"v_accvgpr_read_b32 v34, a178\n"
"v_pk_mul_f32 v[28:29], v[28:29], v[168:169] op_sel_hi:[1,0]\n"
"v_pk_mul_f32 v[30:31], v[30:31], v[168:169] op_sel_hi:[1,0]\n"
"v_accvgpr_read_b32 v35, a179\n"
"v_pk_mul_f32 v[32:33], v[32:33], v[168:169] op_sel_hi:[1,0]\n"
"v_mfma_f32_16x16x32_bf16 v[28:31], v[248:251], v[0:3], v[28:31]\n"
"s_add_u32 s39, s39, s10\n"
"v_cvt_pk_bf16_f32 v24, v24, v25\n"
"v_cvt_pk_bf16_f32 v25, v26, v27\n"
"v_accvgpr_read_b32 v36, a180\n"
"v_accvgpr_read_b32 v37, a181\n"
"v_accvgpr_read_b32 v38, a182\n"
"v_pk_mul_f32 v[34:35], v[34:35], v[168:169] op_sel_hi:[1,0]\n"
"v_accvgpr_read_b32 v39, a183\n"
"v_pk_mul_f32 v[36:37], v[36:37], v[168:169] op_sel_hi:[1,0]\n"
"v_mfma_f32_16x16x32_bf16 v[32:35], v[250:253], v[4:7], v[32:35]\n"
"s_or_b64 s[86:87], s[86:87], vcc\n"
"ds_write_b64 v240, v[24:25] offset:18432\n"
"v_cvt_pk_bf16_f32 v28, v28, v29\n"
"v_cvt_pk_bf16_f32 v29, v30, v31\n"
"v_pk_mul_f32 v[38:39], v[38:39], v[168:169] op_sel_hi:[1,0]\n"
"ds_write_b64 v241, v[28:29] offset:18432\n"
"v_accvgpr_read_b32 v40, a184\n"
"v_mfma_f32_16x16x32_bf16 v[36:39], v[248:251], v[4:7], v[36:39]\n"
"v_cvt_pk_bf16_f32 v32, v32, v33\n"
"v_cvt_pk_bf16_f32 v33, v34, v35\n"
"ds_write_b64 v242, v[32:33] offset:18432\n"
"v_accvgpr_read_b32 v41, a185\n"
"v_accvgpr_read_b32 v42, a186\n"
"v_accvgpr_read_b32 v43, a187\n"
"v_pk_mul_f32 v[40:41], v[40:41], v[168:169] op_sel_hi:[1,0]\n"
"v_pk_mul_f32 v[42:43], v[42:43], v[168:169] op_sel_hi:[1,0]\n"
"v_cvt_pk_bf16_f32 v36, v36, v37\n"
"v_cvt_pk_bf16_f32 v37, v38, v39\n"
"ds_write_b64 v243, v[36:37] offset:18432\n"
"v_mfma_f32_16x16x32_bf16 v[40:43], v[250:253], v[8:11], v[40:43]\n"
"v_accvgpr_read_b32 v44, a188\n"
"ds_read_b128 a[168:171], v238 offset:18432\n"
"ds_read_b128 a[172:175], v238 offset:21504\n"
"v_accvgpr_read_b32 v45, a189\n"
"v_accvgpr_read_b32 v46, a190\n"
"v_accvgpr_read_b32 v47, a191\n"
"v_pk_mul_f32 v[44:45], v[44:45], v[168:169] op_sel_hi:[1,0]\n"
"v_pk_mul_f32 v[46:47], v[46:47], v[168:169] op_sel_hi:[1,0]\n"
"v_accvgpr_read_b32 v48, a192\n"
"v_accvgpr_read_b32 v49, a193\n"
"v_mfma_f32_16x16x32_bf16 v[44:47], v[248:251], v[8:11], v[44:47]\n"
"v_accvgpr_read_b32 v50, a194\n"
"v_accvgpr_read_b32 v51, a195\n"
"v_cvt_pk_bf16_f32 v40, v40, v41\n"
"v_cvt_pk_bf16_f32 v41, v42, v43\n"
"v_accvgpr_read_b32 v52, a196\n"
"v_pk_mul_f32 v[48:49], v[48:49], v[168:169] op_sel_hi:[1,0]\n"
"v_pk_mul_f32 v[50:51], v[50:51], v[168:169] op_sel_hi:[1,0]\n"
"v_accvgpr_read_b32 v53, a197\n"
"v_accvgpr_read_b32 v54, a198\n"
"v_mfma_f32_16x16x32_bf16 v[48:51], v[250:253], v[12:15], v[48:51]\n"
"ds_write_b64 v240, v[40:41] offset:19456\n"
"v_accvgpr_read_b32 v55, a199\n"
"v_cvt_pk_bf16_f32 v44, v44, v45\n"
"v_pk_mul_f32 v[52:53], v[52:53], v[168:169] op_sel_hi:[1,0]\n"
"v_pk_mul_f32 v[54:55], v[54:55], v[168:169] op_sel_hi:[1,0]\n"
"v_cvt_pk_bf16_f32 v45, v46, v47\n"
"v_accvgpr_read_b32 v56, a200\n"
"v_mfma_f32_16x16x32_bf16 v[52:55], v[248:251], v[12:15], v[52:55]\n"
"ds_write_b64 v241, v[44:45] offset:19456\n"
"v_accvgpr_read_b32 v57, a201\n"
"v_accvgpr_read_b32 v58, a202\n"
"v_accvgpr_read_b32 v59, a203\n"
"v_pk_mul_f32 v[56:57], v[56:57], v[168:169] op_sel_hi:[1,0]\n"
"v_pk_mul_f32 v[58:59], v[58:59], v[168:169] op_sel_hi:[1,0]\n"
"v_cvt_pk_bf16_f32 v48, v48, v49\n"
"v_cvt_pk_bf16_f32 v49, v50, v51\n"
"v_mfma_f32_16x16x32_bf16 v[56:59], v[250:253], v[16:19], v[56:59]\n"
"s_waitcnt lgkmcnt(2)\n"
"ds_write_b64 v242, v[48:49] offset:19456\n"
"buffer_store_dwordx4 a[168:171], v239, s[52:55], s39 offen offset:0 sc1\n"
"v_accvgpr_read_b32 v60, a204\n"
"v_accvgpr_read_b32 v61, a205\n"
"v_accvgpr_read_b32 v62, a206\n"
"v_accvgpr_read_b32 v63, a207\n"
"v_pk_mul_f32 v[60:61], v[60:61], v[168:169] op_sel_hi:[1,0]\n"
"v_pk_mul_f32 v[62:63], v[62:63], v[168:169] op_sel_hi:[1,0]\n"
"v_cvt_pk_bf16_f32 v52, v52, v53\n"
"v_cvt_pk_bf16_f32 v53, v54, v55\n"
"v_mfma_f32_16x16x32_bf16 v[60:63], v[248:251], v[16:19], v[60:63]\n"
"ds_write_b64 v243, v[52:53] offset:19456\n"
"buffer_store_dwordx4 a[172:175], v239, s[52:55], s39 offen offset:3072 sc1\n"
"ds_read_b128 a[184:187], v238 offset:19456\n"
"ds_read_b128 a[188:191], v238 offset:22528\n"
"v_accvgpr_read_b32 v64, a208\n"
"v_accvgpr_read_b32 v65, a209\n"
"v_accvgpr_read_b32 v66, a210\n"
"v_accvgpr_read_b32 v67, a211\n"
"v_pk_mul_f32 v[64:65], v[64:65], v[168:169] op_sel_hi:[1,0]\n"
"v_pk_mul_f32 v[66:67], v[66:67], v[168:169] op_sel_hi:[1,0]\n"
"v_cvt_pk_bf16_f32 v56, v56, v57\n"
"v_cvt_pk_bf16_f32 v57, v58, v59\n"
"v_mfma_f32_16x16x32_bf16 v[64:67], v[250:253], v[20:23], v[64:67]\n"
"ds_write_b64 v240, v[56:57] offset:20480\n"
"v_accvgpr_read_b32 v68, a212\n"
"v_accvgpr_read_b32 v69, a213\n"
"v_accvgpr_read_b32 v70, a214\n"
"v_accvgpr_read_b32 v71, a215\n"
"v_pk_mul_f32 v[68:69], v[68:69], v[168:169] op_sel_hi:[1,0]\n"
"v_pk_mul_f32 v[70:71], v[70:71], v[168:169] op_sel_hi:[1,0]\n"
"v_cvt_pk_bf16_f32 v60, v60, v61\n"
"v_cvt_pk_bf16_f32 v61, v62, v63\n"
"v_mfma_f32_16x16x32_bf16 v[68:71], v[248:251], v[20:23], v[68:71]\n"
"ds_write_b64 v241, v[60:61] offset:20480\n"
"v_cvt_pk_bf16_f32 v64, v64, v65\n"
"v_cvt_pk_bf16_f32 v65, v66, v67\n"
"ds_write_b64 v242, v[64:65] offset:20480\n"
"s_waitcnt lgkmcnt(3)\n"
"buffer_store_dwordx4 a[184:187], v239, s[52:55], s39 offen offset:128 sc1\n"
"buffer_store_dwordx4 a[188:191], v239, s[52:55], s39 offen offset:3200 sc1\n"
"s_nop 0\n"
"v_cvt_pk_bf16_f32 v68, v68, v69\n"
"v_cvt_pk_bf16_f32 v69, v70, v71\n"
"ds_write_b64 v243, v[68:69] offset:20480\n"
"ds_read_b128 a[200:203], v238 offset:20480\n"
"ds_read_b128 a[204:207], v238 offset:23552\n"
"s_waitcnt lgkmcnt(0)\n"
"buffer_store_dwordx4 a[200:203], v239, s[52:55], s39 offen offset:256 sc1\n"
"buffer_store_dwordx4 a[204:207], v239, s[52:55], s39 offen offset:3328 sc1\n"
"s_branch L_END\n"
".p2align 12\n"
".p2align 5\n"
"L_W1EA:\n"
"s_nop 7\n"
"s_branch L_TABDONE\n"
"L_EA:\n"
"s_nop 7\n"
"ds_read_b128 a[72:75], v238 offset:18432\n"
"ds_read_b128 a[76:79], v238 offset:21504\n"
"v_accvgpr_read_b32 v172, a220\n"
"v_add_f32 v246, v172, v246\n"
"v_accvgpr_read_b32 v172, a221\n"
"v_add_f32 v172, v172, v246\n"
"v_cmp_nle_f32 vcc, 0x1f800000, v172\n"
"v_rcp_f32 v173, v172\n"
"s_or_b64 s[86:87], s[86:87], vcc\n"
"ds_read_b128 a[80:83], v238 offset:19456\n"
"ds_read_b128 a[84:87], v238 offset:22528\n"
"v_cmp_nge_f32 vcc, 0x67800000, v172\n"
"v_mul_f32 v172, s81, v173\n"
"v_pk_mul_f32 v[96:97], v[96:97], v[172:173] op_sel_hi:[1,0]\n"
"v_pk_mul_f32 v[98:99], v[98:99], v[172:173] op_sel_hi:[1,0]\n"
"v_pk_mul_f32 v[100:101], v[100:101], v[172:173] op_sel_hi:[1,0]\n"
"v_pk_mul_f32 v[102:103], v[102:103], v[172:173] op_sel_hi:[1,0]\n"
"v_mfma_f32_16x16x32_bf16 v[96:99], v[250:253], a[232:235], v[96:99]\n"
"s_add_u32 s38, s89, 49152\n"
"ds_read_b128 a[88:91], v238 offset:20480\n"
"v_accvgpr_read_b32 v174, a222\n"
"v_mfma_f32_16x16x32_bf16 v[100:103], v[248:251], a[232:235], v[100:103]\n"
"s_or_b64 s[86:87], s[86:87], vcc\n"
"ds_read_b128 a[92:95], v238 offset:23552\n"
"ds_read_b128 a[0:3], v238 offset:0\n"
"v_add_f32 v247, v174, v247\n"
"v_cvt_pk_bf16_f32 v96, v96, v97\n"
"v_pk_mul_f32 v[104:105], v[104:105], v[172:173] op_sel_hi:[1,0]\n"
"v_pk_mul_f32 v[106:107], v[106:107], v[172:173] op_sel_hi:[1,0]\n"
"v_cvt_pk_bf16_f32 v97, v98, v99\n"
"v_pk_mul_f32 v[108:109], v[108:109], v[172:173] op_sel_hi:[1,0]\n"
"v_mfma_f32_16x16x32_bf16 v[104:107], v[250:253], a[236:239], v[104:107]\n"
"s_add_u32 s39, s89, 73728\n"
"ds_write_b64 v240, v[96:97] offset:12288\n"
"v_cvt_pk_bf16_f32 v100, v100, v101\n"
"v_cvt_pk_bf16_f32 v101, v102, v103\n"
"v_pk_mul_f32 v[110:111], v[110:111], v[172:173] op_sel_hi:[1,0]\n"
"v_pk_mul_f32 v[112:113], v[112:113], v[172:173] op_sel_hi:[1,0]\n"
"v_pk_mul_f32 v[114:115], v[114:115], v[172:173] op_sel_hi:[1,0]\n"
"v_mfma_f32_16x16x32_bf16 v[108:111], v[248:251], a[236:239], v[108:111]\n"
"s_waitcnt lgkmcnt(5)\n"
"ds_write_b64 v241, v[100:101] offset:12288\n"
"v_mfma_f32_16x16x32_bf16 v[112:115], v[250:253], a[240:243], v[112:115]\n"
"s_add_u32 s39, s39, s10\n"
"v_cvt_pk_bf16_f32 v104, v104, v105\n"
"v_cvt_pk_bf16_f32 v105, v106, v107\n"
"v_accvgpr_read_b32 v174, a223\n"
"v_add_f32 v174, v174, v247\n"
"v_cvt_pk_bf16_f32 v108, v108, v109\n"
"v_pk_mul_f32 v[116:117], v[116:117], v[172:173] op_sel_hi:[1,0]\n"
"v_pk_mul_f32 v[118:119], v[118:119], v[172:173] op_sel_hi:[1,0]\n"
"v_cvt_pk_bf16_f32 v109, v110, v111\n"
"v_pk_mul_f32 v[120:121], v[120:121], v[172:173] op_sel_hi:[1,0]\n"
"v_mfma_f32_16x16x32_bf16 v[116:119], v[248:251], a[240:243], v[116:119]\n"
"s_waitcnt lgkmcnt(3)\n"
"ds_write_b64 v242, v[104:105] offset:12288\n"
"v_cvt_pk_bf16_f32 v112, v112, v113\n"
"v_cvt_pk_bf16_f32 v113, v114, v115\n"
"v_pk_mul_f32 v[122:123], v[122:123], v[172:173] op_sel_hi:[1,0]\n"
"v_pk_mul_f32 v[124:125], v[124:125], v[172:173] op_sel_hi:[1,0]\n"
"v_pk_mul_f32 v[126:127], v[126:127], v[172:173] op_sel_hi:[1,0]\n"
"v_mfma_f32_16x16x32_bf16 v[120:123], v[250:253], a[244:247], v[120:123]\n"
"s_add_u32 s36, s89, 0\n"
"ds_write_b64 v243, v[108:109] offset:12288\n"
"v_mfma_f32_16x16x32_bf16 v[124:127], v[248:251], a[244:247], v[124:127]\n"
"s_add_u32 s37, s89, 24576\n"
"v_cvt_pk_bf16_f32 v116, v116, v117\n"
"v_cvt_pk_bf16_f32 v117, v118, v119\n"
"v_cmp_nle_f32 vcc, 0x1f800000, v174\n"
"v_rcp_f32 v175, v174\n"
"v_cvt_pk_bf16_f32 v120, v120, v121\n"
"v_cvt_pk_bf16_f32 v121, v122, v123\n"
"v_pk_mul_f32 v[128:129], v[128:129], v[172:173] op_sel_hi:[1,0]\n"
"v_cvt_pk_bf16_f32 v124, v124, v125\n"
"v_cvt_pk_bf16_f32 v125, v126, v127\n"
"v_pk_mul_f32 v[130:131], v[130:131], v[172:173] op_sel_hi:[1,0]\n"
"v_pk_mul_f32 v[132:133], v[132:133], v[172:173] op_sel_hi:[1,0]\n"
"v_pk_mul_f32 v[134:135], v[134:135], v[172:173] op_sel_hi:[1,0]\n"
"v_mfma_f32_16x16x32_bf16 v[128:131], v[250:253], a[248:251], v[128:131]\n"
"s_or_b64 s[86:87], s[86:87], vcc\n"
"ds_read_b128 v[96:99], v238 offset:12288\n"
"ds_read_b128 v[100:103], v238 offset:15360\n"
"v_mfma_f32_16x16x32_bf16 v[132:135], v[248:251], a[248:251], v[132:135]\n"
"ds_write_b64 v240, v[112:113] offset:13312\n"
"v_cmp_nge_f32 vcc, 0x67800000, v174\n"
"v_pk_mul_f32 v[136:137], v[136:137], v[172:173] op_sel_hi:[1,0]\n"
"v_pk_mul_f32 v[138:139], v[138:139], v[172:173] op_sel_hi:[1,0]\n"
"v_cvt_pk_bf16_f32 v128, v128, v129\n"
"v_cvt_pk_bf16_f32 v129, v130, v131\n"
"v_mfma_f32_16x16x32_bf16 v[136:139], v[250:253], a[252:255], v[136:139]\n"
"s_or_b64 s[86:87], s[86:87], vcc\n"
"ds_write_b64 v241, v[116:117] offset:13312\n"
"v_cvt_pk_bf16_f32 v132, v132, v133\n"
"v_cvt_pk_bf16_f32 v133, v134, v135\n"
"v_pk_mul_f32 v[140:141], v[140:141], v[172:173] op_sel_hi:[1,0]\n"
"v_pk_mul_f32 v[142:143], v[142:143], v[172:173] op_sel_hi:[1,0]\n"
"v_mul_f32 v174, s81, v175\n"
"v_accvgpr_read_b32 v176, a168\n"
"v_mfma_f32_16x16x32_bf16 v[140:143], v[248:251], a[252:255], v[140:143]\n"
"ds_write_b64 v242, v[120:121] offset:13312\n"
"v_accvgpr_read_b32 v177, a169\n"
"v_accvgpr_read_b32 v178, a170\n"
"v_accvgpr_read_b32 v179, a171\n"
"v_pk_mul_f32 v[176:177], v[176:177], v[174:175] op_sel_hi:[1,0]\n"
"v_pk_mul_f32 v[178:179], v[178:179], v[174:175] op_sel_hi:[1,0]\n"
"v_cvt_pk_bf16_f32 v136, v136, v137\n"
"v_cvt_pk_bf16_f32 v137, v138, v139\n"
"s_waitcnt lgkmcnt(3)\n"
"ds_write_b64 v243, v[124:125] offset:13312\n"
"buffer_store_dwordx4 v[96:99], v239, s[52:55], s38 offen offset:0 sc1\n"
"ds_read_b128 v[112:115], v238 offset:13312\n"
"ds_read_b128 v[116:119], v238 offset:16384\n"
"v_mfma_f32_16x16x32_bf16 v[176:179], v[250:253], a[72:75], v[176:179]\n"
"ds_write_b64 v240, v[128:129] offset:14336\n"
"buffer_store_dwordx4 v[100:103], v239, s[52:55], s38 offen offset:3072 sc1\n"
"ds_write_b64 v241, v[132:133] offset:14336\n"
"v_accvgpr_read_b32 v180, a172\n"
"v_accvgpr_read_b32 v181, a173\n"
"v_accvgpr_read_b32 v182, a174\n"
"v_accvgpr_read_b32 v183, a175\n"
"v_pk_mul_f32 v[180:181], v[180:181], v[174:175] op_sel_hi:[1,0]\n"
"v_pk_mul_f32 v[182:183], v[182:183], v[174:175] op_sel_hi:[1,0]\n"
"v_cvt_pk_bf16_f32 v140, v140, v141\n"
"v_cvt_pk_bf16_f32 v141, v142, v143\n"
"v_mfma_f32_16x16x32_bf16 v[180:183], v[248:251], a[72:75], v[180:183]\n"
"ds_write_b64 v242, v[136:137] offset:14336\n"
"v_accvgpr_read_b32 v184, a176\n"
"v_accvgpr_read_b32 v185, a177\n"
"v_accvgpr_read_b32 v186, a178\n"
"v_accvgpr_read_b32 v187, a179\n"
"v_pk_mul_f32 v[184:185], v[184:185], v[174:175] op_sel_hi:[1,0]\n"
"s_waitcnt lgkmcnt(3)\n"
"ds_write_b64 v243, v[140:141] offset:14336\n"
"buffer_store_dwordx4 v[112:115], v239, s[52:55], s38 offen offset:128 sc1\n"
"ds_read_b128 v[128:131], v238 offset:14336\n"
"ds_read_b128 v[132:135], v238 offset:17408\n"
"v_pk_mul_f32 v[186:187], v[186:187], v[174:175] op_sel_hi:[1,0]\n"
"v_cvt_pk_bf16_f32 v176, v176, v177\n"
"v_cvt_pk_bf16_f32 v177, v178, v179\n"
"v_mfma_f32_16x16x32_bf16 v[184:187], v[250:253], a[76:79], v[184:187]\n"
"ds_write_b64 v240, v[176:177] offset:18432\n"
"buffer_store_dwordx4 v[116:119], v239, s[52:55], s38 offen offset:3200 sc1\n"
"ds_read_b128 a[4:7], v238 offset:3072\n"
"ds_read_b128 a[8:11], v238 offset:1024\n"
"v_accvgpr_read_b32 v188, a180\n"
"v_accvgpr_read_b32 v189, a181\n"
"v_accvgpr_read_b32 v190, a182\n"
"v_accvgpr_read_b32 v191, a183\n"
"v_pk_mul_f32 v[188:189], v[188:189], v[174:175] op_sel_hi:[1,0]\n"
"v_pk_mul_f32 v[190:191], v[190:191], v[174:175] op_sel_hi:[1,0]\n"
"v_cvt_pk_bf16_f32 v180, v180, v181\n"
"v_cvt_pk_bf16_f32 v181, v182, v183\n"
"v_mfma_f32_16x16x32_bf16 v[188:191], v[248:251], a[76:79], v[188:191]\n"
"ds_write_b64 v241, v[180:181] offset:18432\n"
"v_cvt_pk_bf16_f32 v184, v184, v185\n"
"v_cvt_pk_bf16_f32 v185, v186, v187\n"
"v_accvgpr_read_b32 v192, a184\n"
"v_accvgpr_read_b32 v193, a185\n"
"v_accvgpr_read_b32 v194, a186\n"
"v_accvgpr_read_b32 v195, a187\n"
"v_pk_mul_f32 v[192:193], v[192:193], v[174:175] op_sel_hi:[1,0]\n"
"v_cvt_pk_bf16_f32 v188, v188, v189\n"
"v_cvt_pk_bf16_f32 v189, v190, v191\n"
"v_pk_mul_f32 v[194:195], v[194:195], v[174:175] op_sel_hi:[1,0]\n"
"v_accvgpr_read_b32 v196, a188\n"
"v_accvgpr_read_b32 v197, a189\n"
"v_mfma_f32_16x16x32_bf16 v[192:195], v[250:253], a[80:83], v[192:195]\n"
"s_waitcnt lgkmcnt(4)\n"
"ds_write_b64 v242, v[184:185] offset:18432\n"
"buffer_store_dwordx4 v[128:131], v239, s[52:55], s38 offen offset:256 sc1\n"
"ds_write_b64 v243, v[188:189] offset:18432\n"
"v_accvgpr_read_b32 v198, a190\n"
"v_accvgpr_read_b32 v199, a191\n"
"v_pk_mul_f32 v[196:197], v[196:197], v[174:175] op_sel_hi:[1,0]\n"
"v_pk_mul_f32 v[198:199], v[198:199], v[174:175] op_sel_hi:[1,0]\n"
"v_accvgpr_read_b32 v200, a192\n"
"v_accvgpr_read_b32 v201, a193\n"
"v_mfma_f32_16x16x32_bf16 v[196:199], v[248:251], a[80:83], v[196:199]\n"
"s_waitcnt lgkmcnt(3)\n"
"ds_read_b128 a[168:171], v238 offset:18432\n"
"buffer_store_dwordx4 v[132:135], v239, s[52:55], s38 offen offset:3328 sc1\n"
"ds_read_b128 a[172:175], v238 offset:21504\n"
"ds_read_b128 a[12:15], v238 offset:4096\n"
"v_accvgpr_read_b32 v202, a194\n"
"v_accvgpr_read_b32 v203, a195\n"
"v_pk_mul_f32 v[200:201], v[200:201], v[174:175] op_sel_hi:[1,0]\n"
"v_pk_mul_f32 v[202:203], v[202:203], v[174:175] op_sel_hi:[1,0]\n"
"v_cvt_pk_bf16_f32 v192, v192, v193\n"
"v_cvt_pk_bf16_f32 v193, v194, v195\n"
"v_mfma_f32_16x16x32_bf16 v[200:203], v[250:253], a[84:87], v[200:203]\n"
"ds_write_b64 v240, v[192:193] offset:19456\n"
"v_accvgpr_read_b32 v204, a196\n"
"v_accvgpr_read_b32 v205, a197\n"
"v_accvgpr_read_b32 v206, a198\n"
"v_accvgpr_read_b32 v207, a199\n"
"v_pk_mul_f32 v[204:205], v[204:205], v[174:175] op_sel_hi:[1,0]\n"
"v_pk_mul_f32 v[206:207], v[206:207], v[174:175] op_sel_hi:[1,0]\n"
"v_cvt_pk_bf16_f32 v196, v196, v197\n"
"v_cvt_pk_bf16_f32 v197, v198, v199\n"
"v_mfma_f32_16x16x32_bf16 v[204:207], v[248:251], a[84:87], v[204:207]\n"
"ds_write_b64 v241, v[196:197] offset:19456\n"
"v_accvgpr_read_b32 v208, a200\n"
"v_accvgpr_read_b32 v209, a201\n"
"v_accvgpr_read_b32 v210, a202\n"
"v_accvgpr_read_b32 v211, a203\n"
"v_pk_mul_f32 v[208:209], v[208:209], v[174:175] op_sel_hi:[1,0]\n"
"v_pk_mul_f32 v[210:211], v[210:211], v[174:175] op_sel_hi:[1,0]\n"
"v_cvt_pk_bf16_f32 v200, v200, v201\n"
"v_cvt_pk_bf16_f32 v201, v202, v203\n"
"v_mfma_f32_16x16x32_bf16 v[208:211], v[250:253], a[88:91], v[208:211]\n"
"s_waitcnt lgkmcnt(3)\n"
"ds_write_b64 v242, v[200:201] offset:19456\n"
"buffer_store_dwordx4 a[168:171], v239, s[52:55], s39 offen offset:0 sc1\n"
"ds_read_b128 a[16:19], v238 offset:2048\n"
"ds_read_b128 a[20:23], v238 offset:5120\n"
"v_accvgpr_read_b32 v212, a204\n"
"v_accvgpr_read_b32 v213, a205\n"
"v_accvgpr_read_b32 v214, a206\n"
"v_accvgpr_read_b32 v215, a207\n"
"v_pk_mul_f32 v[212:213], v[212:213], v[174:175] op_sel_hi:[1,0]\n"
"v_pk_mul_f32 v[214:215], v[214:215], v[174:175] op_sel_hi:[1,0]\n"
"v_cvt_pk_bf16_f32 v204, v204, v205\n"
"v_cvt_pk_bf16_f32 v205, v206, v207\n"
"v_mfma_f32_16x16x32_bf16 v[212:215], v[248:251], a[88:91], v[212:215]\n"
"ds_write_b64 v243, v[204:205] offset:19456\n"
"buffer_store_dwordx4 a[172:175], v239, s[52:55], s39 offen offset:3072 sc1\n"
"ds_read_b128 a[184:187], v238 offset:19456\n"
"ds_read_b128 a[188:191], v238 offset:22528\n"
"v_accvgpr_read_b32 v216, a208\n"
"v_accvgpr_read_b32 v217, a209\n"
"v_accvgpr_read_b32 v218, a210\n"
"v_accvgpr_read_b32 v219, a211\n"
"v_pk_mul_f32 v[216:217], v[216:217], v[174:175] op_sel_hi:[1,0]\n"
"v_pk_mul_f32 v[218:219], v[218:219], v[174:175] op_sel_hi:[1,0]\n"
"v_cvt_pk_bf16_f32 v208, v208, v209\n"
"v_cvt_pk_bf16_f32 v209, v210, v211\n"
"v_mfma_f32_16x16x32_bf16 v[216:219], v[250:253], a[92:95], v[216:219]\n"
"ds_write_b64 v240, v[208:209] offset:20480\n"
"v_accvgpr_read_b32 v220, a212\n"
"v_accvgpr_read_b32 v221, a213\n"
"v_accvgpr_read_b32 v222, a214\n"
"v_accvgpr_read_b32 v223, a215\n"
"v_pk_mul_f32 v[220:221], v[220:221], v[174:175] op_sel_hi:[1,0]\n"
"v_pk_mul_f32 v[222:223], v[222:223], v[174:175] op_sel_hi:[1,0]\n"
"v_cvt_pk_bf16_f32 v212, v212, v213\n"
"v_cvt_pk_bf16_f32 v213, v214, v215\n"
"v_mfma_f32_16x16x32_bf16 v[220:223], v[248:251], a[92:95], v[220:223]\n"
"s_waitcnt lgkmcnt(4)\n"
"ds_write_b64 v241, v[212:213] offset:20480\n"
"v_cvt_pk_bf16_f32 v216, v216, v217\n"
"v_cvt_pk_bf16_f32 v217, v218, v219\n"
"v_accvgpr_read_b32 v168, a216\n"
"v_add_f32 v244, v168, v244\n"
"v_accvgpr_read_b32 v168, a217\n"
"v_add_f32 v168, v168, v244\n"
"v_cvt_pk_bf16_f32 v220, v220, v221\n"
"v_cvt_pk_bf16_f32 v221, v222, v223\n"
"v_cmp_nle_f32 vcc, 0x1f800000, v168\n"
"v_rcp_f32 v169, v168\n"
"s_or_b64 s[86:87], s[86:87], vcc\n"
"ds_write_b64 v242, v[216:217] offset:20480\n"
"v_cmp_nge_f32 vcc, 0x67800000, v168\n"
"v_mul_f32 v168, s81, v169\n"
"v_pk_mul_f32 v[0:1], v[0:1], v[168:169] op_sel_hi:[1,0]\n"
"v_pk_mul_f32 v[2:3], v[2:3], v[168:169] op_sel_hi:[1,0]\n"
"v_pk_mul_f32 v[4:5], v[4:5], v[168:169] op_sel_hi:[1,0]\n"
"v_pk_mul_f32 v[6:7], v[6:7], v[168:169] op_sel_hi:[1,0]\n"
"v_mfma_f32_16x16x32_bf16 v[0:3], v[250:253], a[0:3], v[0:3]\n"
"s_waitcnt lgkmcnt(3)\n"
"ds_write_b64 v243, v[220:221] offset:20480\n"
"v_mfma_f32_16x16x32_bf16 v[4:7], v[248:251], a[0:3], v[4:7]\n"
"s_or_b64 s[86:87], s[86:87], vcc\n"
"buffer_store_dwordx4 a[184:187], v239, s[52:55], s39 offen offset:128 sc1\n"
"ds_read_b128 a[200:203], v238 offset:20480\n"
"ds_read_b128 a[204:207], v238 offset:23552\n"
"v_pk_mul_f32 v[8:9], v[8:9], v[168:169] op_sel_hi:[1,0]\n"
"v_pk_mul_f32 v[10:11], v[10:11], v[168:169] op_sel_hi:[1,0]\n"
"v_cvt_pk_bf16_f32 v0, v0, v1\n"
"v_cvt_pk_bf16_f32 v1, v2, v3\n"
"v_mfma_f32_16x16x32_bf16 v[8:11], v[250:253], a[4:7], v[8:11]\n"
"ds_write_b64 v240, v[0:1] offset:0\n"
"buffer_store_dwordx4 a[188:191], v239, s[52:55], s39 offen offset:3200 sc1\n"
"ds_read_b128 a[24:27], v238 offset:6144\n"
"ds_read_b128 a[28:31], v238 offset:9216\n"
"v_pk_mul_f32 v[12:13], v[12:13], v[168:169] op_sel_hi:[1,0]\n"
"v_pk_mul_f32 v[14:15], v[14:15], v[168:169] op_sel_hi:[1,0]\n"
"v_cvt_pk_bf16_f32 v4, v4, v5\n"
"v_cvt_pk_bf16_f32 v5, v6, v7\n"
"v_mfma_f32_16x16x32_bf16 v[12:15], v[248:251], a[4:7], v[12:15]\n"
"ds_write_b64 v241, v[4:5] offset:0\n"
"v_cvt_pk_bf16_f32 v8, v8, v9\n"
"v_cvt_pk_bf16_f32 v9, v10, v11\n"
"v_pk_mul_f32 v[16:17], v[16:17], v[168:169] op_sel_hi:[1,0]\n"
"v_pk_mul_f32 v[18:19], v[18:19], v[168:169] op_sel_hi:[1,0]\n"
"v_pk_mul_f32 v[20:21], v[20:21], v[168:169] op_sel_hi:[1,0]\n"
"v_pk_mul_f32 v[22:23], v[22:23], v[168:169] op_sel_hi:[1,0]\n"
"v_mfma_f32_16x16x32_bf16 v[16:19], v[250:253], a[8:11], v[16:19]\n"
"s_waitcnt lgkmcnt(4)\n"
"ds_write_b64 v242, v[8:9] offset:0\n"
"v_mfma_f32_16x16x32_bf16 v[20:23], v[248:251], a[8:11], v[20:23]\n"
"buffer_store_dwordx4 a[200:203], v239, s[52:55], s39 offen offset:256 sc1\n"
"ds_read_b128 a[32:35], v238 offset:7168\n"
"ds_read_b128 a[36:39], v238 offset:10240\n"
"v_cvt_pk_bf16_f32 v12, v12, v13\n"
"v_cvt_pk_bf16_f32 v13, v14, v15\n"
"v_pk_mul_f32 v[24:25], v[24:25], v[168:169] op_sel_hi:[1,0]\n"
"v_pk_mul_f32 v[26:27], v[26:27], v[168:169] op_sel_hi:[1,0]\n"
"v_cvt_pk_bf16_f32 v16, v16, v17\n"
"v_cvt_pk_bf16_f32 v17, v18, v19\n"
"v_mfma_f32_16x16x32_bf16 v[24:27], v[250:253], a[12:15], v[24:27]\n"
"ds_write_b64 v243, v[12:13] offset:0\n"
"buffer_store_dwordx4 a[204:207], v239, s[52:55], s39 offen offset:3328 sc1\n"
"ds_read_b128 v[0:3], v238 offset:0\n"
"ds_read_b128 v[4:7], v238 offset:3072\n"
"v_pk_mul_f32 v[28:29], v[28:29], v[168:169] op_sel_hi:[1,0]\n"
"v_pk_mul_f32 v[30:31], v[30:31], v[168:169] op_sel_hi:[1,0]\n"
"v_cvt_pk_bf16_f32 v20, v20, v21\n"
"v_cvt_pk_bf16_f32 v21, v22, v23\n"
"v_mfma_f32_16x16x32_bf16 v[28:31], v[248:251], a[12:15], v[28:31]\n"
"ds_write_b64 v240, v[16:17] offset:1024\n"
"v_cvt_pk_bf16_f32 v24, v24, v25\n"
"v_cvt_pk_bf16_f32 v25, v26, v27\n"
"v_pk_mul_f32 v[32:33], v[32:33], v[168:169] op_sel_hi:[1,0]\n"
"v_pk_mul_f32 v[34:35], v[34:35], v[168:169] op_sel_hi:[1,0]\n"
"v_pk_mul_f32 v[36:37], v[36:37], v[168:169] op_sel_hi:[1,0]\n"
"v_pk_mul_f32 v[38:39], v[38:39], v[168:169] op_sel_hi:[1,0]\n"
"v_mfma_f32_16x16x32_bf16 v[32:35], v[250:253], a[16:19], v[32:35]\n"
"s_waitcnt lgkmcnt(5)\n"
"ds_write_b64 v241, v[20:21] offset:1024\n"
"v_cvt_pk_bf16_f32 v28, v28, v29\n"
"v_cvt_pk_bf16_f32 v29, v30, v31\n"
"v_mfma_f32_16x16x32_bf16 v[36:39], v[248:251], a[16:19], v[36:39]\n"
"ds_write_b64 v242, v[24:25] offset:1024\n"
"v_accvgpr_read_b32 v170, a218\n"
"v_add_f32 v245, v170, v245\n"
"v_pk_mul_f32 v[40:41], v[40:41], v[168:169] op_sel_hi:[1,0]\n"
"v_pk_mul_f32 v[42:43], v[42:43], v[168:169] op_sel_hi:[1,0]\n"
"v_cvt_pk_bf16_f32 v32, v32, v33\n"
"v_cvt_pk_bf16_f32 v33, v34, v35\n"
"s_waitcnt lgkmcnt(3)\n"
"ds_write_b64 v243, v[28:29] offset:1024\n"
"v_mfma_f32_16x16x32_bf16 v[40:43], v[250:253], a[20:23], v[40:43]\n"
"buffer_store_dwordx4 v[0:3], v239, s[52:55], s36 offen offset:0 sc1\n"
"ds_read_b128 v[16:19], v238 offset:1024\n"
"ds_read_b128 v[20:23], v238 offset:4096\n"
"v_accvgpr_read_b32 v170, a219\n"
"v_add_f32 v170, v170, v245\n"
"v_pk_mul_f32 v[44:45], v[44:45], v[168:169] op_sel_hi:[1,0]\n"
"v_pk_mul_f32 v[46:47], v[46:47], v[168:169] op_sel_hi:[1,0]\n"
"v_cmp_nle_f32 vcc, 0x1f800000, v170\n"
"v_cvt_pk_bf16_f32 v36, v36, v37\n"
"v_mfma_f32_16x16x32_bf16 v[44:47], v[248:251], a[20:23], v[44:47]\n"
"s_or_b64 s[86:87], s[86:87], vcc\n"
"buffer_store_dwordx4 v[4:7], v239, s[52:55], s36 offen offset:3072 sc1\n"
"ds_write_b64 v240, v[32:33] offset:2048\n"
"v_rcp_f32 v171, v170\n"
"v_cvt_pk_bf16_f32 v37, v38, v39\n"
"v_cmp_nge_f32 vcc, 0x67800000, v170\n"
"v_mul_f32 v170, s81, v171\n"
"v_pk_mul_f32 v[48:49], v[48:49], v[170:171] op_sel_hi:[1,0]\n"
"v_pk_mul_f32 v[50:51], v[50:51], v[170:171] op_sel_hi:[1,0]\n"
"v_cvt_pk_bf16_f32 v40, v40, v41\n"
"v_cvt_pk_bf16_f32 v41, v42, v43\n"
"v_mfma_f32_16x16x32_bf16 v[48:51], v[250:253], a[24:27], v[48:51]\n"
"s_or_b64 s[86:87], s[86:87], vcc\n"
"ds_write_b64 v241, v[36:37] offset:2048\n"
"v_cvt_pk_bf16_f32 v44, v44, v45\n"
"v_cvt_pk_bf16_f32 v45, v46, v47\n"
"v_pk_mul_f32 v[52:53], v[52:53], v[170:171] op_sel_hi:[1,0]\n"
"v_pk_mul_f32 v[54:55], v[54:55], v[170:171] op_sel_hi:[1,0]\n"
"s_waitcnt lgkmcnt(2)\n"
"ds_write_b64 v242, v[40:41] offset:2048\n"
"v_mfma_f32_16x16x32_bf16 v[52:55], v[248:251], a[24:27], v[52:55]\n"
"buffer_store_dwordx4 v[16:19], v239, s[52:55], s36 offen offset:128 sc1\n"
"ds_write_b64 v243, v[44:45] offset:2048\n"
"v_pk_mul_f32 v[56:57], v[56:57], v[170:171] op_sel_hi:[1,0]\n"
"v_pk_mul_f32 v[58:59], v[58:59], v[170:171] op_sel_hi:[1,0]\n"
"v_cvt_pk_bf16_f32 v48, v48, v49\n"
"v_cvt_pk_bf16_f32 v49, v50, v51\n"
"v_mfma_f32_16x16x32_bf16 v[56:59], v[250:253], a[28:31], v[56:59]\n"
"ds_read_b128 v[32:35], v238 offset:2048\n"
"buffer_store_dwordx4 v[20:23], v239, s[52:55], s36 offen offset:3200 sc1\n"
"ds_read_b128 a[40:43], v238 offset:8192\n"
"ds_read_b128 v[36:39], v238 offset:5120\n"
"v_pk_mul_f32 v[60:61], v[60:61], v[170:171] op_sel_hi:[1,0]\n"
"v_pk_mul_f32 v[62:63], v[62:63], v[170:171] op_sel_hi:[1,0]\n"
"v_cvt_pk_bf16_f32 v52, v52, v53\n"
"v_cvt_pk_bf16_f32 v53, v54, v55\n"
"v_mfma_f32_16x16x32_bf16 v[60:63], v[248:251], a[28:31], v[60:63]\n"
"ds_read_b128 a[44:47], v238 offset:11264\n"
"ds_write_b64 v240, v[48:49] offset:6144\n"
"v_cvt_pk_bf16_f32 v56, v56, v57\n"
"v_cvt_pk_bf16_f32 v57, v58, v59\n"
"v_pk_mul_f32 v[64:65], v[64:65], v[170:171] op_sel_hi:[1,0]\n"
"v_pk_mul_f32 v[66:67], v[66:67], v[170:171] op_sel_hi:[1,0]\n"
"v_pk_mul_f32 v[68:69], v[68:69], v[170:171] op_sel_hi:[1,0]\n"
"v_pk_mul_f32 v[70:71], v[70:71], v[170:171] op_sel_hi:[1,0]\n"
"v_cvt_pk_bf16_f32 v60, v60, v61\n"
"v_cvt_pk_bf16_f32 v61, v62, v63\n"
"v_mfma_f32_16x16x32_bf16 v[64:67], v[250:253], a[32:35], v[64:67]\n"
"ds_write_b64 v241, v[52:53] offset:6144\n"
"v_mfma_f32_16x16x32_bf16 v[68:71], v[248:251], a[32:35], v[68:71]\n"
"ds_write_b64 v242, v[56:57] offset:6144\n"
"v_pk_mul_f32 v[72:73], v[72:73], v[170:171] op_sel_hi:[1,0]\n"
"v_pk_mul_f32 v[74:75], v[74:75], v[170:171] op_sel_hi:[1,0]\n"
"s_waitcnt lgkmcnt(4)\n"
"buffer_store_dwordx4 v[32:35], v239, s[52:55], s36 offen offset:256 sc1\n"
"ds_write_b64 v243, v[60:61] offset:6144\n"
"s_waitcnt lgkmcnt(4)\n"
"v_mfma_f32_16x16x32_bf16 v[72:75], v[250:253], a[36:39], v[72:75]\n"
"ds_read_b128 v[48:51], v238 offset:6144\n"
"v_cvt_pk_bf16_f32 v64, v64, v65\n"
"v_cvt_pk_bf16_f32 v65, v66, v67\n"
"v_cvt_pk_bf16_f32 v68, v68, v69\n"
"v_pk_mul_f32 v[76:77], v[76:77], v[170:171] op_sel_hi:[1,0]\n"
"v_pk_mul_f32 v[78:79], v[78:79], v[170:171] op_sel_hi:[1,0]\n"
"v_cvt_pk_bf16_f32 v69, v70, v71\n"
"v_pk_mul_f32 v[80:81], v[80:81], v[170:171] op_sel_hi:[1,0]\n"
"v_mfma_f32_16x16x32_bf16 v[76:79], v[248:251], a[36:39], v[76:79]\n"
"ds_read_b128 v[52:55], v238 offset:9216\n"
"buffer_store_dwordx4 v[36:39], v239, s[52:55], s36 offen offset:3328 sc1\n"
"ds_write_b64 v240, v[64:65] offset:7168\n"
"v_pk_mul_f32 v[82:83], v[82:83], v[170:171] op_sel_hi:[1,0]\n"
"v_cvt_pk_bf16_f32 v72, v72, v73\n"
"v_cvt_pk_bf16_f32 v73, v74, v75\n"
"v_mfma_f32_16x16x32_bf16 v[80:83], v[250:253], a[40:43], v[80:83]\n"
"ds_write_b64 v241, v[68:69] offset:7168\n"
"v_cvt_pk_bf16_f32 v76, v76, v77\n"
"v_cvt_pk_bf16_f32 v77, v78, v79\n"
"v_pk_mul_f32 v[84:85], v[84:85], v[170:171] op_sel_hi:[1,0]\n"
"v_pk_mul_f32 v[86:87], v[86:87], v[170:171] op_sel_hi:[1,0]\n"
"v_pk_mul_f32 v[88:89], v[88:89], v[170:171] op_sel_hi:[1,0]\n"
"v_pk_mul_f32 v[90:91], v[90:91], v[170:171] op_sel_hi:[1,0]\n"
"v_mfma_f32_16x16x32_bf16 v[84:87], v[248:251], a[40:43], v[84:87]\n"
"ds_write_b64 v242, v[72:73] offset:7168\n"
"v_mfma_f32_16x16x32_bf16 v[88:91], v[250:253], a[44:47], v[88:91]\n"
"v_cvt_pk_bf16_f32 v80, v80, v81\n"
"v_cvt_pk_bf16_f32 v81, v82, v83\n"
"ds_write_b64 v243, v[76:77] offset:7168\n"
"s_waitcnt lgkmcnt(4)\n"
"buffer_store_dwordx4 v[48:51], v239, s[52:55], s37 offen offset:0 sc1\n"
"ds_read_b128 v[64:67], v238 offset:7168\n"
"ds_read_b128 v[68:71], v238 offset:10240\n"
"v_pk_mul_f32 v[92:93], v[92:93], v[170:171] op_sel_hi:[1,0]\n"
"v_pk_mul_f32 v[94:95], v[94:95], v[170:171] op_sel_hi:[1,0]\n"
"v_cvt_pk_bf16_f32 v84, v84, v85\n"
"v_cvt_pk_bf16_f32 v85, v86, v87\n"
"v_mfma_f32_16x16x32_bf16 v[92:95], v[248:251], a[44:47], v[92:95]\n"
"ds_write_b64 v240, v[80:81] offset:8192\n"
"buffer_store_dwordx4 v[52:55], v239, s[52:55], s37 offen offset:3072 sc1\n"
"ds_write_b64 v241, v[84:85] offset:8192\n"
"v_cvt_pk_bf16_f32 v88, v88, v89\n"
"v_cvt_pk_bf16_f32 v89, v90, v91\n"
"ds_write_b64 v242, v[88:89] offset:8192\n"
"s_waitcnt lgkmcnt(3)\n"
"buffer_store_dwordx4 v[64:67], v239, s[52:55], s37 offen offset:128 sc1\n"
"v_cvt_pk_bf16_f32 v92, v92, v93\n"
"v_cvt_pk_bf16_f32 v93, v94, v95\n"
"ds_write_b64 v243, v[92:93] offset:8192\n"
"buffer_store_dwordx4 v[68:71], v239, s[52:55], s37 offen offset:3200 sc1\n"
"ds_read_b128 v[80:83], v238 offset:8192\n"
"ds_read_b128 v[84:87], v238 offset:11264\n"
"s_waitcnt lgkmcnt(0)\n"
"buffer_store_dwordx4 v[80:83], v239, s[52:55], s37 offen offset:256 sc1\n"
"buffer_store_dwordx4 v[84:87], v239, s[52:55], s37 offen offset:3328 sc1\n"
"L_END:\n"
"s_or_b32 %0, s86, s87\n"
  : "=s"(bad)
  : "s"(ka), "s"(b), "s"(wave)
  : "v0","v1","v2","v3","v4","v5","v6","v7","v8","v9","v10","v11","v12","v13","v14","v15","v16","v17","v18","v19","v20","v21","v22","v23","v24","v25","v26","v27","v28","v29","v30","v31","v32","v33","v34","v35","v36","v37","v38","v39","v40","v41","v42","v43","v44","v45","v46","v47","v48","v49","v50","v51","v52","v53","v54","v55","v56","v57","v58","v59","v60","v61","v62","v63","v64","v65","v66","v67","v68","v69","v70","v71","v72","v73","v74","v75","v76","v77","v78","v79","v80","v81","v82","v83","v84","v85","v86","v87","v88","v89","v90","v91","v92","v93","v94","v95","v96","v97","v98","v99","v100","v101","v102","v103","v104","v105","v106","v107","v108","v109","v110","v111","v112","v113","v114","v115","v116","v117","v118","v119","v120","v121","v122","v123","v124","v125","v126","v127","v128","v129","v130","v131","v132","v133","v134","v135","v136","v137","v138","v139","v140","v141","v142","v143","v144","v145","v146","v147","v148","v149","v150","v151","v152","v153","v154","v155","v156","v157","v158","v159","v160","v161","v162","v163","v164","v165","v166","v167","v168","v169","v170","v171","v172","v173","v174","v175","v176","v177","v178","v179","v180","v181","v182","v183","v184","v185","v186","v187","v188","v189","v190","v191","v192","v193","v194","v195","v196","v197","v198","v199","v200","v201","v202","v203","v204","v205","v206","v207","v208","v209","v210","v211","v212","v213","v214","v215","v216","v217","v218","v219","v220","v221","v222","v223","v224","v225","v226","v227","v228","v229","v230","v231","v232","v233","v234","v235","v236","v237","v238","v239","v240","v241","v242","v243","v244","v245","v246","v247","v248","v249","v250","v251","v252","v253","v254","v255","a0","a1","a2","a3","a4","a5","a6","a7","a8","a9","a10","a11","a12","a13","a14","a15","a16","a17","a18","a19","a20","a21","a22","a23","a24","a25","a26","a27","a28","a29","a30","a31","a32","a33","a34","a35","a36","a37","a38","a39","a40","a41","a42","a43","a44","a45","a46","a47","a48","a49","a50","a51","a52","a53","a54","a55","a56","a57","a58","a59","a60","a61","a62","a63","a64","a65","a66","a67","a68","a69","a70","a71","a72","a73","a74","a75","a76","a77","a78","a79","a80","a81","a82","a83","a84","a85","a86","a87","a88","a89","a90","a91","a92","a93","a94","a95","a96","a97","a98","a99","a100","a101","a102","a103","a104","a105","a106","a107","a108","a109","a110","a111","a112","a113","a114","a115","a116","a117","a118","a119","a120","a121","a122","a123","a124","a125","a126","a127","a128","a129","a130","a131","a132","a133","a134","a135","a136","a137","a138","a139","a140","a141","a142","a143","a144","a145","a146","a147","a148","a149","a150","a151","a152","a153","a154","a155","a156","a157","a158","a159","a160","a161","a162","a163","a164","a165","a166","a167","a168","a169","a170","a171","a172","a173","a174","a175","a176","a177","a178","a179","a180","a181","a182","a183","a184","a185","a186","a187","a188","a189","a190","a191","a192","a193","a194","a195","a196","a197","a198","a199","a200","a201","a202","a203","a204","a205","a206","a207","a208","a209","a210","a211","a212","a213","a214","a215","a216","a217","a218","a219","a220","a221","a222","a223","a224","a225","a226","a227","a228","a229","a230","a231","a232","a233","a234","a235","a236","a237","a238","a239","a240","a241","a242","a243","a244","a245","a246","a247","a248","a249","a250","a251","a252","a253","a254","a255","s8","s9","s10","s11","s12","s13","s14","s15","s16","s17","s18","s19","s20","s21","s22","s23","s24","s25","s26","s27","s28","s29","s30","s31","s36","s37","s38","s39","s40","s41","s42","s43","s44","s45","s46","s47","s48","s49","s50","s51","s52","s53","s54","s55","s56","s57","s58","s59","s60","s61","s62","s63","s64","s65","s66","s67","s68","s69","s70","s71","s72","s73","s74","s75","s76","s77","s78","s79","s80","s81","s82","s83","s84","s85","s86","s87","s88","s89","s90","s91","s92","s93","s94","s95","s96","s97","s98","s99","vcc","scc","memory");
  if (bad) {
    asm volatile("s_waitcnt vmcnt(0) lgkmcnt(0)" ::: "memory");
    const u64* p = (const u64*)__builtin_amdgcn_kernarg_segment_ptr();
    exact_rows((const u16*)p[0], (const u16*)p[1], (const u16*)p[2], (const u16*)p[3], (const u16*)p[4], (const int*)p[5],
               (const u16*)p[6], (u16*)p[7], (b & 7) * 4 + (b >> 6), (b >> 3) & 7, wave);
  }
}

extern "C" void kernel_launch(void* const* d_in, const int* in_sizes, int n_in, void* d_out, int out_size, void* d_ws, size_t ws_size,
                              hipStream_t stream) {
  (void)in_sizes; (void)n_in; (void)out_size; (void)ws_size;
  (void)hipFuncSetAttribute((const void*)memattn_kernel, hipFuncAttributeMaxDynamicSharedMemorySize, 163840);
  memattn_kernel<<<256, 256, 163840, stream>>>((const u16*)d_in[0], (const u16*)d_in[1], (const u16*)d_in[2], (const u16*)d_in[3],
                                               (const u16*)d_in[4], (const int*)d_in[5], (const u16*)d_in[6], (u16*)d_out, (char*)d_ws);
}
```
